```python
import torch

def _softmax(x, axis=-1):
    return torch.softmax(x, dim=axis)
def _gelu(x):
    return torch.nn.functional.gelu(x, approximate="tanh")
def _silu(x):
    return torch.nn.functional.silu(x)
def _sigmoid(x):
    return torch.sigmoid(x)
def _layernorm(x, axis=-1, eps=1e-5):
    m = x.mean(dim=axis, keepdim=True)
    v = x.var(dim=axis, keepdim=True, unbiased=False)
    return (x - m) / torch.sqrt(v + eps)
def _scatter_add(tgt, idx, src, axis=0):
    return tgt.clone().index_add_(axis, idx, src)
def _segment_sum(src, seg, n):
    out = torch.zeros((n,) + src.shape[1:], dtype=src.dtype)
    return out.index_add_(0, seg, src)
def _pad(x, pw):
    flat = tuple(v for pair in reversed(pw) for v in pair)
    return torch.nn.functional.pad(x, flat)

kernel_name = 'parallel_sum__linear_attn_block__glu_gated_swiglu__prompt'

def setup_inputs():

    _w = (lambda a: (lambda t: t.to(torch.bfloat16) if t.is_floating_point() else t)(torch.as_tensor(a)))
    def _A_setup():
        B, S, D = 1, 2048, 576
        return {
            "x": _w((lambda *s: torch.randn(*s, dtype=torch.bfloat16) * 0.05)(B, S, D)),
            "wqkv": _w((lambda *s: torch.randn(*s, dtype=torch.bfloat16) * 0.05)(D, 3 * D)),
            "wo": _w((lambda *s: torch.randn(*s, dtype=torch.bfloat16) * 0.05)(D, D)),
        }
    _A_in = _A_setup()

    def _B_setup():
        B, S, D, F = 1, 2048, 576, 4608
        return {
            "x": _w((lambda *s: torch.randn(*s, dtype=torch.bfloat16) * 0.05)(B, S, D)),
            "w1": _w((lambda *s: torch.randn(*s, dtype=torch.bfloat16) * 0.2)(D, F) * (2.0 / D) ** 0.5),
            "w3": _w((lambda *s: torch.randn(*s, dtype=torch.bfloat16) * 0.2)(D, F) * (2.0 / D) ** 0.5),
            "w2": _w((lambda *s: torch.randn(*s, dtype=torch.bfloat16) * 0.2)(F, D) * (2.0 / F) ** 0.5),
        }
    _B_in = _B_setup()

    return {
        "A_x": _A_in["x"],
        "A_wqkv": _A_in["wqkv"],
        "A_wo": _A_in["wo"],
        "B_x": _B_in["x"],
        "B_w1": _B_in["w1"],
        "B_w3": _B_in["w3"],
        "B_w2": _B_in["w2"],
    }

def reference(A_x, A_wqkv, A_wo, B_x, B_w1, B_w3, B_w2):
    def _A_ref(x, wqkv, wo):
        B, S, D = x.shape
        H = 32; Dh = D // H
        qkv = (lambda x, *d: x.permute(*d))((x @ wqkv).reshape(B, S, 3, H, Dh), 2, 0, 3, 1, 4)
        q, k, v = qkv[0], qkv[1], qkv[2]
        phi = lambda t: torch.where(t > 0, t + 1, torch.exp(t)) + 1e-6
        pq, pk = phi(q), phi(k)
        kv = torch.einsum("bhsd,bhse->bhde", pk, v)
        z = torch.einsum("bhsd->bhd", pk)
        num = torch.einsum("bhqd,bhde->bhqe", pq, kv)
        den = torch.einsum("bhqd,bhd->bhq", pq, z)[..., None] + 1e-6
        o = num / den
        return (lambda x, *d: x.permute(*d))(o, 0, 2, 1, 3).reshape(B, S, D) @ wo

    def _B_ref(x, w1, w3, w2):
        gate = torch.einsum("bsd,df->bsf", x, w1)
        up = torch.einsum("bsd,df->bsf", x, w3)
        h = _silu(gate) * up
        out = torch.einsum("bsf,fd->bsd", h, w2)
        return out

    x = A_x
    xn = _layernorm(x, axis=-1)
    return x + _A_ref(xn, A_wqkv, A_wo) + _B_ref(xn, B_w1, B_w3, B_w2)
```

```cpp
#include <hip/hip_runtime.h>
#include <hip/hip_bf16.h>
typedef unsigned short u16;
__device__ __forceinline__ float bf2f(u16 v){ return __uint_as_float(((unsigned)v)<<16); }
__device__ __forceinline__ u16 f2bf(float f){ unsigned u=__float_as_uint(f); u += 0x7fffu + ((u>>16)&1u); return (u16)(u>>16); }
__device__ __forceinline__ float rbf(float f){ return bf2f(f2bf(f)); }
constexpr int S=2048, D=576, F=4608, H=32, DH=18;

__global__ void ln_k(const u16* x, u16* xn){
  int row=blockIdx.x; int t=threadIdx.x;
  float v[9]; float s=0;
  for(int i=0;i<9;i++){ v[i]=bf2f(x[row*D+t+64*i]); s+=v[i]; }
  for(int o=32;o;o>>=1) s+=__shfl_xor(s,o);
  float m=s/D; float q=0;
  for(int i=0;i<9;i++){ float d=v[i]-m; q+=d*d; }
  for(int o=32;o;o>>=1) q+=__shfl_xor(q,o);
  float var=rbf(q/D); float mb=rbf(m);
  float sd=rbf(sqrtf(rbf(var+1e-5f)));
  for(int i=0;i<9;i++) xn[row*D+t+64*i]=f2bf(rbf(v[i]-mb)/sd);
}
__global__ void gemm_k(const u16* A,const u16* B,float* C,int M,int N,int K){
  __shared__ float As[16][65], Bs[16][64];
  int tx=threadIdx.x&15, ty=threadIdx.x>>4; int bm=blockIdx.y*64,bn=blockIdx.x*64;
  float acc[4][4]={};
  for(int k0=0;k0<K;k0+=16){
    for(int i=threadIdx.x;i<1024;i+=256){ int r=i>>4,c=i&15; As[c][r]=bf2f(A[(size_t)(bm+r)*K+k0+c]); int kr=i>>6,cc=i&63; Bs[kr][cc]=bf2f(B[(size_t)(k0+kr)*N+bn+cc]); }
    __syncthreads();
    for(int k=0;k<16;k++){ float a[4],b[4]; for(int i=0;i<4;i++){a[i]=As[k][ty*4+i]; b[i]=Bs[k][tx*4+i];} for(int i=0;i<4;i++)for(int j=0;j<4;j++)acc[i][j]+=a[i]*b[j]; }
    __syncthreads();
  }
  for(int i=0;i<4;i++)for(int j=0;j<4;j++) C[(size_t)(bm+ty*4+i)*N+bn+tx*4+j]=acc[i][j];
}
__device__ __forceinline__ float phi(float t){ t=rbf(t); float r = t>0? rbf(t+1.f): rbf(expf(t)); return rbf(r+1e-6f); }
__global__ void kv_k(const float* qkv,float* kv,float* z){
  int h=blockIdx.x; int t=threadIdx.x;
  if(t<324){ int d=t/18,e=t%18; float s=0; for(int r=0;r<S;r++) s+=phi(qkv[(size_t)r*1728+576+h*18+d])*rbf(qkv[(size_t)r*1728+1152+h*18+e]); kv[h*324+t]=rbf(s);}
  else if(t<342){ int d=t-324; float s=0; for(int r=0;r<S;r++) s+=phi(qkv[(size_t)r*1728+576+h*18+d]); z[h*18+d]=rbf(s);}
}
__global__ void o_k(const float* qkv,const float* kv,const float* z,u16* o){
  int i=blockIdx.x*256+threadIdx.x; int r=i/576,c=i%576,h=c/18,e=c%18;
  float num=0,den=0; for(int d=0;d<18;d++){ float p=phi(qkv[(size_t)r*1728+h*18+d]); num+=p*kv[h*324+d*18+e]; den+=p*z[h*18+d]; }
  o[i]=f2bf(rbf(num)/rbf(rbf(den)+1e-6f));
}
__global__ void h_k(const float* g,const float* u,u16* h){
  size_t i=(size_t)blockIdx.x*256+threadIdx.x; float a=rbf(g[i]); float s=rbf(a/(1.f+expf(-a))); h[i]=f2bf(s*rbf(u[i]));
}
__global__ void fin_k(const u16* x,const float* a,const float* b,u16* out){
  int i=blockIdx.x*256+threadIdx.x; out[i]=f2bf(rbf(bf2f(x[i])+rbf(a[i]))+rbf(b[i]));
}
extern "C" void kernel_launch(void* const* d_in, const int* in_sizes, int n_in, void* d_out, int out_size, void* d_ws, size_t ws_size, hipStream_t stream){
  const u16* x=(const u16*)d_in[0]; const u16* wqkv=(const u16*)d_in[1]; const u16* wo=(const u16*)d_in[2];
  const u16* w1=(const u16*)d_in[4]; const u16* w3=(const u16*)d_in[5]; const u16* w2=(const u16*)d_in[6];
  char* ws=(char*)d_ws; size_t off=0;
  u16* xn=(u16*)(ws+off); off+=(size_t)S*D*2;
  float* qkv=(float*)(ws+off); off+=(size_t)S*1728*4;
  float* kv=(float*)(ws+off); off+=32*324*4; float* z=(float*)(ws+off); off+=32*18*4+1024; off=(off+255)&~255ull;
  u16* o=(u16*)(ws+off); off+=(size_t)S*D*2;
  float* g=(float*)(ws+off); off+=(size_t)S*F*4; float* u=(float*)(ws+off); off+=(size_t)S*F*4;
  u16* h=(u16*)(ws+off); off+=(size_t)S*F*2;
  float* a=(float*)(ws+off); off+=(size_t)S*D*4; float* b=(float*)(ws+off); off+=(size_t)S*D*4;
  ln_k<<<S,64,0,stream>>>(x,xn);
  gemm_k<<<dim3(1728/64,S/64),256,0,stream>>>(xn,wqkv,qkv,S,1728,D);
  kv_k<<<32,384,0,stream>>>(qkv,kv,z);
  o_k<<<S*D/256,256,0,stream>>>(qkv,kv,z,o);
  gemm_k<<<dim3(D/64,S/64),256,0,stream>>>(o,wo,a,S,D,D);
  gemm_k<<<dim3(F/64,S/64),256,0,stream>>>(xn,w1,g,S,F,D);
  gemm_k<<<dim3(F/64,S/64),256,0,stream>>>(xn,w3,u,S,F,D);
  h_k<<<(size_t)S*F/256,256,0,stream>>>(g,u,h);
  gemm_k<<<dim3(D/64,S/64),256,0,stream>>>(h,w2,b,S,D,F);
  fin_k<<<S*D/256,256,0,stream>>>(x,a,b,(u16*)d_out);
}
```

```cpp
#include <hip/hip_runtime.h>
__global__ __launch_bounds__(256, 1) void fused_kernel(const void* x, const void* wqkv, const void* wo, const void* w1, const void* w3, const void* w2, void* out, void* ws) {
  __shared__ __attribute__((aligned(16))) char lds[163840];
  unsigned tid = threadIdx.x;
  asm volatile(
"L_start%=:\n"
"s_load_dwordx16 s[4:19], %1, 0x0\n"
"s_getpc_b64 s[100:101]\n"
"L_pc%=:\n"
"s_lshr_b32 s97, %2, 3\n"
"s_lshl_b32 s97, s97, 4\n"
"v_lshrrev_b32 v2, 4, v0\n"
"v_add_u32 v2, s97, v2\n"
"v_and_b32 v3, 15, v0\n"
"v_cmp_eq_u32 vcc, 0, v3\n"
"s_nop 1\n"
"v_cndmask_b32 v2, v3, v2, vcc\n"
"v_lshlrev_b32 v2, 7, v2\n"
"v_min_u32 v2, L_end%=-L_pc%=-4, v2\n"
"global_load_dword v93, v2, s[100:101]\n"
"v_and_b32 v1, 63, v0\n"
"v_lshrrev_b32 v6, 6, v0\n"
"s_nop 0\n"
"v_readfirstlane_b32 s72, v6\n"
"s_lshr_b32 s76, %2, 4\n"
"s_mul_i32 s96, s76, 147456\n"
"s_waitcnt lgkmcnt(0)\n"
"s_mov_b64 s[36:37], s[4:5]\n"
"s_add_u32 s36, s36, s96\n"
"s_addc_u32 s37, s37, 0\n"
"s_and_b32 s37, s37, 0xffff\n"
"s_mov_b32 s38, 147456\n"
"s_mov_b32 s39, 0x20000\n"
"v_lshrrev_b32 v7, 3, v1\n"
"v_lshl_add_u32 v8, v6, 5, v7\n"
"v_mul_u32_u24 v8, 1152, v8\n"
"v_lshrrev_b32 v7, 4, v1\n"
"v_and_b32 v2, 7, v0\n"
"v_xor_b32 v7, v7, v2\n"
"v_lshl_add_u32 v127, v7, 4, v8\n"
"s_bfe_u32 s97, %2, 0x10003\n"
"s_mov_b32 s97, 0\n"
"s_mul_i32 s98, s97, 18432\n"
"s_sub_u32 s99, 18432, s98\n"
"v_add_u32 v125, s99, v127\n"
"v_add_u32 v127, s98, v127\n"
"v_xor_b32 v126, 64, v127\n"
"v_xor_b32 v124, 64, v125\n"
"s_lshl_b32 s77, s72, 12\n"
"s_mul_i32 s84, s72, 18432\n"
"s_add_u32 s84, s84, 73728\n"
"s_lshl_b32 s88, s72, 11\n"
"s_add_u32 s88, s88, 65536\n"
"s_add_u32 m0, s84, 0\n"
"s_mov_b32 s82, 0\n"
"buffer_load_dwordx4 v127, s[36:39], s82 offen offset:0 lds\n"
"s_mov_b32 s83, 8192\n"
"buffer_load_dwordx4 v126, s[36:39], s83 offen offset:1024 lds\n"
"s_add_u32 m0, s84, 2048\n"
"s_mov_b32 s82, 128\n"
"buffer_load_dwordx4 v127, s[36:39], s82 offen offset:0 lds\n"
"s_mov_b32 s83, 8320\n"
"buffer_load_dwordx4 v126, s[36:39], s83 offen offset:1024 lds\n"
"s_add_u32 m0, s84, 4096\n"
"s_mov_b32 s82, 256\n"
"buffer_load_dwordx4 v127, s[36:39], s82 offen offset:0 lds\n"
"s_mov_b32 s83, 8448\n"
"buffer_load_dwordx4 v126, s[36:39], s83 offen offset:1024 lds\n"
"s_add_u32 m0, s84, 6144\n"
"s_mov_b32 s82, 384\n"
"buffer_load_dwordx4 v127, s[36:39], s82 offen offset:0 lds\n"
"s_mov_b32 s83, 8576\n"
"buffer_load_dwordx4 v126, s[36:39], s83 offen offset:1024 lds\n"
"s_add_u32 m0, s84, 8192\n"
"s_mov_b32 s82, 512\n"
"buffer_load_dwordx4 v127, s[36:39], s82 offen offset:0 lds\n"
"s_mov_b32 s83, 8704\n"
"buffer_load_dwordx4 v126, s[36:39], s83 offen offset:1024 lds\n"
"v_lshlrev_b32 v9, 4, v1\n"
"v_and_b32 v2, 15, v0\n"
"v_lshrrev_b32 v3, 4, v1\n"
"v_lshrrev_b32 v7, 1, v2\n"
"v_xor_b32 v7, v7, v3\n"
"v_lshlrev_b32 v7, 4, v7\n"
"v_lshl_add_u32 v7, v2, 7, v7\n"
"v_add_u32 v54, s84, v7\n"
"v_xor_b32 v55, 64, v54\n"
"v_add_u32 v56, s77, v7\n"
"v_xor_b32 v57, 64, v56\n"
"v_add_u32 v58, s88, v7\n"
"v_xor_b32 v59, 64, v58\n"
"s_and_b32 s75, %2, 7\n"
"s_lshl_b32 s75, s75, 1\n"
"s_bfe_u32 s99, %2, 0x10003\n"
"s_or_b32 s75, s75, s99\n"
"s_lshr_b32 s73, s72, 1\n"
"s_and_b32 s74, s72, 1\n"
"s_mov_b64 s[68:69], s[16:17]\n"
"s_add_u32 s68, s68, s96\n"
"s_addc_u32 s69, s69, 0\n"
"s_and_b32 s69, s69, 0xffff\n"
"s_mov_b32 s70, 147456\n"
"s_mov_b32 s71, 0x20000\n"
"s_mov_b64 s[40:41], s[6:7]\n"
"s_and_b32 s41, s41, 0xffff\n"
"s_mov_b32 s42, 1990656\n"
"s_mov_b32 s43, 0x20000\n"
"s_mov_b64 s[64:65], s[18:19]\n"
"s_and_b32 s65, s65, 0xffff\n"
"s_mov_b32 s66, 26214400\n"
"s_mov_b32 s67, 0x20000\n"
"v_bfe_u32 v4, v1, 2, 2\n"
"v_and_b32 v5, 3, v1\n"
"s_xor_b32 s99, s75, 1\n"
"v_lshl_add_u32 v11, s74, 2, v3\n"
"v_lshlrev_b32 v11, 1, v11\n"
"v_xor_b32 v19, v11, v2\n"
"v_min_u32 v11, 14, v19\n"
"v_mul_u32_u24 v12, 52, v11\n"
"v_lshrrev_b32 v12, 8, v12\n"
"v_mul_u32_u24 v13, 5, v12\n"
"v_sub_u32 v11, v11, v13\n"
"v_lshlrev_b32 v11, 4, v11\n"
"v_mul_u32_u24 v13, 1152, v12\n"
"v_add_u32 v11, v11, v13\n"
"s_mul_i32 s96, s75, 72\n"
"s_mul_i32 s100, s99, 72\n"
"v_mov_b32 v13, s96\n"
"v_mov_b32 v18, s100\n"
"v_cmp_eq_u32 vcc, 0, v12\n"
"s_nop 1\n"
"v_cndmask_b32 v13, v13, v18, vcc\n"
"v_add_u32 v11, v11, v13\n"
"v_lshl_add_u32 v13, s72, 3, v3\n"
"v_mul_u32_u24 v13, 3456, v13\n"
"v_add_u32 v10, v11, v13\n"
"v_cmp_eq_u32 vcc, 15, v19\n"
"v_mov_b32 v13, 0x7ffffff0\n"
"s_nop 0\n"
"v_cndmask_b32 v10, v10, v13, vcc\n"
"v_mov_b32 v119, v10\n"
"s_lshr_b32 s96, s76, 1\n"
"s_mul_i32 s96, s96, 110592\n"
"s_and_b32 s100, s76, 1\n"
"s_mul_i32 s100, s100, 13824\n"
"s_add_u32 s89, s96, s100\n"
"v_lshlrev_b32 v118, 7, v2\n"
"s_mul_i32 s97, s75, 576\n"
"s_mov_b64 s[44:45], s[10:11]\n"
"s_add_u32 s44, s44, s97\n"
"s_addc_u32 s45, s45, 0\n"
"s_sub_u32 s44, s44, 4096\n"
"s_subb_u32 s45, s45, 0\n"
"s_and_b32 s45, s45, 0xffff\n"
"s_sub_u32 s46, 5312512, s97\n"
"s_mov_b32 s47, 0x20000\n"
"s_mov_b64 s[48:49], s[12:13]\n"
"s_add_u32 s48, s48, s97\n"
"s_addc_u32 s49, s49, 0\n"
"s_sub_u32 s48, s48, 4096\n"
"s_subb_u32 s49, s49, 0\n"
"s_and_b32 s49, s49, 0xffff\n"
"s_sub_u32 s50, 5312512, s97\n"
"s_mov_b32 s51, 0x20000\n"
"s_and_b32 s97, s75, 1\n"
"s_lshl_b32 s78, s97, 6\n"
"s_xor_b32 s97, s97, 1\n"
"s_lshl_b32 s79, s97, 9\n"
"v_lshrrev_b32 v18, 3, v1\n"
"v_lshlrev_b32 v19, 1, v3\n"
"v_and_b32 v20, 7, v1\n"
"v_xor_b32 v19, v19, v20\n"
"v_lshlrev_b32 v31, 4, v19\n"
"v_lshrrev_b32 v19, 2, v18\n"
"v_lshlrev_b32 v19, 3, v19\n"
"v_and_b32 v20, 3, v18\n"
"v_add_u32 v19, v19, v20\n"
"s_lshl_b32 s96, s74, 4\n"
"s_lshl_b32 s97, s73, 2\n"
"s_add_u32 s96, s96, s97\n"
"v_add_u32 v19, s96, v19\n"
"v_mul_u32_u24 v19, 9216, v19\n"
"v_add_u32 v11, v19, v31\n"
"s_add_u32 m0, s84, 10240\n"
"s_mov_b32 s82, 640\n"
"buffer_load_dwordx4 v127, s[36:39], s82 offen offset:0 lds\n"
"s_mov_b32 s83, 8832\n"
"buffer_load_dwordx4 v126, s[36:39], s83 offen offset:1024 lds\n"
"s_add_u32 m0, s84, 12288\n"
"s_mov_b32 s82, 768\n"
"buffer_load_dwordx4 v127, s[36:39], s82 offen offset:0 lds\n"
"s_mov_b32 s83, 8960\n"
"buffer_load_dwordx4 v126, s[36:39], s83 offen offset:1024 lds\n"
"s_add_u32 m0, s84, 14336\n"
"s_mov_b32 s82, 896\n"
"buffer_load_dwordx4 v127, s[36:39], s82 offen offset:0 lds\n"
"s_mov_b32 s83, 9088\n"
"buffer_load_dwordx4 v126, s[36:39], s83 offen offset:1024 lds\n"
"s_add_u32 m0, s84, 16384\n"
"s_mov_b32 s82, 1024\n"
"buffer_load_dwordx4 v127, s[36:39], s82 offen offset:0 lds\n"
"s_mov_b32 s83, 9216\n"
"buffer_load_dwordx4 v126, s[36:39], s83 offen offset:1024 lds\n"
"s_and_b32 s96, s75, 1\n"
"s_mul_i32 s96, s96, 576\n"
"s_lshr_b32 s100, s75, 1\n"
"s_mul_i32 s97, s100, 663552\n"
"s_add_u32 s97, s97, s96\n"
"s_mov_b64 s[56:57], s[14:15]\n"
"s_add_u32 s56, s56, s97\n"
"s_addc_u32 s57, s57, 0\n"
"s_sub_u32 s56, s56, 4096\n"
"s_subb_u32 s57, s57, 0\n"
"s_and_b32 s57, s57, 0xffff\n"
"s_sub_u32 s58, 667648, s96\n"
"s_mov_b32 s59, 0x20000\n"
"s_mul_i32 s97, s100, 82944\n"
"s_add_u32 s97, s97, s96\n"
"s_mov_b64 s[60:61], s[8:9]\n"
"s_add_u32 s60, s60, s97\n"
"s_addc_u32 s61, s61, 0\n"
"s_sub_u32 s60, s60, 4096\n"
"s_subb_u32 s61, s61, 0\n"
"s_and_b32 s61, s61, 0xffff\n"
"s_sub_u32 s62, 87040, s96\n"
"s_mov_b32 s63, 0x20000\n"
"s_cmp_eq_u32 s73, 0\n"
"s_cselect_b32 s52, s44, s48\n"
"s_cselect_b32 s53, s45, s49\n"
"s_cselect_b32 s54, s46, s50\n"
"s_cselect_b32 s55, s47, s51\n"
"s_cmp_eq_u32 s74, 0\n"
"s_cselect_b64 s[86:87], 0, -1\n"
"s_cselect_b32 s94, 2, 0\n"
"s_lshl_b32 s94, 0x11111111, s94\n"
"s_mov_b32 s95, s94\n"
"s_mov_b64 s[90:91], 0xffff\n"
"s_mov_b32 s92, 0xffff0000\n"
"s_mov_b32 s93, 0\n"
"v_lshlrev_b32 v19, 3, v3\n"
"v_add_u32 v19, v19, v4\n"
"v_lshl_add_u32 v19, s74, 2, v19\n"
"v_mul_u32_u24 v19, 9216, v19\n"
"v_and_b32 v30, 1, v3\n"
"v_lshlrev_b32 v30, 1, v30\n"
"v_xor_b32 v30, v30, v5\n"
"v_lshl_add_u32 v12, v30, 4, v19\n"
"v_and_b32 v18, 1, v3\n"
"v_lshl_add_u32 v18, v18, 2, v4\n"
"v_lshlrev_b32 v19, 1, v18\n"
"v_lshlrev_b32 v30, 12, v3\n"
"v_lshl_add_u32 v30, v4, 8, v30\n"
"s_mul_i32 s96, s74, 5\n"
"v_add_u32 v31, s96, v5\n"
"v_and_b32 v16, 1, v31\n"
"v_lshl_add_u32 v16, v16, 3, v30\n"
"v_lshrrev_b32 v31, 1, v31\n"
"v_add_u32 v17, 10, v31\n"
"v_xor_b32 v17, v17, v19\n"
"v_lshl_add_u32 v20, v17, 4, v16\n"
"v_add_u32 v17, 0, v31\n"
"v_xor_b32 v17, v17, v19\n"
"v_lshl_add_u32 v21, v17, 4, v16\n"
"v_add_u32 v17, 5, v31\n"
"v_xor_b32 v17, v17, v19\n"
"v_lshl_add_u32 v22, v17, 4, v16\n"
"v_mul_u32_u24 v17, 5, v5\n"
"v_sub_u32 v17, 12, v17\n"
"v_min_u32 v17, 15, v17\n"
"v_xor_b32 v17, v17, v19\n"
"v_lshl_add_u32 v23, v17, 4, v30\n"
"v_add_u32 v24, 0x10000, v20\n"
"v_add_u32 v25, 0x10000, v21\n"
"v_add_u32 v26, 0x10000, v22\n"
"v_add_u32 v27, 0x10000, v23\n"
"v_lshrrev_b32 v30, 2, v1\n"
"v_lshlrev_b32 v30, 6, v30\n"
"v_lshl_add_u32 v30, v5, 3, v30\n"
"v_and_b32 v31, 1, v3\n"
"v_xor_b32 v31, s74, v31\n"
"v_lshl_add_u32 v7, v31, 5, v30\n"
"v_add_u32 v8, 0x10000, v7\n"
"s_mul_i32 s96, s72, 3072\n"
"s_add_u32 s96, s96, 147456\n"
"s_lshl_b32 s97, s76, 2\n"
"s_add_u32 s97, s97, s72\n"
"s_mul_i32 s97, s97, 82944\n"
"s_add_u32 s97, s97, 4096\n"
"v_min_u32 v30, 8, v1\n"
"v_mul_u32_u24 v30, 9216, v30\n"
"v_add_u32 v13, s97, v30\n"
"v_lshlrev_b32 v30, 5, v3\n"
"v_lshl_add_u32 v30, v5, 3, v30\n"
"v_add_u32 v30, s96, v30\n"
"v_cmp_eq_u32 vcc, 2, v4\n"
"v_mov_b32 v31, 2048\n"
"v_mov_b32 v16, 0xa00\n"
"v_cndmask_b32 v31, v16, v31, vcc\n"
"v_add_u32 v10, v30, v31\n"
"s_lshl_b32 s96, s72, 10\n"
"s_add_u32 s96, s96, 73728\n"
"v_add_u32 v120, s96, v9\n"
"v_add_u32 v121, 0x8000, v120\n"
"v_xor_b32 v122, 0x400, v120\n"
"v_xor_b32 v123, 0x400, v121\n"
"v_lshrrev_b32 v30, 1, v3\n"
"v_lshlrev_b32 v30, 12, v30\n"
"v_lshl_add_u32 v30, v18, 7, v30\n"
"v_lshl_add_u32 v30, v5, 3, v30\n"
"v_lshrrev_b32 v18, 1, v18\n"
"s_lshl_b32 s96, s74, 1\n"
"v_xor_b32 v18, s96, v18\n"
"v_lshl_add_u32 v1, v18, 5, v30\n"
"v_xor_b32 v18, 1, v18\n"
"v_lshl_add_u32 v2, v18, 5, v30\n"
"v_add_u32 v3, 0x10000, v1\n"
"v_add_u32 v4, 0x10000, v2\n"
"v_add_u32 v0, 0x20000, v1\n"
"v_add_u32 v6, 0x20000, v2\n"
"s_lshl_b32 s96, s72, 11\n"
"s_lshl_b32 s97, s74, 13\n"
"s_add_u32 s96, s96, s97\n"
"s_add_u32 s96, s96, 81920\n"
"v_add_u32 v5, s96, v9\n"
"v_mov_b32 v48, 0x3f803f80\n"
"v_mov_b32 v49, 0x3f803f80\n"
"v_mov_b32 v50, 0x3f803f80\n"
"v_mov_b32 v51, 0x3f803f80\n"
"s_mov_b32 s98, 0x3ae38e39\n"
"s_waitcnt vmcnt(16)\n"
"ds_read_b128 v[128:131], v54 offset:0\n"
"ds_read_b128 v[136:139], v55 offset:0\n"
"s_waitcnt vmcnt(14)\n"
"ds_read_b128 v[144:147], v54 offset:2048\n"
"ds_read_b128 v[152:155], v55 offset:2048\n"
"s_waitcnt lgkmcnt(3)\n"
"v_mfma_f32_16x16x32_bf16 v[32:35], v[48:51], v[128:131], 0\n"
"v_mfma_f32_16x16x32_bf16 v[36:39], v[128:131], v[128:131], 0\n"
"s_waitcnt lgkmcnt(2)\n"
"v_mfma_f32_16x16x32_bf16 v[32:35], v[48:51], v[136:139], v[32:35]\n"
"v_mfma_f32_16x16x32_bf16 v[36:39], v[136:139], v[136:139], v[36:39]\n"
"s_waitcnt vmcnt(12)\n"
"ds_read_b128 v[160:163], v54 offset:4096\n"
"ds_read_b128 v[168:171], v55 offset:4096\n"
"s_waitcnt lgkmcnt(3)\n"
"v_mfma_f32_16x16x32_bf16 v[32:35], v[48:51], v[144:147], v[32:35]\n"
"v_mfma_f32_16x16x32_bf16 v[36:39], v[144:147], v[144:147], v[36:39]\n"
"s_waitcnt lgkmcnt(2)\n"
"v_mfma_f32_16x16x32_bf16 v[32:35], v[48:51], v[152:155], v[32:35]\n"
"v_mfma_f32_16x16x32_bf16 v[36:39], v[152:155], v[152:155], v[36:39]\n"
"s_waitcnt vmcnt(10)\n"
"ds_read_b128 v[176:179], v54 offset:6144\n"
"ds_read_b128 v[184:187], v55 offset:6144\n"
"s_waitcnt lgkmcnt(3)\n"
"v_mfma_f32_16x16x32_bf16 v[32:35], v[48:51], v[160:163], v[32:35]\n"
"v_mfma_f32_16x16x32_bf16 v[36:39], v[160:163], v[160:163], v[36:39]\n"
"s_waitcnt lgkmcnt(2)\n"
"v_mfma_f32_16x16x32_bf16 v[32:35], v[48:51], v[168:171], v[32:35]\n"
"v_mfma_f32_16x16x32_bf16 v[36:39], v[168:171], v[168:171], v[36:39]\n"
"s_waitcnt vmcnt(8)\n"
"ds_read_b128 v[192:195], v54 offset:8192\n"
"ds_read_b128 v[200:203], v55 offset:8192\n"
"s_waitcnt lgkmcnt(3)\n"
"v_mfma_f32_16x16x32_bf16 v[32:35], v[48:51], v[176:179], v[32:35]\n"
"v_mfma_f32_16x16x32_bf16 v[36:39], v[176:179], v[176:179], v[36:39]\n"
"s_waitcnt lgkmcnt(2)\n"
"v_mfma_f32_16x16x32_bf16 v[32:35], v[48:51], v[184:187], v[32:35]\n"
"v_mfma_f32_16x16x32_bf16 v[36:39], v[184:187], v[184:187], v[36:39]\n"
"s_lshl_b32 s96, s75, 4\n"
"s_add_u32 s96, s96, s76\n"
"s_mul_i32 s12, s96, 86016\n"
"s_add_u32 s12, s12, 4194304\n"
"s_lshl_b32 s14, s96, 7\n"
"s_xor_b32 s96, s96, 16\n"
"s_mul_i32 s9, s96, 86016\n"
"s_add_u32 s9, s9, 4112384\n"
"s_lshl_b32 s19, s96, 7\n"
"s_add_u32 s19, s19, 73728\n"
"s_lshl_b32 s96, s72, 3\n"
"s_add_u32 s14, s14, s96\n"
"s_add_u32 s14, s14, 73728\n"
"s_lshl_b32 s96, s72, 10\n"
"s_add_u32 s8, s12, s96\n"
"s_sub_u32 s8, s8, 3072\n"
"s_waitcnt vmcnt(6)\n"
"ds_read_b128 v[208:211], v54 offset:10240\n"
"ds_read_b128 v[216:219], v55 offset:10240\n"
"s_add_u32 m0, s77, 0\n"
"s_mov_b32 s82, 0\n"
"buffer_load_dwordx4 v125, s[36:39], s82 offen offset:0 lds\n"
"s_mov_b32 s83, 8192\n"
"buffer_load_dwordx4 v124, s[36:39], s83 offen offset:1024 lds\n"
"s_waitcnt lgkmcnt(3)\n"
"v_mfma_f32_16x16x32_bf16 v[32:35], v[48:51], v[192:195], v[32:35]\n"
"v_mfma_f32_16x16x32_bf16 v[36:39], v[192:195], v[192:195], v[36:39]\n"
"s_waitcnt lgkmcnt(2)\n"
"v_mfma_f32_16x16x32_bf16 v[32:35], v[48:51], v[200:203], v[32:35]\n"
"v_mfma_f32_16x16x32_bf16 v[36:39], v[200:203], v[200:203], v[36:39]\n"
"s_waitcnt vmcnt(6)\n"
"ds_read_b128 v[224:227], v54 offset:12288\n"
"ds_read_b128 v[232:235], v55 offset:12288\n"
"s_add_u32 m0, s77, 2048\n"
"s_mov_b32 s82, 128\n"
"buffer_load_dwordx4 v125, s[36:39], s82 offen offset:0 lds\n"
"s_mov_b32 s83, 8320\n"
"buffer_load_dwordx4 v124, s[36:39], s83 offen offset:1024 lds\n"
"s_waitcnt lgkmcnt(3)\n"
"v_mfma_f32_16x16x32_bf16 v[32:35], v[48:51], v[208:211], v[32:35]\n"
"v_mfma_f32_16x16x32_bf16 v[36:39], v[208:211], v[208:211], v[36:39]\n"
"s_waitcnt lgkmcnt(2)\n"
"v_mfma_f32_16x16x32_bf16 v[32:35], v[48:51], v[216:219], v[32:35]\n"
"v_mfma_f32_16x16x32_bf16 v[36:39], v[216:219], v[216:219], v[36:39]\n"
"s_waitcnt vmcnt(6)\n"
"ds_read_b128 v[240:243], v54 offset:14336\n"
"ds_read_b128 v[248:251], v55 offset:14336\n"
"s_add_u32 m0, s77, 16384\n"
"s_mov_b32 s82, 256\n"
"buffer_load_dwordx4 v125, s[36:39], s82 offen offset:0 lds\n"
"s_mov_b32 s83, 8448\n"
"buffer_load_dwordx4 v124, s[36:39], s83 offen offset:1024 lds\n"
"s_waitcnt lgkmcnt(3)\n"
"v_mfma_f32_16x16x32_bf16 v[32:35], v[48:51], v[224:227], v[32:35]\n"
"v_mfma_f32_16x16x32_bf16 v[36:39], v[224:227], v[224:227], v[36:39]\n"
"s_waitcnt lgkmcnt(2)\n"
"v_mfma_f32_16x16x32_bf16 v[32:35], v[48:51], v[232:235], v[32:35]\n"
"v_mfma_f32_16x16x32_bf16 v[36:39], v[232:235], v[232:235], v[36:39]\n"
"s_waitcnt vmcnt(6)\n"
"ds_read_b128 v[96:99], v54 offset:16384\n"
"ds_read_b128 v[104:107], v55 offset:16384\n"
"s_add_u32 m0, s77, 18432\n"
"s_mov_b32 s82, 384\n"
"buffer_load_dwordx4 v125, s[36:39], s82 offen offset:0 lds\n"
"s_mov_b32 s83, 8576\n"
"buffer_load_dwordx4 v124, s[36:39], s83 offen offset:1024 lds\n"
"s_waitcnt lgkmcnt(3)\n"
"v_mfma_f32_16x16x32_bf16 v[32:35], v[48:51], v[240:243], v[32:35]\n"
"v_mfma_f32_16x16x32_bf16 v[36:39], v[240:243], v[240:243], v[36:39]\n"
"s_waitcnt lgkmcnt(2)\n"
"v_mfma_f32_16x16x32_bf16 v[32:35], v[48:51], v[248:251], v[32:35]\n"
"v_mfma_f32_16x16x32_bf16 v[36:39], v[248:251], v[248:251], v[36:39]\n"
"s_add_u32 m0, s77, 32768\n"
"s_mov_b32 s82, 512\n"
"buffer_load_dwordx4 v125, s[36:39], s82 offen offset:0 lds\n"
"s_mov_b32 s83, 8704\n"
"buffer_load_dwordx4 v124, s[36:39], s83 offen offset:1024 lds\n"
"s_waitcnt lgkmcnt(1)\n"
"v_mfma_f32_16x16x32_bf16 v[32:35], v[48:51], v[96:99], v[32:35]\n"
"v_mfma_f32_16x16x32_bf16 v[36:39], v[96:99], v[96:99], v[36:39]\n"
"s_waitcnt lgkmcnt(0)\n"
"v_mfma_f32_16x16x32_bf16 v[32:35], v[48:51], v[104:107], v[32:35]\n"
"v_mfma_f32_16x16x32_bf16 v[36:39], v[104:107], v[104:107], v[36:39]\n"
"s_mov_b32 s100, 0x22222222\n"
"s_mov_b32 s101, 0x22222222\n"
"s_nop 5\n"
"v_cndmask_b32 v36, v36, v37, s[100:101]\n"
"s_mov_b32 s100, 0x44444444\n"
"s_mov_b32 s101, 0x44444444\n"
"v_cndmask_b32 v36, v36, v38, s[100:101]\n"
"s_mov_b32 s100, 0x88888888\n"
"s_mov_b32 s101, 0x88888888\n"
"v_cndmask_b32 v36, v36, v39, s[100:101]\n"
"s_mov_b32 s100, 0x00f0000f\n"
"s_mov_b32 s101, 0xf0000f00\n"
"v_cndmask_b32 v36, 0, v36, s[100:101]\n"
"v_mul_f32 v32, 0x3ae38e39, v32\n"
"v_mov_b32 v37, v36\n"
"s_nop 1\n"
"v_permlane32_swap_b32 v36, v37\n"
"v_add_f32 v36, v36, v37\n"
"v_mov_b32 v37, v36\n"
"s_nop 1\n"
"v_permlane16_swap_b32 v36, v37\n"
"v_add_f32 v36, v36, v37\n"
"v_mul_f32 v34, v32, v32\n"
"v_fma_f32 v36, v36, s98, -v34\n"
"v_max_f32 v36, 0, v36\n"
"v_add_f32 v36, 0x3727c5ac, v36\n"
"v_rsq_f32 v36, v36\n"
"s_nop 0\n"
"v_mul_f32 v36, 0xbfb8aa3b, v36\n"
"v_cvt_pk_bf16_f32 v36, v36, v36\n"
"v_and_b32 v35, 0xffff0000, v36\n"
"v_and_b32 v33, 0xffff, v36\n"
"v_mul_f32 v32, -v32, v35\n"
"s_barrier\n"
"v_dot2_f32_bf16 v64, v128, v33, v32\n"
"v_dot2_f32_bf16 v65, v128, v35, v32\n"
"v_dot2_f32_bf16 v66, v129, v33, v32\n"
"v_dot2_f32_bf16 v67, v129, v35, v32\n"
"v_dot2_f32_bf16 v68, v130, v33, v32\n"
"v_dot2_f32_bf16 v69, v130, v35, v32\n"
"v_dot2_f32_bf16 v70, v131, v33, v32\n"
"v_dot2_f32_bf16 v71, v131, v35, v32\n"
"v_cvt_pk_bf16_f32 v128, v64, v65\n"
"v_cvt_pk_bf16_f32 v129, v66, v67\n"
"v_cvt_pk_bf16_f32 v130, v68, v69\n"
"v_cvt_pk_bf16_f32 v131, v70, v71\n"
"s_add_u32 m0, s77, 34816\n"
"s_mov_b32 s82, 640\n"
"buffer_load_dwordx4 v125, s[36:39], s82 offen offset:0 lds\n"
"s_mov_b32 s83, 8832\n"
"buffer_load_dwordx4 v124, s[36:39], s83 offen offset:1024 lds\n"
"ds_write_b128 v120, v[128:131] offset:0\n"
"v_dot2_f32_bf16 v72, v136, v33, v32\n"
"v_dot2_f32_bf16 v73, v136, v35, v32\n"
"v_dot2_f32_bf16 v74, v137, v33, v32\n"
"v_dot2_f32_bf16 v75, v137, v35, v32\n"
"v_dot2_f32_bf16 v76, v138, v33, v32\n"
"v_dot2_f32_bf16 v77, v138, v35, v32\n"
"v_dot2_f32_bf16 v78, v139, v33, v32\n"
"v_dot2_f32_bf16 v79, v139, v35, v32\n"
"v_cvt_pk_bf16_f32 v136, v72, v73\n"
"v_cvt_pk_bf16_f32 v137, v74, v75\n"
"v_cvt_pk_bf16_f32 v138, v76, v77\n"
"v_cvt_pk_bf16_f32 v139, v78, v79\n"
"s_add_u32 m0, s77, 49152\n"
"s_mov_b32 s82, 768\n"
"buffer_load_dwordx4 v125, s[36:39], s82 offen offset:0 lds\n"
"s_mov_b32 s83, 8960\n"
"buffer_load_dwordx4 v124, s[36:39], s83 offen offset:1024 lds\n"
"ds_write_b128 v120, v[136:139] offset:4096\n"
"v_dot2_f32_bf16 v64, v144, v33, v32\n"
"v_dot2_f32_bf16 v65, v144, v35, v32\n"
"v_dot2_f32_bf16 v66, v145, v33, v32\n"
"v_dot2_f32_bf16 v67, v145, v35, v32\n"
"v_dot2_f32_bf16 v68, v146, v33, v32\n"
"v_dot2_f32_bf16 v69, v146, v35, v32\n"
"v_dot2_f32_bf16 v70, v147, v33, v32\n"
"v_dot2_f32_bf16 v71, v147, v35, v32\n"
"v_cvt_pk_bf16_f32 v144, v64, v65\n"
"v_cvt_pk_bf16_f32 v145, v66, v67\n"
"v_cvt_pk_bf16_f32 v146, v68, v69\n"
"v_cvt_pk_bf16_f32 v147, v70, v71\n"
"s_add_u32 m0, s77, 51200\n"
"s_mov_b32 s82, 896\n"
"buffer_load_dwordx4 v125, s[36:39], s82 offen offset:0 lds\n"
"s_mov_b32 s83, 9088\n"
"buffer_load_dwordx4 v124, s[36:39], s83 offen offset:1024 lds\n"
"s_waitcnt vmcnt(14)\n"
"ds_read_b128 v[132:135], v56 offset:0\n"
"ds_read_b128 v[140:143], v57 offset:0\n"
"ds_write_b128 v120, v[144:147] offset:8192\n"
"v_dot2_f32_bf16 v72, v152, v33, v32\n"
"v_dot2_f32_bf16 v73, v152, v35, v32\n"
"v_dot2_f32_bf16 v74, v153, v33, v32\n"
"v_dot2_f32_bf16 v75, v153, v35, v32\n"
"v_dot2_f32_bf16 v76, v154, v33, v32\n"
"v_dot2_f32_bf16 v77, v154, v35, v32\n"
"v_dot2_f32_bf16 v78, v155, v33, v32\n"
"v_dot2_f32_bf16 v79, v155, v35, v32\n"
"v_cvt_pk_bf16_f32 v152, v72, v73\n"
"v_cvt_pk_bf16_f32 v153, v74, v75\n"
"v_cvt_pk_bf16_f32 v154, v76, v77\n"
"v_cvt_pk_bf16_f32 v155, v78, v79\n"
"s_add_u32 m0, s88, 0\n"
"s_mov_b32 s82, 1024\n"
"buffer_load_dwordx4 v125, s[36:39], s82 offen offset:0 lds\n"
"s_mov_b32 s83, 9216\n"
"buffer_load_dwordx4 v124, s[36:39], s83 offen offset:1024 lds\n"
"ds_write_b128 v120, v[152:155] offset:12288\n"
"v_dot2_f32_bf16 v64, v160, v33, v32\n"
"v_dot2_f32_bf16 v65, v160, v35, v32\n"
"v_dot2_f32_bf16 v66, v161, v33, v32\n"
"v_dot2_f32_bf16 v67, v161, v35, v32\n"
"v_dot2_f32_bf16 v68, v162, v33, v32\n"
"v_dot2_f32_bf16 v69, v162, v35, v32\n"
"v_dot2_f32_bf16 v70, v163, v33, v32\n"
"v_dot2_f32_bf16 v71, v163, v35, v32\n"
"s_waitcnt lgkmcnt(3)\n"
"v_mfma_f32_16x16x32_bf16 v[40:43], v[48:51], v[132:135], 0\n"
"v_cvt_pk_bf16_f32 v160, v64, v65\n"
"v_cvt_pk_bf16_f32 v161, v66, v67\n"
"v_mfma_f32_16x16x32_bf16 v[44:47], v[132:135], v[132:135], 0\n"
"v_cvt_pk_bf16_f32 v162, v68, v69\n"
"v_cvt_pk_bf16_f32 v163, v70, v71\n"
"s_waitcnt vmcnt(14)\n"
"ds_read_b128 v[148:151], v56 offset:2048\n"
"ds_read_b128 v[156:159], v57 offset:2048\n"
"ds_write_b128 v120, v[160:163] offset:16384\n"
"v_dot2_f32_bf16 v72, v168, v33, v32\n"
"v_dot2_f32_bf16 v73, v168, v35, v32\n"
"v_dot2_f32_bf16 v74, v169, v33, v32\n"
"v_dot2_f32_bf16 v75, v169, v35, v32\n"
"v_dot2_f32_bf16 v76, v170, v33, v32\n"
"v_dot2_f32_bf16 v77, v170, v35, v32\n"
"v_dot2_f32_bf16 v78, v171, v33, v32\n"
"v_dot2_f32_bf16 v79, v171, v35, v32\n"
"s_waitcnt lgkmcnt(5)\n"
"v_mfma_f32_16x16x32_bf16 v[40:43], v[48:51], v[140:143], v[40:43]\n"
"v_cvt_pk_bf16_f32 v168, v72, v73\n"
"v_cvt_pk_bf16_f32 v169, v74, v75\n"
"v_mfma_f32_16x16x32_bf16 v[44:47], v[140:143], v[140:143], v[44:47]\n"
"v_cvt_pk_bf16_f32 v170, v76, v77\n"
"v_cvt_pk_bf16_f32 v171, v78, v79\n"
"buffer_load_dword a216, v13, s[44:47], s78 offen offset:0\n"
"ds_write_b128 v120, v[168:171] offset:20480\n"
"v_dot2_f32_bf16 v64, v176, v33, v32\n"
"v_dot2_f32_bf16 v65, v176, v35, v32\n"
"v_dot2_f32_bf16 v66, v177, v33, v32\n"
"v_dot2_f32_bf16 v67, v177, v35, v32\n"
"v_dot2_f32_bf16 v68, v178, v33, v32\n"
"v_dot2_f32_bf16 v69, v178, v35, v32\n"
"v_dot2_f32_bf16 v70, v179, v33, v32\n"
"v_dot2_f32_bf16 v71, v179, v35, v32\n"
"s_waitcnt lgkmcnt(3)\n"
"v_mfma_f32_16x16x32_bf16 v[40:43], v[48:51], v[148:151], v[40:43]\n"
"v_cvt_pk_bf16_f32 v176, v64, v65\n"
"v_cvt_pk_bf16_f32 v177, v66, v67\n"
"v_mfma_f32_16x16x32_bf16 v[44:47], v[148:151], v[148:151], v[44:47]\n"
"v_cvt_pk_bf16_f32 v178, v68, v69\n"
"v_cvt_pk_bf16_f32 v179, v70, v71\n"
"s_waitcnt vmcnt(13)\n"
"ds_read_b128 v[164:167], v56 offset:16384\n"
"ds_read_b128 v[172:175], v57 offset:16384\n"
"buffer_load_dwordx4 v[80:83], v119, s[40:43], s89 offen\n"
"s_add_u32 s89, s89, 884736\n"
"ds_write_b128 v120, v[176:179] offset:24576\n"
"v_dot2_f32_bf16 v72, v184, v33, v32\n"
"v_dot2_f32_bf16 v73, v184, v35, v32\n"
"v_dot2_f32_bf16 v74, v185, v33, v32\n"
"v_dot2_f32_bf16 v75, v185, v35, v32\n"
"v_dot2_f32_bf16 v76, v186, v33, v32\n"
"v_dot2_f32_bf16 v77, v186, v35, v32\n"
"v_dot2_f32_bf16 v78, v187, v33, v32\n"
"v_dot2_f32_bf16 v79, v187, v35, v32\n"
"s_waitcnt lgkmcnt(5)\n"
"v_mfma_f32_16x16x32_bf16 v[40:43], v[48:51], v[156:159], v[40:43]\n"
"v_cvt_pk_bf16_f32 v184, v72, v73\n"
"v_cvt_pk_bf16_f32 v185, v74, v75\n"
"v_mfma_f32_16x16x32_bf16 v[44:47], v[156:159], v[156:159], v[44:47]\n"
"v_cvt_pk_bf16_f32 v186, v76, v77\n"
"v_cvt_pk_bf16_f32 v187, v78, v79\n"
"buffer_load_dwordx4 v[84:87], v119, s[40:43], s89 offen\n"
"s_add_u32 s89, s89, 884736\n"
"ds_write_b128 v120, v[184:187] offset:28672\n"
"v_dot2_f32_bf16 v64, v192, v33, v32\n"
"v_dot2_f32_bf16 v65, v192, v35, v32\n"
"v_dot2_f32_bf16 v66, v193, v33, v32\n"
"v_dot2_f32_bf16 v67, v193, v35, v32\n"
"v_dot2_f32_bf16 v68, v194, v33, v32\n"
"v_dot2_f32_bf16 v69, v194, v35, v32\n"
"v_dot2_f32_bf16 v70, v195, v33, v32\n"
"v_dot2_f32_bf16 v71, v195, v35, v32\n"
"s_waitcnt lgkmcnt(3)\n"
"v_mfma_f32_16x16x32_bf16 v[40:43], v[48:51], v[164:167], v[40:43]\n"
"v_cvt_pk_bf16_f32 v192, v64, v65\n"
"v_cvt_pk_bf16_f32 v193, v66, v67\n"
"v_mfma_f32_16x16x32_bf16 v[44:47], v[164:167], v[164:167], v[44:47]\n"
"v_cvt_pk_bf16_f32 v194, v68, v69\n"
"v_cvt_pk_bf16_f32 v195, v70, v71\n"
"s_waitcnt vmcnt(13)\n"
"ds_read_b128 v[180:183], v56 offset:18432\n"
"ds_read_b128 v[188:191], v57 offset:18432\n"
"buffer_load_dwordx4 v[88:91], v119, s[40:43], s89 offen\n"
"ds_write_b128 v121, v[192:195] offset:0\n"
"v_dot2_f32_bf16 v72, v200, v33, v32\n"
"v_dot2_f32_bf16 v73, v200, v35, v32\n"
"v_dot2_f32_bf16 v74, v201, v33, v32\n"
"v_dot2_f32_bf16 v75, v201, v35, v32\n"
"v_dot2_f32_bf16 v76, v202, v33, v32\n"
"v_dot2_f32_bf16 v77, v202, v35, v32\n"
"v_dot2_f32_bf16 v78, v203, v33, v32\n"
"v_dot2_f32_bf16 v79, v203, v35, v32\n"
"s_waitcnt lgkmcnt(5)\n"
"v_mfma_f32_16x16x32_bf16 v[40:43], v[48:51], v[172:175], v[40:43]\n"
"v_cvt_pk_bf16_f32 v200, v72, v73\n"
"v_cvt_pk_bf16_f32 v201, v74, v75\n"
"v_mfma_f32_16x16x32_bf16 v[44:47], v[172:175], v[172:175], v[44:47]\n"
"v_cvt_pk_bf16_f32 v202, v76, v77\n"
"v_cvt_pk_bf16_f32 v203, v78, v79\n"
"s_lshl_b32 s96, s75, 3\n"
"s_xor_b32 s96, s96, 8\n"
"buffer_load_dwordx2 v[14:15], v118, s[64:67], s96 offen sc1\n"
"ds_write_b128 v121, v[200:203] offset:4096\n"
"v_dot2_f32_bf16 v64, v208, v33, v32\n"
"v_dot2_f32_bf16 v65, v208, v35, v32\n"
"v_dot2_f32_bf16 v66, v209, v33, v32\n"
"v_dot2_f32_bf16 v67, v209, v35, v32\n"
"v_dot2_f32_bf16 v68, v210, v33, v32\n"
"v_dot2_f32_bf16 v69, v210, v35, v32\n"
"v_dot2_f32_bf16 v70, v211, v33, v32\n"
"v_dot2_f32_bf16 v71, v211, v35, v32\n"
"s_waitcnt lgkmcnt(3)\n"
"v_mfma_f32_16x16x32_bf16 v[40:43], v[48:51], v[180:183], v[40:43]\n"
"v_cvt_pk_bf16_f32 v208, v64, v65\n"
"v_cvt_pk_bf16_f32 v209, v66, v67\n"
"v_mfma_f32_16x16x32_bf16 v[44:47], v[180:183], v[180:183], v[44:47]\n"
"v_cvt_pk_bf16_f32 v210, v68, v69\n"
"v_cvt_pk_bf16_f32 v211, v70, v71\n"
"s_waitcnt vmcnt(13)\n"
"ds_read_b128 v[196:199], v56 offset:32768\n"
"ds_read_b128 v[204:207], v57 offset:32768\n"
"s_lshl_b32 s96, s75, 3\n"
"buffer_load_dwordx2 v[28:29], v118, s[64:67], s96 offen sc1\n"
"ds_write_b128 v121, v[208:211] offset:8192\n"
"v_dot2_f32_bf16 v72, v216, v33, v32\n"
"v_dot2_f32_bf16 v73, v216, v35, v32\n"
"v_dot2_f32_bf16 v74, v217, v33, v32\n"
"v_dot2_f32_bf16 v75, v217, v35, v32\n"
"v_dot2_f32_bf16 v76, v218, v33, v32\n"
"v_dot2_f32_bf16 v77, v218, v35, v32\n"
"v_dot2_f32_bf16 v78, v219, v33, v32\n"
"v_dot2_f32_bf16 v79, v219, v35, v32\n"
"s_waitcnt lgkmcnt(5)\n"
"v_mfma_f32_16x16x32_bf16 v[40:43], v[48:51], v[188:191], v[40:43]\n"
"v_cvt_pk_bf16_f32 v216, v72, v73\n"
"v_cvt_pk_bf16_f32 v217, v74, v75\n"
"v_mfma_f32_16x16x32_bf16 v[44:47], v[188:191], v[188:191], v[44:47]\n"
"v_cvt_pk_bf16_f32 v218, v76, v77\n"
"v_cvt_pk_bf16_f32 v219, v78, v79\n"
"s_add_u32 m0, s77, 147456\n"
"s_add_u32 s82, s78, 4096\n"
"buffer_load_dwordx4 v11, s[44:47], s82 offen offset:0 lds\n"
"ds_write_b128 v121, v[216:219] offset:12288\n"
"v_dot2_f32_bf16 v64, v224, v33, v32\n"
"v_dot2_f32_bf16 v65, v224, v35, v32\n"
"v_dot2_f32_bf16 v66, v225, v33, v32\n"
"v_dot2_f32_bf16 v67, v225, v35, v32\n"
"v_dot2_f32_bf16 v68, v226, v33, v32\n"
"v_dot2_f32_bf16 v69, v226, v35, v32\n"
"v_dot2_f32_bf16 v70, v227, v33, v32\n"
"v_dot2_f32_bf16 v71, v227, v35, v32\n"
"s_waitcnt lgkmcnt(3)\n"
"v_mfma_f32_16x16x32_bf16 v[40:43], v[48:51], v[196:199], v[40:43]\n"
"v_cvt_pk_bf16_f32 v224, v64, v65\n"
"v_cvt_pk_bf16_f32 v225, v66, v67\n"
"v_mfma_f32_16x16x32_bf16 v[44:47], v[196:199], v[196:199], v[44:47]\n"
"v_cvt_pk_bf16_f32 v226, v68, v69\n"
"v_cvt_pk_bf16_f32 v227, v70, v71\n"
"s_waitcnt vmcnt(13)\n"
"ds_read_b128 v[212:215], v56 offset:34816\n"
"ds_read_b128 v[220:223], v57 offset:34816\n"
"s_add_u32 m0, s77, 147456\n"
"s_add_u32 s83, s78, 297984\n"
"buffer_load_dwordx4 v11, s[44:47], s83 offen offset:1024 lds\n"
"ds_write_b128 v121, v[224:227] offset:16384\n"
"v_dot2_f32_bf16 v72, v232, v33, v32\n"
"v_dot2_f32_bf16 v73, v232, v35, v32\n"
"v_dot2_f32_bf16 v74, v233, v33, v32\n"
"v_dot2_f32_bf16 v75, v233, v35, v32\n"
"v_dot2_f32_bf16 v76, v234, v33, v32\n"
"v_dot2_f32_bf16 v77, v234, v35, v32\n"
"v_dot2_f32_bf16 v78, v235, v33, v32\n"
"v_dot2_f32_bf16 v79, v235, v35, v32\n"
"s_waitcnt lgkmcnt(5)\n"
"v_mfma_f32_16x16x32_bf16 v[40:43], v[48:51], v[204:207], v[40:43]\n"
"v_cvt_pk_bf16_f32 v232, v72, v73\n"
"v_cvt_pk_bf16_f32 v233, v74, v75\n"
"v_mfma_f32_16x16x32_bf16 v[44:47], v[204:207], v[204:207], v[44:47]\n"
"v_cvt_pk_bf16_f32 v234, v76, v77\n"
"v_cvt_pk_bf16_f32 v235, v78, v79\n"
"s_add_u32 m0, s77, 147456\n"
"s_add_u32 s82, s78, 591872\n"
"buffer_load_dwordx4 v11, s[44:47], s82 offen offset:2048 lds\n"
"ds_write_b128 v121, v[232:235] offset:20480\n"
"v_dot2_f32_bf16 v64, v240, v33, v32\n"
"v_dot2_f32_bf16 v65, v240, v35, v32\n"
"v_dot2_f32_bf16 v66, v241, v33, v32\n"
"v_dot2_f32_bf16 v67, v241, v35, v32\n"
"v_dot2_f32_bf16 v68, v242, v33, v32\n"
"v_dot2_f32_bf16 v69, v242, v35, v32\n"
"v_dot2_f32_bf16 v70, v243, v33, v32\n"
"v_dot2_f32_bf16 v71, v243, v35, v32\n"
"s_waitcnt lgkmcnt(3)\n"
"v_mfma_f32_16x16x32_bf16 v[40:43], v[48:51], v[212:215], v[40:43]\n"
"v_cvt_pk_bf16_f32 v240, v64, v65\n"
"v_cvt_pk_bf16_f32 v241, v66, v67\n"
"v_mfma_f32_16x16x32_bf16 v[44:47], v[212:215], v[212:215], v[44:47]\n"
"v_cvt_pk_bf16_f32 v242, v68, v69\n"
"v_cvt_pk_bf16_f32 v243, v70, v71\n"
"s_waitcnt vmcnt(13)\n"
"ds_read_b128 v[228:231], v56 offset:49152\n"
"ds_read_b128 v[236:239], v57 offset:49152\n"
"s_add_u32 m0, s77, 147456\n"
"s_add_u32 s83, s78, 885760\n"
"buffer_load_dwordx4 v11, s[44:47], s83 offen offset:3072 lds\n"
"ds_write_b128 v121, v[240:243] offset:24576\n"
"v_dot2_f32_bf16 v72, v248, v33, v32\n"
"v_dot2_f32_bf16 v73, v248, v35, v32\n"
"v_dot2_f32_bf16 v74, v249, v33, v32\n"
"v_dot2_f32_bf16 v75, v249, v35, v32\n"
"v_dot2_f32_bf16 v76, v250, v33, v32\n"
"v_dot2_f32_bf16 v77, v250, v35, v32\n"
"v_dot2_f32_bf16 v78, v251, v33, v32\n"
"v_dot2_f32_bf16 v79, v251, v35, v32\n"
"s_waitcnt lgkmcnt(5)\n"
"v_mfma_f32_16x16x32_bf16 v[40:43], v[48:51], v[220:223], v[40:43]\n"
"v_cvt_pk_bf16_f32 v248, v72, v73\n"
"v_cvt_pk_bf16_f32 v249, v74, v75\n"
"v_mfma_f32_16x16x32_bf16 v[44:47], v[220:223], v[220:223], v[44:47]\n"
"v_cvt_pk_bf16_f32 v250, v76, v77\n"
"v_cvt_pk_bf16_f32 v251, v78, v79\n"
"buffer_load_dword a217, v13, s[48:51], s78 offen offset:0\n"
"ds_write_b128 v121, v[248:251] offset:28672\n"
"v_dot2_f32_bf16 v64, v96, v33, v32\n"
"v_dot2_f32_bf16 v65, v96, v35, v32\n"
"v_dot2_f32_bf16 v66, v97, v33, v32\n"
"v_dot2_f32_bf16 v67, v97, v35, v32\n"
"v_dot2_f32_bf16 v68, v98, v33, v32\n"
"v_dot2_f32_bf16 v69, v98, v35, v32\n"
"v_dot2_f32_bf16 v70, v99, v33, v32\n"
"v_dot2_f32_bf16 v71, v99, v35, v32\n"
"s_waitcnt lgkmcnt(3)\n"
"v_mfma_f32_16x16x32_bf16 v[40:43], v[48:51], v[228:231], v[40:43]\n"
"v_cvt_pk_bf16_f32 v96, v64, v65\n"
"v_cvt_pk_bf16_f32 v97, v66, v67\n"
"v_mfma_f32_16x16x32_bf16 v[44:47], v[228:231], v[228:231], v[44:47]\n"
"v_cvt_pk_bf16_f32 v98, v68, v69\n"
"v_cvt_pk_bf16_f32 v99, v70, v71\n"
"s_waitcnt vmcnt(13)\n"
"ds_read_b128 v[244:247], v56 offset:51200\n"
"ds_read_b128 v[252:255], v57 offset:51200\n"
"ds_write_b128 v121, v[96:99] offset:32768\n"
"v_dot2_f32_bf16 v72, v104, v33, v32\n"
"v_dot2_f32_bf16 v73, v104, v35, v32\n"
"v_dot2_f32_bf16 v74, v105, v33, v32\n"
"v_dot2_f32_bf16 v75, v105, v35, v32\n"
"v_dot2_f32_bf16 v76, v106, v33, v32\n"
"v_dot2_f32_bf16 v77, v106, v35, v32\n"
"v_dot2_f32_bf16 v78, v107, v33, v32\n"
"v_dot2_f32_bf16 v79, v107, v35, v32\n"
"s_waitcnt lgkmcnt(5)\n"
"v_mfma_f32_16x16x32_bf16 v[40:43], v[48:51], v[236:239], v[40:43]\n"
"v_cvt_pk_bf16_f32 v104, v72, v73\n"
"v_cvt_pk_bf16_f32 v105, v74, v75\n"
"v_mfma_f32_16x16x32_bf16 v[44:47], v[236:239], v[236:239], v[44:47]\n"
"v_cvt_pk_bf16_f32 v106, v76, v77\n"
"v_cvt_pk_bf16_f32 v107, v78, v79\n"
"ds_write_b128 v121, v[104:107] offset:36864\n"
"s_waitcnt vmcnt(11)\n"
"ds_read_b128 v[100:103], v58 offset:0\n"
"ds_read_b128 v[108:111], v59 offset:0\n"
"s_waitcnt lgkmcnt(5)\n"
"v_mfma_f32_16x16x32_bf16 v[40:43], v[48:51], v[244:247], v[40:43]\n"
"v_mfma_f32_16x16x32_bf16 v[44:47], v[244:247], v[244:247], v[44:47]\n"
"s_waitcnt lgkmcnt(4)\n"
"v_mfma_f32_16x16x32_bf16 v[40:43], v[48:51], v[252:255], v[40:43]\n"
"v_mfma_f32_16x16x32_bf16 v[44:47], v[252:255], v[252:255], v[44:47]\n"
"s_waitcnt lgkmcnt(1)\n"
"v_mfma_f32_16x16x32_bf16 v[40:43], v[48:51], v[100:103], v[40:43]\n"
"v_mfma_f32_16x16x32_bf16 v[44:47], v[100:103], v[100:103], v[44:47]\n"
"s_waitcnt lgkmcnt(0)\n"
"v_mfma_f32_16x16x32_bf16 v[40:43], v[48:51], v[108:111], v[40:43]\n"
"v_mfma_f32_16x16x32_bf16 v[44:47], v[108:111], v[108:111], v[44:47]\n"
"s_barrier\n"
"s_mov_b32 s100, 0x22222222\n"
"s_mov_b32 s101, 0x22222222\n"
"s_nop 4\n"
"v_cndmask_b32 v44, v44, v45, s[100:101]\n"
"ds_read_b128 a[0:3], v122 offset:0\n"
"s_mov_b32 s100, 0x44444444\n"
"s_mov_b32 s101, 0x44444444\n"
"v_cndmask_b32 v44, v44, v46, s[100:101]\n"
"ds_read_b128 a[8:11], v122 offset:4096\n"
"s_mov_b32 s100, 0x88888888\n"
"s_mov_b32 s101, 0x88888888\n"
"v_cndmask_b32 v44, v44, v47, s[100:101]\n"
"ds_read_b128 a[16:19], v122 offset:8192\n"
"s_mov_b32 s100, 0x00f0000f\n"
"s_mov_b32 s101, 0xf0000f00\n"
"v_cndmask_b32 v44, 0, v44, s[100:101]\n"
"ds_read_b128 a[24:27], v122 offset:12288\n"
"v_mul_f32 v40, 0x3ae38e39, v40\n"
"ds_read_b128 a[32:35], v122 offset:16384\n"
"v_mov_b32 v45, v44\n"
"ds_read_b128 a[40:43], v122 offset:20480\n"
"s_nop 0\n"
"v_permlane32_swap_b32 v44, v45\n"
"ds_read_b128 a[48:51], v122 offset:24576\n"
"v_add_f32 v44, v44, v45\n"
"ds_read_b128 a[56:59], v122 offset:28672\n"
"v_mov_b32 v45, v44\n"
"ds_read_b128 a[64:67], v123 offset:0\n"
"s_nop 0\n"
"v_permlane16_swap_b32 v44, v45\n"
"ds_read_b128 a[72:75], v123 offset:4096\n"
"v_add_f32 v44, v44, v45\n"
"ds_read_b128 a[80:83], v123 offset:8192\n"
"v_mul_f32 v42, v40, v40\n"
"ds_read_b128 a[88:91], v123 offset:12288\n"
"v_fma_f32 v44, v44, s98, -v42\n"
"ds_read_b128 a[96:99], v123 offset:16384\n"
"v_max_f32 v44, 0, v44\n"
"ds_read_b128 a[104:107], v123 offset:20480\n"
"v_add_f32 v44, 0x3727c5ac, v44\n"
"ds_read_b128 a[112:115], v123 offset:24576\n"
"v_rsq_f32 v44, v44\n"
"ds_read_b128 a[120:123], v123 offset:28672\n"
"v_mul_f32 v44, 0xbfb8aa3b, v44\n"
"ds_read_b128 a[128:131], v123 offset:32768\n"
"v_cvt_pk_bf16_f32 v44, v44, v44\n"
"ds_read_b128 a[136:139], v123 offset:36864\n"
"v_and_b32 v43, 0xffff0000, v44\n"
"ds_read_b128 a[144:147], v121 offset:32768\n"
"v_and_b32 v41, 0xffff, v44\n"
"ds_read_b128 a[152:155], v121 offset:36864\n"
"v_mul_f32 v40, -v40, v43\n"
"s_mul_i32 s96, s75, 147456\n"
"s_lshl_b32 s97, s72, 10\n"
"s_add_u32 s96, s96, s97\n"
"s_add_u32 s40, s96, 1048576\n"
"s_lshl_b32 s96, s76, 12\n"
"s_add_u32 s89, s96, s40\n"
"s_mov_b32 s13, 293888\n"
"s_mov_b32 s15, 297984\n"
"s_waitcnt lgkmcnt(0)\n"
"s_barrier\n"
"v_dot2_f32_bf16 v64, v132, v41, v40\n"
"v_dot2_f32_bf16 v65, v132, v43, v40\n"
"v_dot2_f32_bf16 v66, v133, v41, v40\n"
"v_dot2_f32_bf16 v67, v133, v43, v40\n"
"v_dot2_f32_bf16 v68, v134, v41, v40\n"
"v_dot2_f32_bf16 v69, v134, v43, v40\n"
"v_dot2_f32_bf16 v70, v135, v41, v40\n"
"v_dot2_f32_bf16 v71, v135, v43, v40\n"
"v_cvt_pk_bf16_f32 v132, v64, v65\n"
"v_cvt_pk_bf16_f32 v133, v66, v67\n"
"v_cvt_pk_bf16_f32 v134, v68, v69\n"
"v_cvt_pk_bf16_f32 v135, v70, v71\n"
"s_waitcnt vmcnt(9)\n"
"buffer_store_dwordx4 v[80:83], v9, s[64:67], s89 offen sc1\n"
"ds_write_b128 v120, v[132:135] offset:0\n"
"v_dot2_f32_bf16 v72, v140, v41, v40\n"
"v_dot2_f32_bf16 v73, v140, v43, v40\n"
"v_dot2_f32_bf16 v74, v141, v41, v40\n"
"v_dot2_f32_bf16 v75, v141, v43, v40\n"
"v_dot2_f32_bf16 v76, v142, v41, v40\n"
"v_dot2_f32_bf16 v77, v142, v43, v40\n"
"v_dot2_f32_bf16 v78, v143, v41, v40\n"
"v_dot2_f32_bf16 v79, v143, v43, v40\n"
"v_cvt_pk_bf16_f32 v140, v72, v73\n"
"v_cvt_pk_bf16_f32 v141, v74, v75\n"
"v_cvt_pk_bf16_f32 v142, v76, v77\n"
"v_cvt_pk_bf16_f32 v143, v78, v79\n"
"s_add_u32 s89, s89, 65536\n"
"s_waitcnt vmcnt(9)\n"
"buffer_store_dwordx4 v[84:87], v9, s[64:67], s89 offen sc1\n"
"ds_write_b128 v120, v[140:143] offset:4096\n"
"v_dot2_f32_bf16 v64, v148, v41, v40\n"
"v_dot2_f32_bf16 v65, v148, v43, v40\n"
"v_dot2_f32_bf16 v66, v149, v41, v40\n"
"v_dot2_f32_bf16 v67, v149, v43, v40\n"
"v_dot2_f32_bf16 v68, v150, v41, v40\n"
"v_dot2_f32_bf16 v69, v150, v43, v40\n"
"v_dot2_f32_bf16 v70, v151, v41, v40\n"
"v_dot2_f32_bf16 v71, v151, v43, v40\n"
"v_cvt_pk_bf16_f32 v148, v64, v65\n"
"v_cvt_pk_bf16_f32 v149, v66, v67\n"
"v_cvt_pk_bf16_f32 v150, v68, v69\n"
"v_cvt_pk_bf16_f32 v151, v70, v71\n"
"s_add_u32 s89, s89, 65536\n"
"s_cmp_lt_u32 s76, 4\n"
"s_cselect_b32 s89, s89, 0x7ff00000\n"
"s_waitcnt vmcnt(9)\n"
"buffer_store_dwordx4 v[88:91], v9, s[64:67], s89 offen sc1\n"
"ds_write_b128 v120, v[148:151] offset:8192\n"
"v_dot2_f32_bf16 v72, v156, v41, v40\n"
"v_dot2_f32_bf16 v73, v156, v43, v40\n"
"v_dot2_f32_bf16 v74, v157, v41, v40\n"
"v_dot2_f32_bf16 v75, v157, v43, v40\n"
"v_dot2_f32_bf16 v76, v158, v41, v40\n"
"v_dot2_f32_bf16 v77, v158, v43, v40\n"
"v_dot2_f32_bf16 v78, v159, v41, v40\n"
"v_dot2_f32_bf16 v79, v159, v43, v40\n"
"v_cvt_pk_bf16_f32 v156, v72, v73\n"
"v_cvt_pk_bf16_f32 v157, v74, v75\n"
"v_cvt_pk_bf16_f32 v158, v76, v77\n"
"v_cvt_pk_bf16_f32 v159, v78, v79\n"
"s_add_u32 m0, s77, 0\n"
"s_add_u32 s82, s78, 1183744\n"
"buffer_load_dwordx4 v11, s[44:47], s82 offen offset:0 lds\n"
"ds_write_b128 v120, v[156:159] offset:12288\n"
"v_dot2_f32_bf16 v64, v164, v41, v40\n"
"v_dot2_f32_bf16 v65, v164, v43, v40\n"
"v_dot2_f32_bf16 v66, v165, v41, v40\n"
"v_dot2_f32_bf16 v67, v165, v43, v40\n"
"v_dot2_f32_bf16 v68, v166, v41, v40\n"
"v_dot2_f32_bf16 v69, v166, v43, v40\n"
"v_dot2_f32_bf16 v70, v167, v41, v40\n"
"v_dot2_f32_bf16 v71, v167, v43, v40\n"
"v_cvt_pk_bf16_f32 v164, v64, v65\n"
"v_cvt_pk_bf16_f32 v165, v66, v67\n"
"v_cvt_pk_bf16_f32 v166, v68, v69\n"
"v_cvt_pk_bf16_f32 v167, v70, v71\n"
"s_add_u32 m0, s77, 0\n"
"s_add_u32 s83, s78, 1477632\n"
"buffer_load_dwordx4 v11, s[44:47], s83 offen offset:1024 lds\n"
"ds_write_b128 v120, v[164:167] offset:16384\n"
"v_dot2_f32_bf16 v72, v172, v41, v40\n"
"v_dot2_f32_bf16 v73, v172, v43, v40\n"
"v_dot2_f32_bf16 v74, v173, v41, v40\n"
"v_dot2_f32_bf16 v75, v173, v43, v40\n"
"v_dot2_f32_bf16 v76, v174, v41, v40\n"
"v_dot2_f32_bf16 v77, v174, v43, v40\n"
"v_dot2_f32_bf16 v78, v175, v41, v40\n"
"v_dot2_f32_bf16 v79, v175, v43, v40\n"
"v_cvt_pk_bf16_f32 v172, v72, v73\n"
"v_cvt_pk_bf16_f32 v173, v74, v75\n"
"v_cvt_pk_bf16_f32 v174, v76, v77\n"
"v_cvt_pk_bf16_f32 v175, v78, v79\n"
"s_add_u32 m0, s77, 0\n"
"s_add_u32 s82, s78, 1771520\n"
"buffer_load_dwordx4 v11, s[44:47], s82 offen offset:2048 lds\n"
"ds_write_b128 v120, v[172:175] offset:20480\n"
"v_dot2_f32_bf16 v64, v180, v41, v40\n"
"v_dot2_f32_bf16 v65, v180, v43, v40\n"
"v_dot2_f32_bf16 v66, v181, v41, v40\n"
"v_dot2_f32_bf16 v67, v181, v43, v40\n"
"v_dot2_f32_bf16 v68, v182, v41, v40\n"
"v_dot2_f32_bf16 v69, v182, v43, v40\n"
"v_dot2_f32_bf16 v70, v183, v41, v40\n"
"v_dot2_f32_bf16 v71, v183, v43, v40\n"
"v_cvt_pk_bf16_f32 v180, v64, v65\n"
"v_cvt_pk_bf16_f32 v181, v66, v67\n"
"v_cvt_pk_bf16_f32 v182, v68, v69\n"
"v_cvt_pk_bf16_f32 v183, v70, v71\n"
"s_add_u32 m0, s77, 0\n"
"s_add_u32 s83, s78, 2065408\n"
"buffer_load_dwordx4 v11, s[44:47], s83 offen offset:3072 lds\n"
"ds_write_b128 v120, v[180:183] offset:24576\n"
"v_dot2_f32_bf16 v72, v188, v41, v40\n"
"v_dot2_f32_bf16 v73, v188, v43, v40\n"
"v_dot2_f32_bf16 v74, v189, v41, v40\n"
"v_dot2_f32_bf16 v75, v189, v43, v40\n"
"v_dot2_f32_bf16 v76, v190, v41, v40\n"
"v_dot2_f32_bf16 v77, v190, v43, v40\n"
"v_dot2_f32_bf16 v78, v191, v41, v40\n"
"v_dot2_f32_bf16 v79, v191, v43, v40\n"
"v_cvt_pk_bf16_f32 v188, v72, v73\n"
"v_cvt_pk_bf16_f32 v189, v74, v75\n"
"v_cvt_pk_bf16_f32 v190, v76, v77\n"
"v_cvt_pk_bf16_f32 v191, v78, v79\n"
"s_add_u32 m0, s77, 16384\n"
"s_add_u32 s82, s78, 2363392\n"
"buffer_load_dwordx4 v11, s[44:47], s82 offen offset:0 lds\n"
"ds_write_b128 v120, v[188:191] offset:28672\n"
"v_dot2_f32_bf16 v64, v196, v41, v40\n"
"v_dot2_f32_bf16 v65, v196, v43, v40\n"
"v_dot2_f32_bf16 v66, v197, v41, v40\n"
"v_dot2_f32_bf16 v67, v197, v43, v40\n"
"v_dot2_f32_bf16 v68, v198, v41, v40\n"
"v_dot2_f32_bf16 v69, v198, v43, v40\n"
"v_dot2_f32_bf16 v70, v199, v41, v40\n"
"v_dot2_f32_bf16 v71, v199, v43, v40\n"
"v_cvt_pk_bf16_f32 v196, v64, v65\n"
"v_cvt_pk_bf16_f32 v197, v66, v67\n"
"v_cvt_pk_bf16_f32 v198, v68, v69\n"
"v_cvt_pk_bf16_f32 v199, v70, v71\n"
"s_waitcnt vmcnt(13)\n"
"v_readlane_b32 s84, v28, s76\n"
"v_readlane_b32 s85, v29, s76\n"
"v_mov_b32 v18, 0x9e3779b9\n"
"v_add_co_u32 v14, vcc, 0x7f4a7c15, v14\n"
"s_nop 1\n"
"v_addc_co_u32 v15, vcc, v18, v15, vcc\n"
"v_add_co_u32 v28, vcc, 0x7f4a7c15, v28\n"
"s_nop 1\n"
"v_addc_co_u32 v29, vcc, v18, v29, vcc\n"
"s_add_u32 s84, s84, 0x7f4a7c15\n"
"s_addc_u32 s85, s85, 0x9e3779b9\n"
"v_readlane_b32 s10, v14, s76\n"
"v_readlane_b32 s11, v15, s76\n"
"v_mov_b32 v30, 0x7ffffff0\n"
"v_cmp_eq_u32 vcc, 0, v9\n"
"s_nop 1\n"
"v_cndmask_b32 v30, v30, v9, vcc\n"
"v_mov_b32 v16, s84\n"
"v_mov_b32 v17, s85\n"
"s_lshl_b32 s96, s75, 9\n"
"s_add_u32 s99, s96, 12288\n"
"s_lshl_b32 s96, s72, 7\n"
"s_lshl_b32 s97, s76, 3\n"
"s_add_u32 s96, s96, s97\n"
"s_add_u32 s16, s96, s99\n"
"ds_write_b128 v121, v[196:199] offset:0\n"
"v_dot2_f32_bf16 v72, v204, v41, v40\n"
"v_dot2_f32_bf16 v73, v204, v43, v40\n"
"v_dot2_f32_bf16 v74, v205, v41, v40\n"
"v_dot2_f32_bf16 v75, v205, v43, v40\n"
"v_dot2_f32_bf16 v76, v206, v41, v40\n"
"v_dot2_f32_bf16 v77, v206, v43, v40\n"
"v_dot2_f32_bf16 v78, v207, v41, v40\n"
"v_dot2_f32_bf16 v79, v207, v43, v40\n"
"v_cvt_pk_bf16_f32 v204, v72, v73\n"
"v_cvt_pk_bf16_f32 v205, v74, v75\n"
"v_cvt_pk_bf16_f32 v206, v76, v77\n"
"v_cvt_pk_bf16_f32 v207, v78, v79\n"
"s_add_u32 m0, s77, 16384\n"
"s_add_u32 s83, s78, 2657280\n"
"buffer_load_dwordx4 v11, s[44:47], s83 offen offset:1024 lds\n"
"ds_write_b128 v121, v[204:207] offset:4096\n"
"v_dot2_f32_bf16 v64, v212, v41, v40\n"
"v_dot2_f32_bf16 v65, v212, v43, v40\n"
"v_dot2_f32_bf16 v66, v213, v41, v40\n"
"v_dot2_f32_bf16 v67, v213, v43, v40\n"
"v_dot2_f32_bf16 v68, v214, v41, v40\n"
"v_dot2_f32_bf16 v69, v214, v43, v40\n"
"v_dot2_f32_bf16 v70, v215, v41, v40\n"
"v_dot2_f32_bf16 v71, v215, v43, v40\n"
"v_cvt_pk_bf16_f32 v212, v64, v65\n"
"v_cvt_pk_bf16_f32 v213, v66, v67\n"
"v_cvt_pk_bf16_f32 v214, v68, v69\n"
"v_cvt_pk_bf16_f32 v215, v70, v71\n"
"s_add_u32 m0, s77, 16384\n"
"s_add_u32 s82, s78, 2951168\n"
"buffer_load_dwordx4 v11, s[44:47], s82 offen offset:2048 lds\n"
"ds_write_b128 v121, v[212:215] offset:8192\n"
"v_dot2_f32_bf16 v72, v220, v41, v40\n"
"v_dot2_f32_bf16 v73, v220, v43, v40\n"
"v_dot2_f32_bf16 v74, v221, v41, v40\n"
"v_dot2_f32_bf16 v75, v221, v43, v40\n"
"v_dot2_f32_bf16 v76, v222, v41, v40\n"
"v_dot2_f32_bf16 v77, v222, v43, v40\n"
"v_dot2_f32_bf16 v78, v223, v41, v40\n"
"v_dot2_f32_bf16 v79, v223, v43, v40\n"
"v_cvt_pk_bf16_f32 v220, v72, v73\n"
"v_cvt_pk_bf16_f32 v221, v74, v75\n"
"v_cvt_pk_bf16_f32 v222, v76, v77\n"
"v_cvt_pk_bf16_f32 v223, v78, v79\n"
"s_add_u32 m0, s77, 16384\n"
"s_add_u32 s83, s78, 3245056\n"
"buffer_load_dwordx4 v11, s[44:47], s83 offen offset:3072 lds\n"
"ds_write_b128 v121, v[220:223] offset:12288\n"
"v_dot2_f32_bf16 v64, v228, v41, v40\n"
"v_dot2_f32_bf16 v65, v228, v43, v40\n"
"v_dot2_f32_bf16 v66, v229, v41, v40\n"
"v_dot2_f32_bf16 v67, v229, v43, v40\n"
"v_dot2_f32_bf16 v68, v230, v41, v40\n"
"v_dot2_f32_bf16 v69, v230, v43, v40\n"
"v_dot2_f32_bf16 v70, v231, v41, v40\n"
"v_dot2_f32_bf16 v71, v231, v43, v40\n"
"v_cvt_pk_bf16_f32 v228, v64, v65\n"
"v_cvt_pk_bf16_f32 v229, v66, v67\n"
"v_cvt_pk_bf16_f32 v230, v68, v69\n"
"v_cvt_pk_bf16_f32 v231, v70, v71\n"
"s_add_u32 m0, s77, 32768\n"
"s_add_u32 s82, s78, 3543040\n"
"buffer_load_dwordx4 v11, s[44:47], s82 offen offset:0 lds\n"
"ds_write_b128 v121, v[228:231] offset:16384\n"
"v_dot2_f32_bf16 v72, v236, v41, v40\n"
"v_dot2_f32_bf16 v73, v236, v43, v40\n"
"v_dot2_f32_bf16 v74, v237, v41, v40\n"
"v_dot2_f32_bf16 v75, v237, v43, v40\n"
"v_dot2_f32_bf16 v76, v238, v41, v40\n"
"v_dot2_f32_bf16 v77, v238, v43, v40\n"
"v_dot2_f32_bf16 v78, v239, v41, v40\n"
"v_dot2_f32_bf16 v79, v239, v43, v40\n"
"v_cvt_pk_bf16_f32 v236, v72, v73\n"
"v_cvt_pk_bf16_f32 v237, v74, v75\n"
"v_cvt_pk_bf16_f32 v238, v76, v77\n"
"v_cvt_pk_bf16_f32 v239, v78, v79\n"
"s_add_u32 m0, s77, 32768\n"
"s_add_u32 s83, s78, 3836928\n"
"buffer_load_dwordx4 v11, s[44:47], s83 offen offset:1024 lds\n"
"ds_write_b128 v121, v[236:239] offset:20480\n"
"v_dot2_f32_bf16 v64, v244, v41, v40\n"
"v_dot2_f32_bf16 v65, v244, v43, v40\n"
"v_dot2_f32_bf16 v66, v245, v41, v40\n"
"v_dot2_f32_bf16 v67, v245, v43, v40\n"
"v_dot2_f32_bf16 v68, v246, v41, v40\n"
"v_dot2_f32_bf16 v69, v246, v43, v40\n"
"v_dot2_f32_bf16 v70, v247, v41, v40\n"
"v_dot2_f32_bf16 v71, v247, v43, v40\n"
"v_cvt_pk_bf16_f32 v244, v64, v65\n"
"v_cvt_pk_bf16_f32 v245, v66, v67\n"
"v_cvt_pk_bf16_f32 v246, v68, v69\n"
"v_cvt_pk_bf16_f32 v247, v70, v71\n"
"s_add_u32 m0, s77, 32768\n"
"s_add_u32 s82, s78, 4130816\n"
"buffer_load_dwordx4 v11, s[44:47], s82 offen offset:2048 lds\n"
"ds_write_b128 v121, v[244:247] offset:24576\n"
"v_dot2_f32_bf16 v72, v252, v41, v40\n"
"v_dot2_f32_bf16 v73, v252, v43, v40\n"
"v_dot2_f32_bf16 v74, v253, v41, v40\n"
"v_dot2_f32_bf16 v75, v253, v43, v40\n"
"v_dot2_f32_bf16 v76, v254, v41, v40\n"
"v_dot2_f32_bf16 v77, v254, v43, v40\n"
"v_dot2_f32_bf16 v78, v255, v41, v40\n"
"v_dot2_f32_bf16 v79, v255, v43, v40\n"
"v_cvt_pk_bf16_f32 v252, v72, v73\n"
"v_cvt_pk_bf16_f32 v253, v74, v75\n"
"v_cvt_pk_bf16_f32 v254, v76, v77\n"
"v_cvt_pk_bf16_f32 v255, v78, v79\n"
"s_add_u32 m0, s77, 32768\n"
"s_add_u32 s83, s78, 4424704\n"
"buffer_load_dwordx4 v11, s[44:47], s83 offen offset:3072 lds\n"
"ds_write_b128 v121, v[252:255] offset:28672\n"
"v_dot2_f32_bf16 v64, v100, v41, v40\n"
"v_dot2_f32_bf16 v65, v100, v43, v40\n"
"v_dot2_f32_bf16 v66, v101, v41, v40\n"
"v_dot2_f32_bf16 v67, v101, v43, v40\n"
"v_dot2_f32_bf16 v68, v102, v41, v40\n"
"v_dot2_f32_bf16 v69, v102, v43, v40\n"
"v_dot2_f32_bf16 v70, v103, v41, v40\n"
"v_dot2_f32_bf16 v71, v103, v43, v40\n"
"v_cvt_pk_bf16_f32 v100, v64, v65\n"
"v_cvt_pk_bf16_f32 v101, v66, v67\n"
"v_cvt_pk_bf16_f32 v102, v68, v69\n"
"v_cvt_pk_bf16_f32 v103, v70, v71\n"
"s_add_u32 m0, s77, 49152\n"
"s_add_u32 s82, s78, 4722688\n"
"buffer_load_dwordx4 v11, s[44:47], s82 offen offset:0 lds\n"
"ds_write_b128 v121, v[100:103] offset:32768\n"
"v_dot2_f32_bf16 v72, v108, v41, v40\n"
"v_dot2_f32_bf16 v73, v108, v43, v40\n"
"v_dot2_f32_bf16 v74, v109, v41, v40\n"
"v_dot2_f32_bf16 v75, v109, v43, v40\n"
"v_dot2_f32_bf16 v76, v110, v41, v40\n"
"v_dot2_f32_bf16 v77, v110, v43, v40\n"
"v_dot2_f32_bf16 v78, v111, v41, v40\n"
"v_dot2_f32_bf16 v79, v111, v43, v40\n"
"v_cvt_pk_bf16_f32 v108, v72, v73\n"
"v_cvt_pk_bf16_f32 v109, v74, v75\n"
"v_cvt_pk_bf16_f32 v110, v76, v77\n"
"v_cvt_pk_bf16_f32 v111, v78, v79\n"
"s_add_u32 m0, s77, 49152\n"
"s_add_u32 s83, s78, 5016576\n"
"buffer_load_dwordx4 v11, s[44:47], s83 offen offset:1024 lds\n"
"ds_write_b128 v121, v[108:111] offset:36864\n"
"s_add_u32 m0, s77, 49152\n"
"s_add_u32 s82, s78, 2048\n"
"buffer_load_dwordx4 v11, s[48:51], s82 offen offset:2048 lds\n"
"s_add_u32 m0, s77, 49152\n"
"s_add_u32 s83, s78, 295936\n"
"buffer_load_dwordx4 v11, s[48:51], s83 offen offset:3072 lds\n"
"s_waitcnt vmcnt(16)\n"
"buffer_store_dwordx2 v[16:17], v30, s[64:67], s16 offen sc1\n"
"v_lshrrev_b32 v13, 6, v9\n"
"v_min_u32 v13, 8, v13\n"
"v_mul_u32_u24 v13, 9216, v13\n"
"v_bfe_u32 v19, v9, 4, 2\n"
"v_cmp_eq_u32 vcc, 3, v19\n"
"v_lshl_add_u32 v19, v19, 7, s78\n"
"v_add_u32 v19, 128, v19\n"
"v_add_u32 v13, v13, v19\n"
"v_subrev_u32 v19, s79, v19\n"
"v_cndmask_b32 v19, 0, v19, vcc\n"
"v_sub_u32 v13, v13, v19\n"
"s_lshl_b32 s35, s76, 2\n"
"s_add_u32 s35, s35, s72\n"
"s_mul_i32 s35, s35, 82944\n"
"s_add_u32 s35, s35, 4096\n"
"buffer_load_dword a218, v13, s[44:47], s35 offen offset:0\n"
"s_waitcnt lgkmcnt(0)\n"
"s_barrier\n"
"ds_read_b64_tr_b16 a[160:161], v0 offset:16384\n"
"ds_read_b64_tr_b16 a[162:163], v0 offset:24576\n"
"ds_read_b64_tr_b16 a[164:165], v0 offset:17408\n"
"ds_read_b64_tr_b16 a[166:167], v0 offset:25600\n"
"ds_read_b64_tr_b16 a[168:169], v6 offset:16384\n"
"ds_read_b64_tr_b16 a[170:171], v6 offset:24576\n"
"ds_read_b64_tr_b16 a[172:173], v6 offset:17408\n"
"ds_read_b64_tr_b16 a[174:175], v6 offset:25600\n"
"ds_read_b128 a[4:7], v122 offset:0\n"
"ds_read_b128 a[12:15], v122 offset:4096\n"
"s_waitcnt lgkmcnt(2)\n"
"v_mfma_f32_16x16x32_bf16 v[32:35], a[160:163], v[128:131], 0\n"
"ds_read_b128 a[20:23], v122 offset:8192\n"
"ds_read_b64_tr_b16 a[176:177], v0 offset:18432\n"
"v_mfma_f32_16x16x32_bf16 v[32:35], a[164:167], v[136:139], v[32:35]\n"
"s_add_u32 s18, s9, 0\n"
"ds_read_b128 a[28:31], v122 offset:12288\n"
"ds_read_b64_tr_b16 a[178:179], v0 offset:26624\n"
"v_mfma_f32_16x16x32_bf16 v[36:39], a[160:163], v[132:135], 0\n"
"s_lshl_b32 s83, s72, 10\n"
"ds_read_b128 a[36:39], v122 offset:16384\n"
"ds_read_b128 a[44:47], v122 offset:20480\n"
"v_mfma_f32_16x16x32_bf16 v[36:39], a[164:167], v[140:143], v[36:39]\n"
"ds_read_b128 a[52:55], v122 offset:24576\n"
"ds_read_b64_tr_b16 a[180:181], v0 offset:19456\n"
"v_mfma_f32_16x16x32_bf16 v[40:43], a[160:163], a[0:3], 0\n"
"ds_read_b128 a[60:63], v122 offset:28672\n"
"ds_read_b64_tr_b16 a[182:183], v0 offset:27648\n"
"v_mfma_f32_16x16x32_bf16 v[40:43], a[164:167], a[8:11], v[40:43]\n"
"ds_read_b128 a[68:71], v123 offset:0\n"
"ds_read_b128 a[76:79], v123 offset:4096\n"
"s_waitcnt lgkmcnt(13)\n"
"v_mfma_f32_16x16x32_bf16 v[44:47], a[160:163], a[4:7], 0\n"
"ds_read_b128 a[84:87], v123 offset:8192\n"
"ds_read_b64_tr_b16 a[184:185], v6 offset:18432\n"
"s_waitcnt lgkmcnt(14)\n"
"v_mfma_f32_16x16x32_bf16 v[44:47], a[164:167], a[12:15], v[44:47]\n"
"ds_read_b128 a[92:95], v123 offset:12288\n"
"ds_read_b64_tr_b16 a[186:187], v6 offset:26624\n"
"v_mfma_f32_16x16x32_bf16 v[48:51], a[168:171], v[128:131], 0\n"
"ds_read_b128 a[100:103], v123 offset:16384\n"
"ds_read_b128 a[108:111], v123 offset:20480\n"
"v_mfma_f32_16x16x32_bf16 v[48:51], a[172:175], v[136:139], v[48:51]\n"
"ds_read_b128 a[116:119], v123 offset:24576\n"
"ds_read_b64_tr_b16 a[188:189], v6 offset:19456\n"
"v_mfma_f32_16x16x32_bf16 v[52:55], a[168:171], v[132:135], 0\n"
"ds_read_b128 a[124:127], v123 offset:28672\n"
"ds_read_b64_tr_b16 a[190:191], v6 offset:27648\n"
"v_mfma_f32_16x16x32_bf16 v[52:55], a[172:175], v[140:143], v[52:55]\n"
"ds_read_b128 a[132:135], v123 offset:32768\n"
"ds_read_b128 a[140:143], v123 offset:36864\n"
"v_mfma_f32_16x16x32_bf16 v[56:59], a[168:171], a[0:3], 0\n"
"ds_read_b128 a[148:151], v121 offset:32768\n"
"ds_read_b128 a[156:159], v121 offset:36864\n"
"v_mfma_f32_16x16x32_bf16 v[56:59], a[172:175], a[8:11], v[56:59]\n"
"v_lshrrev_b32 v0, 1, v9\n"
"v_mfma_f32_16x16x32_bf16 v[60:63], a[168:171], a[4:7], 0\n"
"v_mfma_f32_16x16x32_bf16 v[60:63], a[172:175], a[12:15], v[60:63]\n"
"s_waitcnt lgkmcnt(4)\n"
"v_mfma_f32_16x16x32_bf16 v[32:35], a[176:179], v[144:147], v[32:35]\n"
"s_waitcnt vmcnt(14)\n"
"s_barrier\n"
"ds_read_b64_tr_b16 a[160:161], v1 offset:0\n"
"v_mfma_f32_16x16x32_bf16 v[32:35], a[180:183], v[152:155], v[32:35]\n"
"ds_read_b64_tr_b16 a[162:163], v1 offset:8192\n"
"v_mfma_f32_16x16x32_bf16 v[36:39], a[176:179], v[148:151], v[36:39]\n"
"v_mfma_f32_16x16x32_bf16 v[36:39], a[180:183], v[156:159], v[36:39]\n"
"ds_read_b64_tr_b16 a[164:165], v1 offset:1024\n"
"v_mfma_f32_16x16x32_bf16 v[40:43], a[176:179], a[16:19], v[40:43]\n"
"ds_read_b64_tr_b16 a[166:167], v1 offset:9216\n"
"v_mfma_f32_16x16x32_bf16 v[40:43], a[180:183], a[24:27], v[40:43]\n"
"v_mfma_f32_16x16x32_bf16 v[44:47], a[176:179], a[20:23], v[44:47]\n"
"ds_read_b64_tr_b16 a[168:169], v2 offset:0\n"
"v_mfma_f32_16x16x32_bf16 v[44:47], a[180:183], a[28:31], v[44:47]\n"
"ds_read_b64_tr_b16 a[170:171], v2 offset:8192\n"
"v_mfma_f32_16x16x32_bf16 v[48:51], a[184:187], v[144:147], v[48:51]\n"
"v_mfma_f32_16x16x32_bf16 v[48:51], a[188:191], v[152:155], v[48:51]\n"
"ds_read_b64_tr_b16 a[172:173], v2 offset:1024\n"
"v_mfma_f32_16x16x32_bf16 v[52:55], a[184:187], v[148:151], v[52:55]\n"
"ds_read_b64_tr_b16 a[174:175], v2 offset:9216\n"
"v_mfma_f32_16x16x32_bf16 v[52:55], a[188:191], v[156:159], v[52:55]\n"
"v_mfma_f32_16x16x32_bf16 v[56:59], a[184:187], a[16:19], v[56:59]\n"
"v_mfma_f32_16x16x32_bf16 v[56:59], a[188:191], a[24:27], v[56:59]\n"
"v_mfma_f32_16x16x32_bf16 v[60:63], a[184:187], a[20:23], v[60:63]\n"
"v_mfma_f32_16x16x32_bf16 v[60:63], a[188:191], a[28:31], v[60:63]\n"
"s_waitcnt lgkmcnt(0)\n"
"v_mfma_f32_16x16x32_bf16 v[32:35], a[160:163], v[160:163], v[32:35]\n"
"ds_read_b64_tr_b16 a[176:177], v1 offset:2048\n"
"v_bfe_u32 v16, v9, 5, 3\n"
"v_mfma_f32_16x16x32_bf16 v[32:35], a[164:167], v[168:171], v[32:35]\n"
"ds_read_b64_tr_b16 a[178:179], v1 offset:10240\n"
"v_bfe_u32 v17, v9, 4, 1\n"
"v_lshlrev_b32 v17, 4, v17\n"
"v_mfma_f32_16x16x32_bf16 v[36:39], a[160:163], v[164:167], v[36:39]\n"
"v_mov_b32 v18, 0x3f80\n"
"v_lshlrev_b32 v17, v17, v18\n"
"v_mfma_f32_16x16x32_bf16 v[36:39], a[164:167], v[172:175], v[36:39]\n"
"ds_read_b64_tr_b16 a[180:181], v1 offset:3072\n"
"v_cmp_eq_u32 vcc, 0, v16\n"
"v_mfma_f32_16x16x32_bf16 v[40:43], a[160:163], a[32:35], v[40:43]\n"
"ds_read_b64_tr_b16 a[182:183], v1 offset:11264\n"
"v_cndmask_b32 v18, 0, v17, vcc\n"
"v_mfma_f32_16x16x32_bf16 v[40:43], a[164:167], a[40:43], v[40:43]\n"
"v_accvgpr_write_b32 a248, v18\n"
"v_cmp_eq_u32 vcc, 1, v16\n"
"v_mfma_f32_16x16x32_bf16 v[44:47], a[160:163], a[36:39], v[44:47]\n"
"ds_read_b64_tr_b16 a[184:185], v2 offset:2048\n"
"v_cndmask_b32 v18, 0, v17, vcc\n"
"v_mfma_f32_16x16x32_bf16 v[44:47], a[164:167], a[44:47], v[44:47]\n"
"ds_read_b64_tr_b16 a[186:187], v2 offset:10240\n"
"v_accvgpr_write_b32 a249, v18\n"
"v_mfma_f32_16x16x32_bf16 v[48:51], a[168:171], v[160:163], v[48:51]\n"
"v_cmp_eq_u32 vcc, 2, v16\n"
"s_nop 1\n"
"v_cndmask_b32 v18, 0, v17, vcc\n"
"v_mfma_f32_16x16x32_bf16 v[48:51], a[172:175], v[168:171], v[48:51]\n"
"ds_read_b64_tr_b16 a[188:189], v2 offset:3072\n"
"v_accvgpr_write_b32 a250, v18\n"
"v_mfma_f32_16x16x32_bf16 v[52:55], a[168:171], v[164:167], v[52:55]\n"
"ds_read_b64_tr_b16 a[190:191], v2 offset:11264\n"
"v_cmp_eq_u32 vcc, 3, v16\n"
"v_mfma_f32_16x16x32_bf16 v[52:55], a[172:175], v[172:175], v[52:55]\n"
"s_nop 0\n"
"v_cndmask_b32 v18, 0, v17, vcc\n"
"v_xor_b32 v6, 0x800, v5\n"
"v_mfma_f32_16x16x32_bf16 v[56:59], a[168:171], a[32:35], v[56:59]\n"
"v_accvgpr_write_b32 a251, v18\n"
"v_cmp_eq_u32 vcc, 4, v16\n"
"v_mfma_f32_16x16x32_bf16 v[56:59], a[172:175], a[40:43], v[56:59]\n"
"s_nop 0\n"
"v_cndmask_b32 v18, 0, v17, vcc\n"
"v_accvgpr_write_b32 a252, v18\n"
"v_mfma_f32_16x16x32_bf16 v[60:63], a[168:171], a[36:39], v[60:63]\n"
"v_cmp_eq_u32 vcc, 5, v16\n"
"s_nop 1\n"
"v_cndmask_b32 v18, 0, v17, vcc\n"
"v_mfma_f32_16x16x32_bf16 v[60:63], a[172:175], a[44:47], v[60:63]\n"
"s_waitcnt lgkmcnt(0)\n"
"v_accvgpr_write_b32 a253, v18\n"
"v_cmp_eq_u32 vcc, 6, v16\n"
"v_mfma_f32_16x16x32_bf16 v[32:35], a[176:179], v[176:179], v[32:35]\n"
"s_waitcnt vmcnt(10)\n"
"s_barrier\n"
"ds_read_b64_tr_b16 a[160:161], v1 offset:16384\n"
"v_mfma_f32_16x16x32_bf16 v[32:35], a[180:183], v[184:187], v[32:35]\n"
"ds_read_b64_tr_b16 a[162:163], v1 offset:24576\n"
"v_cndmask_b32 v18, 0, v17, vcc\n"
"v_mfma_f32_16x16x32_bf16 v[36:39], a[176:179], v[180:183], v[36:39]\n"
"v_accvgpr_write_b32 a254, v18\n"
"v_cmp_eq_u32 vcc, 7, v16\n"
"v_mfma_f32_16x16x32_bf16 v[36:39], a[180:183], v[188:191], v[36:39]\n"
"ds_read_b64_tr_b16 a[164:165], v1 offset:17408\n"
"v_cndmask_b32 v18, 0, v17, vcc\n"
"v_mfma_f32_16x16x32_bf16 v[40:43], a[176:179], a[48:51], v[40:43]\n"
"ds_read_b64_tr_b16 a[166:167], v1 offset:25600\n"
"v_accvgpr_write_b32 a255, v18\n"
"v_mfma_f32_16x16x32_bf16 v[40:43], a[180:183], a[56:59], v[40:43]\n"
"v_mfma_f32_16x16x32_bf16 v[44:47], a[176:179], a[52:55], v[44:47]\n"
"ds_read_b64_tr_b16 a[168:169], v2 offset:16384\n"
"v_mfma_f32_16x16x32_bf16 v[44:47], a[180:183], a[60:63], v[44:47]\n"
"ds_read_b64_tr_b16 a[170:171], v2 offset:24576\n"
"v_mfma_f32_16x16x32_bf16 v[48:51], a[184:187], v[176:179], v[48:51]\n"
"v_mfma_f32_16x16x32_bf16 v[48:51], a[188:191], v[184:187], v[48:51]\n"
"ds_read_b64_tr_b16 a[172:173], v2 offset:17408\n"
"v_mfma_f32_16x16x32_bf16 v[52:55], a[184:187], v[180:183], v[52:55]\n"
"ds_read_b64_tr_b16 a[174:175], v2 offset:25600\n"
"v_mfma_f32_16x16x32_bf16 v[52:55], a[188:191], v[188:191], v[52:55]\n"
"v_mfma_f32_16x16x32_bf16 v[56:59], a[184:187], a[48:51], v[56:59]\n"
"v_mfma_f32_16x16x32_bf16 v[56:59], a[188:191], a[56:59], v[56:59]\n"
"v_mfma_f32_16x16x32_bf16 v[60:63], a[184:187], a[52:55], v[60:63]\n"
"v_mfma_f32_16x16x32_bf16 v[60:63], a[188:191], a[60:63], v[60:63]\n"
"s_waitcnt lgkmcnt(0)\n"
"v_mfma_f32_16x16x32_bf16 v[32:35], a[160:163], v[192:195], v[32:35]\n"
"s_add_u32 m0, s77, 65536\n"
"ds_read_b64_tr_b16 a[176:177], v1 offset:18432\n"
"v_mfma_f32_16x16x32_bf16 v[32:35], a[164:167], v[200:203], v[32:35]\n"
"s_add_u32 s82, s78, 593920\n"
"ds_read_b64_tr_b16 a[178:179], v1 offset:26624\n"
"v_mfma_f32_16x16x32_bf16 v[36:39], a[160:163], v[196:199], v[36:39]\n"
"buffer_load_dwordx4 v11, s[48:51], s82 offen offset:0 lds\n"
"v_mfma_f32_16x16x32_bf16 v[36:39], a[164:167], v[204:207], v[36:39]\n"
"ds_read_b64_tr_b16 a[180:181], v1 offset:19456\n"
"v_mfma_f32_16x16x32_bf16 v[40:43], a[160:163], a[64:67], v[40:43]\n"
"ds_read_b64_tr_b16 a[182:183], v1 offset:27648\n"
"v_mfma_f32_16x16x32_bf16 v[40:43], a[164:167], a[72:75], v[40:43]\n"
"v_mfma_f32_16x16x32_bf16 v[44:47], a[160:163], a[68:71], v[44:47]\n"
"ds_read_b64_tr_b16 a[184:185], v2 offset:18432\n"
"v_mfma_f32_16x16x32_bf16 v[44:47], a[164:167], a[76:79], v[44:47]\n"
"s_add_u32 s82, s82, s13\n"
"ds_read_b64_tr_b16 a[186:187], v2 offset:26624\n"
"v_mfma_f32_16x16x32_bf16 v[48:51], a[168:171], v[192:195], v[48:51]\n"
"buffer_load_dwordx4 v11, s[48:51], s82 offen offset:1024 lds\n"
"v_mfma_f32_16x16x32_bf16 v[48:51], a[172:175], v[200:203], v[48:51]\n"
"ds_read_b64_tr_b16 a[188:189], v2 offset:19456\n"
"v_mfma_f32_16x16x32_bf16 v[52:55], a[168:171], v[196:199], v[52:55]\n"
"ds_read_b64_tr_b16 a[190:191], v2 offset:27648\n"
"v_mfma_f32_16x16x32_bf16 v[52:55], a[172:175], v[204:207], v[52:55]\n"
"s_add_u32 s82, s82, s13\n"
"v_mfma_f32_16x16x32_bf16 v[56:59], a[168:171], a[64:67], v[56:59]\n"
"buffer_load_dwordx4 v11, s[48:51], s82 offen offset:2048 lds\n"
"v_mfma_f32_16x16x32_bf16 v[56:59], a[172:175], a[72:75], v[56:59]\n"
"v_mfma_f32_16x16x32_bf16 v[60:63], a[168:171], a[68:71], v[60:63]\n"
"s_add_u32 s82, s82, s13\n"
"v_mfma_f32_16x16x32_bf16 v[60:63], a[172:175], a[76:79], v[60:63]\n"
"s_waitcnt lgkmcnt(0)\n"
"buffer_load_dwordx4 v11, s[48:51], s82 offen offset:3072 lds\n"
"v_mfma_f32_16x16x32_bf16 v[32:35], a[176:179], v[208:211], v[32:35]\n"
"s_add_u32 m0, s77, 0\n"
"s_waitcnt vmcnt(10)\n"
"s_barrier\n"
"ds_read_b64_tr_b16 a[160:161], v1 offset:32768\n"
"v_mfma_f32_16x16x32_bf16 v[32:35], a[180:183], v[216:219], v[32:35]\n"
"s_add_u32 s82, s82, s15\n"
"ds_read_b64_tr_b16 a[162:163], v1 offset:40960\n"
"v_mfma_f32_16x16x32_bf16 v[36:39], a[176:179], v[212:215], v[36:39]\n"
"buffer_load_dwordx4 v11, s[48:51], s82 offen offset:0 lds\n"
"v_mfma_f32_16x16x32_bf16 v[36:39], a[180:183], v[220:223], v[36:39]\n"
"ds_read_b64_tr_b16 a[164:165], v1 offset:33792\n"
"v_mfma_f32_16x16x32_bf16 v[40:43], a[176:179], a[80:83], v[40:43]\n"
"ds_read_b64_tr_b16 a[166:167], v1 offset:41984\n"
"v_mfma_f32_16x16x32_bf16 v[40:43], a[180:183], a[88:91], v[40:43]\n"
"v_mfma_f32_16x16x32_bf16 v[44:47], a[176:179], a[84:87], v[44:47]\n"
"ds_read_b64_tr_b16 a[168:169], v2 offset:32768\n"
"v_mfma_f32_16x16x32_bf16 v[44:47], a[180:183], a[92:95], v[44:47]\n"
"s_add_u32 s82, s82, s13\n"
"ds_read_b64_tr_b16 a[170:171], v2 offset:40960\n"
"v_mfma_f32_16x16x32_bf16 v[48:51], a[184:187], v[208:211], v[48:51]\n"
"buffer_load_dwordx4 v11, s[48:51], s82 offen offset:1024 lds\n"
"v_mfma_f32_16x16x32_bf16 v[48:51], a[188:191], v[216:219], v[48:51]\n"
"ds_read_b64_tr_b16 a[172:173], v2 offset:33792\n"
"v_mfma_f32_16x16x32_bf16 v[52:55], a[184:187], v[212:215], v[52:55]\n"
"ds_read_b64_tr_b16 a[174:175], v2 offset:41984\n"
"v_mfma_f32_16x16x32_bf16 v[52:55], a[188:191], v[220:223], v[52:55]\n"
"s_add_u32 s82, s82, s13\n"
"v_mfma_f32_16x16x32_bf16 v[56:59], a[184:187], a[80:83], v[56:59]\n"
"buffer_load_dwordx4 v11, s[48:51], s82 offen offset:2048 lds\n"
"v_mfma_f32_16x16x32_bf16 v[56:59], a[188:191], a[88:91], v[56:59]\n"
"v_mfma_f32_16x16x32_bf16 v[60:63], a[184:187], a[84:87], v[60:63]\n"
"s_add_u32 s82, s82, s13\n"
"v_mfma_f32_16x16x32_bf16 v[60:63], a[188:191], a[92:95], v[60:63]\n"
"s_waitcnt lgkmcnt(0)\n"
"buffer_load_dwordx4 v11, s[48:51], s82 offen offset:3072 lds\n"
"v_mfma_f32_16x16x32_bf16 v[32:35], a[160:163], v[224:227], v[32:35]\n"
"s_addk_i32 m0, 16384\n"
"ds_read_b64_tr_b16 a[176:177], v1 offset:34816\n"
"v_mfma_f32_16x16x32_bf16 v[32:35], a[164:167], v[232:235], v[32:35]\n"
"s_add_u32 s82, s82, s15\n"
"ds_read_b64_tr_b16 a[178:179], v1 offset:43008\n"
"v_mfma_f32_16x16x32_bf16 v[36:39], a[160:163], v[228:231], v[36:39]\n"
"buffer_load_dwordx4 v11, s[48:51], s82 offen offset:0 lds\n"
"v_mfma_f32_16x16x32_bf16 v[36:39], a[164:167], v[236:239], v[36:39]\n"
"ds_read_b64_tr_b16 a[180:181], v1 offset:35840\n"
"v_mfma_f32_16x16x32_bf16 v[40:43], a[160:163], a[96:99], v[40:43]\n"
"ds_read_b64_tr_b16 a[182:183], v1 offset:44032\n"
"v_mfma_f32_16x16x32_bf16 v[40:43], a[164:167], a[104:107], v[40:43]\n"
"v_mfma_f32_16x16x32_bf16 v[44:47], a[160:163], a[100:103], v[44:47]\n"
"ds_read_b64_tr_b16 a[184:185], v2 offset:34816\n"
"v_mfma_f32_16x16x32_bf16 v[44:47], a[164:167], a[108:111], v[44:47]\n"
"s_add_u32 s82, s82, s13\n"
"ds_read_b64_tr_b16 a[186:187], v2 offset:43008\n"
"v_mfma_f32_16x16x32_bf16 v[48:51], a[168:171], v[224:227], v[48:51]\n"
"buffer_load_dwordx4 v11, s[48:51], s82 offen offset:1024 lds\n"
"v_mfma_f32_16x16x32_bf16 v[48:51], a[172:175], v[232:235], v[48:51]\n"
"ds_read_b64_tr_b16 a[188:189], v2 offset:35840\n"
"v_mfma_f32_16x16x32_bf16 v[52:55], a[168:171], v[228:231], v[52:55]\n"
"ds_read_b64_tr_b16 a[190:191], v2 offset:44032\n"
"v_mfma_f32_16x16x32_bf16 v[52:55], a[172:175], v[236:239], v[52:55]\n"
"s_add_u32 s82, s82, s13\n"
"v_mfma_f32_16x16x32_bf16 v[56:59], a[168:171], a[96:99], v[56:59]\n"
"buffer_load_dwordx4 v11, s[48:51], s82 offen offset:2048 lds\n"
"v_mfma_f32_16x16x32_bf16 v[56:59], a[172:175], a[104:107], v[56:59]\n"
"v_mfma_f32_16x16x32_bf16 v[60:63], a[168:171], a[100:103], v[60:63]\n"
"s_add_u32 s82, s82, s13\n"
"v_mfma_f32_16x16x32_bf16 v[60:63], a[172:175], a[108:111], v[60:63]\n"
"s_waitcnt lgkmcnt(0)\n"
"buffer_load_dwordx4 v11, s[48:51], s82 offen offset:3072 lds\n"
"v_mfma_f32_16x16x32_bf16 v[32:35], a[176:179], v[240:243], v[32:35]\n"
"s_add_u32 m0, s77, 81920\n"
"s_waitcnt vmcnt(14)\n"
"s_barrier\n"
"ds_read_b64_tr_b16 a[160:161], v1 offset:49152\n"
"v_mfma_f32_16x16x32_bf16 v[32:35], a[180:183], v[248:251], v[32:35]\n"
"s_add_u32 s82, s82, s15\n"
"ds_read_b64_tr_b16 a[162:163], v1 offset:57344\n"
"v_mfma_f32_16x16x32_bf16 v[36:39], a[176:179], v[244:247], v[36:39]\n"
"buffer_load_dwordx4 v11, s[48:51], s82 offen offset:0 lds\n"
"v_mfma_f32_16x16x32_bf16 v[36:39], a[180:183], v[252:255], v[36:39]\n"
"ds_read_b64_tr_b16 a[164:165], v1 offset:50176\n"
"v_mfma_f32_16x16x32_bf16 v[40:43], a[176:179], a[112:115], v[40:43]\n"
"ds_read_b64_tr_b16 a[166:167], v1 offset:58368\n"
"v_mfma_f32_16x16x32_bf16 v[40:43], a[180:183], a[120:123], v[40:43]\n"
"v_mfma_f32_16x16x32_bf16 v[44:47], a[176:179], a[116:119], v[44:47]\n"
"ds_read_b64_tr_b16 a[168:169], v2 offset:49152\n"
"v_mfma_f32_16x16x32_bf16 v[44:47], a[180:183], a[124:127], v[44:47]\n"
"s_add_u32 s82, s82, s13\n"
"ds_read_b64_tr_b16 a[170:171], v2 offset:57344\n"
"v_mfma_f32_16x16x32_bf16 v[48:51], a[184:187], v[240:243], v[48:51]\n"
"buffer_load_dwordx4 v11, s[48:51], s82 offen offset:1024 lds\n"
"v_mfma_f32_16x16x32_bf16 v[48:51], a[188:191], v[248:251], v[48:51]\n"
"ds_read_b64_tr_b16 a[172:173], v2 offset:50176\n"
"v_mfma_f32_16x16x32_bf16 v[52:55], a[184:187], v[244:247], v[52:55]\n"
"ds_read_b64_tr_b16 a[174:175], v2 offset:58368\n"
"v_mfma_f32_16x16x32_bf16 v[52:55], a[188:191], v[252:255], v[52:55]\n"
"s_add_u32 s82, s82, s13\n"
"v_mfma_f32_16x16x32_bf16 v[56:59], a[184:187], a[112:115], v[56:59]\n"
"buffer_load_dwordx4 v11, s[48:51], s82 offen offset:2048 lds\n"
"v_mfma_f32_16x16x32_bf16 v[56:59], a[188:191], a[120:123], v[56:59]\n"
"v_mfma_f32_16x16x32_bf16 v[60:63], a[184:187], a[116:119], v[60:63]\n"
"s_add_u32 s82, s82, s13\n"
"v_mfma_f32_16x16x32_bf16 v[60:63], a[188:191], a[124:127], v[60:63]\n"
"s_waitcnt lgkmcnt(0)\n"
"buffer_load_dwordx4 v11, s[48:51], s82 offen offset:3072 lds\n"
"v_mfma_f32_16x16x32_bf16 v[32:35], a[160:163], a[144:147], v[32:35]\n"
"ds_read_b64_tr_b16 a[176:177], v1 offset:51200\n"
"v_mfma_f32_16x16x32_bf16 v[32:35], a[164:167], a[152:155], v[32:35]\n"
"ds_read_b64_tr_b16 a[178:179], v1 offset:59392\n"
"v_mfma_f32_16x16x32_bf16 v[36:39], a[160:163], a[148:151], v[36:39]\n"
"buffer_load_dword a219, v13, s[48:51], s35 offen offset:0\n"
"v_mfma_f32_16x16x32_bf16 v[36:39], a[164:167], a[156:159], v[36:39]\n"
"ds_read_b64_tr_b16 a[180:181], v1 offset:52224\n"
"v_mfma_f32_16x16x32_bf16 v[40:43], a[160:163], a[128:131], v[40:43]\n"
"ds_read_b64_tr_b16 a[182:183], v1 offset:60416\n"
"v_mfma_f32_16x16x32_bf16 v[40:43], a[164:167], a[136:139], v[40:43]\n"
"v_exp_f32 v16, v32\n"
"v_mfma_f32_16x16x32_bf16 v[44:47], a[160:163], a[132:135], v[44:47]\n"
"ds_read_b64_tr_b16 a[184:185], v2 offset:51200\n"
"v_add_f32 v16, 1.0, v16\n"
"v_mfma_f32_16x16x32_bf16 v[44:47], a[164:167], a[140:143], v[44:47]\n"
"ds_read_b64_tr_b16 a[186:187], v2 offset:59392\n"
"v_mfma_f32_16x16x32_bf16 v[48:51], a[168:171], a[144:147], v[48:51]\n"
"v_exp_f32 v17, v33\n"
"v_mfma_f32_16x16x32_bf16 v[48:51], a[172:175], a[152:155], v[48:51]\n"
"ds_read_b64_tr_b16 a[188:189], v2 offset:52224\n"
"v_add_f32 v17, 1.0, v17\n"
"v_mfma_f32_16x16x32_bf16 v[52:55], a[168:171], a[148:151], v[52:55]\n"
"ds_read_b64_tr_b16 a[190:191], v2 offset:60416\n"
"v_mfma_f32_16x16x32_bf16 v[52:55], a[172:175], a[156:159], v[52:55]\n"
"v_rcp_f32 v16, v16\n"
"v_mfma_f32_16x16x32_bf16 v[56:59], a[168:171], a[128:131], v[56:59]\n"
"v_exp_f32 v18, v34\n"
"v_mfma_f32_16x16x32_bf16 v[56:59], a[172:175], a[136:139], v[56:59]\n"
"v_mul_f32 v32, v32, v16\n"
"v_add_f32 v18, 1.0, v18\n"
"v_mfma_f32_16x16x32_bf16 v[60:63], a[168:171], a[132:135], v[60:63]\n"
"v_rcp_f32 v17, v17\n"
"v_mfma_f32_16x16x32_bf16 v[60:63], a[172:175], a[140:143], v[60:63]\n"
"s_waitcnt lgkmcnt(0)\n"
"v_exp_f32 v19, v35\n"
"v_mfma_f32_16x16x32_bf16 v[64:67], a[176:179], v[128:131], 0\n"
"s_waitcnt vmcnt(13)\n"
"s_barrier\n"
"ds_read_b64_tr_b16 a[160:161], v3 offset:0\n"
"v_mfma_f32_16x16x32_bf16 v[64:67], a[180:183], v[136:139], v[64:67]\n"
"ds_read_b64_tr_b16 a[162:163], v3 offset:8192\n"
"v_mul_f32 v33, v33, v17\n"
"v_mfma_f32_16x16x32_bf16 v[68:71], a[176:179], v[132:135], 0\n"
"v_rcp_f32 v18, v18\n"
"v_mfma_f32_16x16x32_bf16 v[68:71], a[180:183], v[140:143], v[68:71]\n"
"ds_read_b64_tr_b16 a[164:165], v3 offset:1024\n"
"v_add_f32 v19, 1.0, v19\n"
"v_mfma_f32_16x16x32_bf16 v[72:75], a[176:179], a[0:3], 0\n"
"ds_read_b64_tr_b16 a[166:167], v3 offset:9216\n"
"v_mul_f32 v34, v34, v18\n"
"v_mfma_f32_16x16x32_bf16 v[72:75], a[180:183], a[8:11], v[72:75]\n"
"v_exp_f32 v16, v48\n"
"v_mfma_f32_16x16x32_bf16 v[76:79], a[176:179], a[4:7], 0\n"
"ds_read_b64_tr_b16 a[168:169], v4 offset:0\n"
"v_add_f32 v16, 1.0, v16\n"
"v_mfma_f32_16x16x32_bf16 v[76:79], a[180:183], a[12:15], v[76:79]\n"
"ds_read_b64_tr_b16 a[170:171], v4 offset:8192\n"
"v_mfma_f32_16x16x32_bf16 v[80:83], a[184:187], v[128:131], 0\n"
"v_rcp_f32 v19, v19\n"
"v_mfma_f32_16x16x32_bf16 v[80:83], a[188:191], v[136:139], v[80:83]\n"
"ds_read_b64_tr_b16 a[172:173], v4 offset:1024\n"
"v_mul_f32 v35, v35, v19\n"
"v_mfma_f32_16x16x32_bf16 v[84:87], a[184:187], v[132:135], 0\n"
"ds_read_b64_tr_b16 a[174:175], v4 offset:9216\n"
"v_mfma_f32_16x16x32_bf16 v[84:87], a[188:191], v[140:143], v[84:87]\n"
"v_exp_f32 v17, v49\n"
"v_mfma_f32_16x16x32_bf16 v[88:91], a[184:187], a[0:3], 0\n"
"v_rcp_f32 v16, v16\n"
"v_mfma_f32_16x16x32_bf16 v[88:91], a[188:191], a[8:11], v[88:91]\n"
"v_add_f32 v17, 1.0, v17\n"
"v_mul_f32 v48, v48, v16\n"
"v_mfma_f32_16x16x32_bf16 v[92:95], a[184:187], a[4:7], 0\n"
"v_exp_f32 v18, v50\n"
"v_mfma_f32_16x16x32_bf16 v[92:95], a[188:191], a[12:15], v[92:95]\n"
"s_waitcnt lgkmcnt(0)\n"
"v_rcp_f32 v17, v17\n"
"v_mfma_f32_16x16x32_bf16 v[64:67], a[160:163], v[144:147], v[64:67]\n"
"ds_read_b64_tr_b16 a[176:177], v3 offset:2048\n"
"v_add_f32 v18, 1.0, v18\n"
"v_mfma_f32_16x16x32_bf16 v[64:67], a[164:167], v[152:155], v[64:67]\n"
"ds_read_b64_tr_b16 a[178:179], v3 offset:10240\n"
"v_mul_f32 v49, v49, v17\n"
"v_mfma_f32_16x16x32_bf16 v[68:71], a[160:163], v[148:151], v[68:71]\n"
"v_exp_f32 v19, v51\n"
"v_mfma_f32_16x16x32_bf16 v[68:71], a[164:167], v[156:159], v[68:71]\n"
"ds_read_b64_tr_b16 a[180:181], v3 offset:3072\n"
"v_add_f32 v19, 1.0, v19\n"
"v_mfma_f32_16x16x32_bf16 v[72:75], a[160:163], a[16:19], v[72:75]\n"
"ds_read_b64_tr_b16 a[182:183], v3 offset:11264\n"
"v_mfma_f32_16x16x32_bf16 v[72:75], a[164:167], a[24:27], v[72:75]\n"
"v_rcp_f32 v18, v18\n"
"v_mfma_f32_16x16x32_bf16 v[76:79], a[160:163], a[20:23], v[76:79]\n"
"ds_read_b64_tr_b16 a[184:185], v4 offset:2048\n"
"v_mul_f32 v50, v50, v18\n"
"v_mfma_f32_16x16x32_bf16 v[76:79], a[164:167], a[28:31], v[76:79]\n"
"ds_read_b64_tr_b16 a[186:187], v4 offset:10240\n"
"v_mfma_f32_16x16x32_bf16 v[80:83], a[168:171], v[144:147], v[80:83]\n"
"v_exp_f32 v16, v36\n"
"v_mfma_f32_16x16x32_bf16 v[80:83], a[172:175], v[152:155], v[80:83]\n"
"ds_read_b64_tr_b16 a[188:189], v4 offset:3072\n"
"v_add_f32 v16, 1.0, v16\n"
"v_mfma_f32_16x16x32_bf16 v[84:87], a[168:171], v[148:151], v[84:87]\n"
"ds_read_b64_tr_b16 a[190:191], v4 offset:11264\n"
"v_mfma_f32_16x16x32_bf16 v[84:87], a[172:175], v[156:159], v[84:87]\n"
"v_rcp_f32 v19, v19\n"
"v_mfma_f32_16x16x32_bf16 v[88:91], a[168:171], a[16:19], v[88:91]\n"
"v_exp_f32 v17, v37\n"
"v_mfma_f32_16x16x32_bf16 v[88:91], a[172:175], a[24:27], v[88:91]\n"
"v_mul_f32 v51, v51, v19\n"
"v_add_f32 v17, 1.0, v17\n"
"v_mfma_f32_16x16x32_bf16 v[92:95], a[168:171], a[20:23], v[92:95]\n"
"v_rcp_f32 v16, v16\n"
"v_mfma_f32_16x16x32_bf16 v[92:95], a[172:175], a[28:31], v[92:95]\n"
"s_waitcnt lgkmcnt(0)\n"
"v_exp_f32 v18, v38\n"
"v_mfma_f32_16x16x32_bf16 v[64:67], a[176:179], v[160:163], v[64:67]\n"
"s_waitcnt vmcnt(9)\n"
"s_barrier\n"
"ds_read_b64_tr_b16 a[160:161], v1 offset:0\n"
"v_mfma_f32_16x16x32_bf16 v[64:67], a[180:183], v[168:171], v[64:67]\n"
"ds_read_b64_tr_b16 a[162:163], v1 offset:8192\n"
"v_mul_f32 v36, v36, v16\n"
"v_mfma_f32_16x16x32_bf16 v[68:71], a[176:179], v[164:167], v[68:71]\n"
"buffer_load_dwordx2 v[30:31], v0, s[64:67], s99 offen sc1\n"
"v_mfma_f32_16x16x32_bf16 v[68:71], a[180:183], v[172:175], v[68:71]\n"
"ds_read_b64_tr_b16 a[164:165], v1 offset:1024\n"
"v_add_f32 v18, 1.0, v18\n"
"v_mfma_f32_16x16x32_bf16 v[72:75], a[176:179], a[32:35], v[72:75]\n"
"ds_read_b64_tr_b16 a[166:167], v1 offset:9216\n"
"v_mfma_f32_16x16x32_bf16 v[72:75], a[180:183], a[40:43], v[72:75]\n"
"v_rcp_f32 v17, v17\n"
"v_mfma_f32_16x16x32_bf16 v[76:79], a[176:179], a[36:39], v[76:79]\n"
"ds_read_b64_tr_b16 a[168:169], v2 offset:0\n"
"v_mul_f32 v37, v37, v17\n"
"v_mfma_f32_16x16x32_bf16 v[76:79], a[180:183], a[44:47], v[76:79]\n"
"ds_read_b64_tr_b16 a[170:171], v2 offset:8192\n"
"v_mfma_f32_16x16x32_bf16 v[80:83], a[184:187], v[160:163], v[80:83]\n"
"v_exp_f32 v19, v39\n"
"v_mfma_f32_16x16x32_bf16 v[80:83], a[188:191], v[168:171], v[80:83]\n"
"ds_read_b64_tr_b16 a[172:173], v2 offset:1024\n"
"v_add_f32 v19, 1.0, v19\n"
"v_mfma_f32_16x16x32_bf16 v[84:87], a[184:187], v[164:167], v[84:87]\n"
"ds_read_b64_tr_b16 a[174:175], v2 offset:9216\n"
"v_mfma_f32_16x16x32_bf16 v[84:87], a[188:191], v[172:175], v[84:87]\n"
"v_rcp_f32 v18, v18\n"
"v_mfma_f32_16x16x32_bf16 v[88:91], a[184:187], a[32:35], v[88:91]\n"
"v_exp_f32 v16, v52\n"
"v_mfma_f32_16x16x32_bf16 v[88:91], a[188:191], a[40:43], v[88:91]\n"
"v_mul_f32 v38, v38, v18\n"
"v_add_f32 v16, 1.0, v16\n"
"v_mfma_f32_16x16x32_bf16 v[92:95], a[184:187], a[36:39], v[92:95]\n"
"v_rcp_f32 v19, v19\n"
"v_mfma_f32_16x16x32_bf16 v[92:95], a[188:191], a[44:47], v[92:95]\n"
"s_waitcnt lgkmcnt(0)\n"
"v_exp_f32 v17, v53\n"
"v_mfma_f32_16x16x32_bf16 v[64:67], a[160:163], v[176:179], v[64:67]\n"
"ds_read_b64_tr_b16 a[176:177], v1 offset:2048\n"
"v_mul_f32 v39, v39, v19\n"
"v_mfma_f32_16x16x32_bf16 v[64:67], a[164:167], v[184:187], v[64:67]\n"
"ds_read_b64_tr_b16 a[178:179], v1 offset:10240\n"
"v_add_f32 v17, 1.0, v17\n"
"v_mfma_f32_16x16x32_bf16 v[68:71], a[160:163], v[180:183], v[68:71]\n"
"v_rcp_f32 v16, v16\n"
"v_mfma_f32_16x16x32_bf16 v[68:71], a[164:167], v[188:191], v[68:71]\n"
"ds_read_b64_tr_b16 a[180:181], v1 offset:3072\n"
"v_mul_f32 v52, v52, v16\n"
"v_mfma_f32_16x16x32_bf16 v[72:75], a[160:163], a[48:51], v[72:75]\n"
"ds_read_b64_tr_b16 a[182:183], v1 offset:11264\n"
"v_mfma_f32_16x16x32_bf16 v[72:75], a[164:167], a[56:59], v[72:75]\n"
"v_exp_f32 v18, v54\n"
"v_mfma_f32_16x16x32_bf16 v[76:79], a[160:163], a[52:55], v[76:79]\n"
"ds_read_b64_tr_b16 a[184:185], v2 offset:2048\n"
"v_add_f32 v18, 1.0, v18\n"
"v_mfma_f32_16x16x32_bf16 v[76:79], a[164:167], a[60:63], v[76:79]\n"
"ds_read_b64_tr_b16 a[186:187], v2 offset:10240\n"
"v_mfma_f32_16x16x32_bf16 v[80:83], a[168:171], v[176:179], v[80:83]\n"
"v_rcp_f32 v17, v17\n"
"v_mfma_f32_16x16x32_bf16 v[80:83], a[172:175], v[184:187], v[80:83]\n"
"ds_read_b64_tr_b16 a[188:189], v2 offset:3072\n"
"v_mul_f32 v53, v53, v17\n"
"v_mfma_f32_16x16x32_bf16 v[84:87], a[168:171], v[180:183], v[84:87]\n"
"ds_read_b64_tr_b16 a[190:191], v2 offset:11264\n"
"v_mfma_f32_16x16x32_bf16 v[84:87], a[172:175], v[188:191], v[84:87]\n"
"v_exp_f32 v19, v55\n"
"v_mfma_f32_16x16x32_bf16 v[88:91], a[168:171], a[48:51], v[88:91]\n"
"v_rcp_f32 v18, v18\n"
"v_mfma_f32_16x16x32_bf16 v[88:91], a[172:175], a[56:59], v[88:91]\n"
"v_add_f32 v19, 1.0, v19\n"
"v_mul_f32 v54, v54, v18\n"
"v_mfma_f32_16x16x32_bf16 v[92:95], a[168:171], a[52:55], v[92:95]\n"
"v_exp_f32 v16, v40\n"
"v_mfma_f32_16x16x32_bf16 v[92:95], a[172:175], a[60:63], v[92:95]\n"
"s_waitcnt lgkmcnt(0)\n"
"v_rcp_f32 v19, v19\n"
"v_mfma_f32_16x16x32_bf16 v[64:67], a[176:179], v[192:195], v[64:67]\n"
"s_waitcnt vmcnt(6)\n"
"s_barrier\n"
"ds_read_b64_tr_b16 a[160:161], v1 offset:16384\n"
"v_mfma_f32_16x16x32_bf16 v[64:67], a[180:183], v[200:203], v[64:67]\n"
"ds_read_b64_tr_b16 a[162:163], v1 offset:24576\n"
"v_add_f32 v16, 1.0, v16\n"
"v_mfma_f32_16x16x32_bf16 v[68:71], a[176:179], v[196:199], v[68:71]\n"
"v_exp_f32 v17, v41\n"
"v_mfma_f32_16x16x32_bf16 v[68:71], a[180:183], v[204:207], v[68:71]\n"
"ds_read_b64_tr_b16 a[164:165], v1 offset:17408\n"
"v_mul_f32 v55, v55, v19\n"
"v_mfma_f32_16x16x32_bf16 v[72:75], a[176:179], a[64:67], v[72:75]\n"
"ds_read_b64_tr_b16 a[166:167], v1 offset:25600\n"
"v_add_f32 v17, 1.0, v17\n"
"v_mfma_f32_16x16x32_bf16 v[72:75], a[180:183], a[72:75], v[72:75]\n"
"v_rcp_f32 v16, v16\n"
"v_mfma_f32_16x16x32_bf16 v[76:79], a[176:179], a[68:71], v[76:79]\n"
"ds_read_b64_tr_b16 a[168:169], v2 offset:16384\n"
"v_mul_f32 v40, v40, v16\n"
"v_mfma_f32_16x16x32_bf16 v[76:79], a[180:183], a[76:79], v[76:79]\n"
"ds_read_b64_tr_b16 a[170:171], v2 offset:24576\n"
"v_mfma_f32_16x16x32_bf16 v[80:83], a[184:187], v[192:195], v[80:83]\n"
"v_exp_f32 v18, v42\n"
"v_mfma_f32_16x16x32_bf16 v[80:83], a[188:191], v[200:203], v[80:83]\n"
"ds_read_b64_tr_b16 a[172:173], v2 offset:17408\n"
"v_add_f32 v18, 1.0, v18\n"
"v_mfma_f32_16x16x32_bf16 v[84:87], a[184:187], v[196:199], v[84:87]\n"
"ds_read_b64_tr_b16 a[174:175], v2 offset:25600\n"
"v_mfma_f32_16x16x32_bf16 v[84:87], a[188:191], v[204:207], v[84:87]\n"
"v_rcp_f32 v17, v17\n"
"v_mfma_f32_16x16x32_bf16 v[88:91], a[184:187], a[64:67], v[88:91]\n"
"v_exp_f32 v19, v43\n"
"v_mfma_f32_16x16x32_bf16 v[88:91], a[188:191], a[72:75], v[88:91]\n"
"v_mul_f32 v41, v41, v17\n"
"v_add_f32 v19, 1.0, v19\n"
"v_mfma_f32_16x16x32_bf16 v[92:95], a[184:187], a[68:71], v[92:95]\n"
"v_rcp_f32 v18, v18\n"
"v_mfma_f32_16x16x32_bf16 v[92:95], a[188:191], a[76:79], v[92:95]\n"
"s_waitcnt lgkmcnt(0)\n"
"v_exp_f32 v16, v56\n"
"v_mfma_f32_16x16x32_bf16 v[64:67], a[160:163], v[208:211], v[64:67]\n"
"ds_read_b64_tr_b16 a[176:177], v1 offset:18432\n"
"v_mul_f32 v42, v42, v18\n"
"v_mfma_f32_16x16x32_bf16 v[64:67], a[164:167], v[216:219], v[64:67]\n"
"ds_read_b64_tr_b16 a[178:179], v1 offset:26624\n"
"v_add_f32 v16, 1.0, v16\n"
"v_mfma_f32_16x16x32_bf16 v[68:71], a[160:163], v[212:215], v[68:71]\n"
"v_rcp_f32 v19, v19\n"
"v_mfma_f32_16x16x32_bf16 v[68:71], a[164:167], v[220:223], v[68:71]\n"
"ds_read_b64_tr_b16 a[180:181], v1 offset:19456\n"
"v_mul_f32 v43, v43, v19\n"
"v_mfma_f32_16x16x32_bf16 v[72:75], a[160:163], a[80:83], v[72:75]\n"
"ds_read_b64_tr_b16 a[182:183], v1 offset:27648\n"
"v_mfma_f32_16x16x32_bf16 v[72:75], a[164:167], a[88:91], v[72:75]\n"
"v_exp_f32 v17, v57\n"
"v_mfma_f32_16x16x32_bf16 v[76:79], a[160:163], a[84:87], v[76:79]\n"
"ds_read_b64_tr_b16 a[184:185], v2 offset:18432\n"
"v_add_f32 v17, 1.0, v17\n"
"v_mfma_f32_16x16x32_bf16 v[76:79], a[164:167], a[92:95], v[76:79]\n"
"ds_read_b64_tr_b16 a[186:187], v2 offset:26624\n"
"v_mfma_f32_16x16x32_bf16 v[80:83], a[168:171], v[208:211], v[80:83]\n"
"v_rcp_f32 v16, v16\n"
"v_mfma_f32_16x16x32_bf16 v[80:83], a[172:175], v[216:219], v[80:83]\n"
"ds_read_b64_tr_b16 a[188:189], v2 offset:19456\n"
"v_mul_f32 v56, v56, v16\n"
"v_mfma_f32_16x16x32_bf16 v[84:87], a[168:171], v[212:215], v[84:87]\n"
"ds_read_b64_tr_b16 a[190:191], v2 offset:27648\n"
"v_mfma_f32_16x16x32_bf16 v[84:87], a[172:175], v[220:223], v[84:87]\n"
"v_exp_f32 v18, v58\n"
"v_mfma_f32_16x16x32_bf16 v[88:91], a[168:171], a[80:83], v[88:91]\n"
"v_rcp_f32 v17, v17\n"
"v_mfma_f32_16x16x32_bf16 v[88:91], a[172:175], a[88:91], v[88:91]\n"
"v_add_f32 v18, 1.0, v18\n"
"v_mul_f32 v57, v57, v17\n"
"v_mfma_f32_16x16x32_bf16 v[92:95], a[168:171], a[84:87], v[92:95]\n"
"v_exp_f32 v19, v59\n"
"v_mfma_f32_16x16x32_bf16 v[92:95], a[172:175], a[92:95], v[92:95]\n"
"s_waitcnt lgkmcnt(0)\n"
"s_waitcnt vmcnt(0)\n"
"v_cmp_ne_u64 vcc, v[30:31], v[28:29]\n"
"s_nop 1\n"
"s_cbranch_vccz L_ok0_%=\n"
"s_mov_b32 s100, 0x4000\n"
"L_rt0_%=:\n"
"s_sleep 2\n"
"buffer_load_dwordx2 v[30:31], v0, s[64:67], s99 offen sc1\n"
"s_waitcnt vmcnt(0)\n"
"v_cmp_ne_u64 vcc, v[30:31], v[28:29]\n"
"s_nop 4\n"
"s_cbranch_vccz L_ok0_%=\n"
"s_sub_u32 s100, s100, 1\n"
"s_cmp_lg_u32 s100, 0\n"
"s_cbranch_scc1 L_rt0_%=\n"
"L_ok0_%=:\n"
"v_mfma_f32_16x16x32_bf16 v[64:67], a[176:179], v[224:227], v[64:67]\n"
"s_addk_i32 m0, 16384\n"
"s_barrier\n"
"ds_read_b64_tr_b16 a[160:161], v3 offset:16384\n"
"v_mfma_f32_16x16x32_bf16 v[64:67], a[180:183], v[232:235], v[64:67]\n"
"s_add_u32 s82, s40, 0\n"
"ds_read_b64_tr_b16 a[162:163], v3 offset:24576\n"
"v_add_f32 v19, 1.0, v19\n"
"v_mfma_f32_16x16x32_bf16 v[68:71], a[176:179], v[228:231], v[68:71]\n"
"buffer_load_dwordx4 v9, s[64:67], s82 offen offset:0 sc1 lds\n"
"v_mfma_f32_16x16x32_bf16 v[68:71], a[180:183], v[236:239], v[68:71]\n"
"ds_read_b64_tr_b16 a[164:165], v3 offset:17408\n"
"v_mfma_f32_16x16x32_bf16 v[72:75], a[176:179], a[96:99], v[72:75]\n"
"ds_read_b64_tr_b16 a[166:167], v3 offset:25600\n"
"v_mfma_f32_16x16x32_bf16 v[72:75], a[180:183], a[104:107], v[72:75]\n"
"v_rcp_f32 v18, v18\n"
"v_mfma_f32_16x16x32_bf16 v[76:79], a[176:179], a[100:103], v[76:79]\n"
"ds_read_b64_tr_b16 a[168:169], v4 offset:16384\n"
"v_mul_f32 v58, v58, v18\n"
"v_mfma_f32_16x16x32_bf16 v[76:79], a[180:183], a[108:111], v[76:79]\n"
"s_addk_i32 s82, 3072\n"
"ds_read_b64_tr_b16 a[170:171], v4 offset:24576\n"
"v_mfma_f32_16x16x32_bf16 v[80:83], a[184:187], v[224:227], v[80:83]\n"
"buffer_load_dwordx4 v9, s[64:67], s82 offen offset:1024 sc1 lds\n"
"v_mfma_f32_16x16x32_bf16 v[80:83], a[188:191], v[232:235], v[80:83]\n"
"ds_read_b64_tr_b16 a[172:173], v4 offset:17408\n"
"v_mfma_f32_16x16x32_bf16 v[84:87], a[184:187], v[228:231], v[84:87]\n"
"ds_read_b64_tr_b16 a[174:175], v4 offset:25600\n"
"v_mfma_f32_16x16x32_bf16 v[84:87], a[188:191], v[236:239], v[84:87]\n"
"s_addk_i32 s82, 3072\n"
"v_exp_f32 v16, v44\n"
"v_mfma_f32_16x16x32_bf16 v[88:91], a[184:187], a[96:99], v[88:91]\n"
"buffer_load_dwordx4 v9, s[64:67], s82 offen offset:2048 sc1 lds\n"
"v_mfma_f32_16x16x32_bf16 v[88:91], a[188:191], a[104:107], v[88:91]\n"
"v_rcp_f32 v19, v19\n"
"v_mfma_f32_16x16x32_bf16 v[92:95], a[184:187], a[100:103], v[92:95]\n"
"s_addk_i32 s82, 3072\n"
"v_add_f32 v16, 1.0, v16\n"
"v_mul_f32 v59, v59, v19\n"
"v_mfma_f32_16x16x32_bf16 v[92:95], a[188:191], a[108:111], v[92:95]\n"
"s_waitcnt lgkmcnt(0)\n"
"buffer_load_dwordx4 v9, s[64:67], s82 offen offset:3072 sc1 lds\n"
"v_mfma_f32_16x16x32_bf16 v[64:67], a[160:163], v[240:243], v[64:67]\n"
"s_addk_i32 m0, 16384\n"
"ds_read_b64_tr_b16 a[176:177], v3 offset:18432\n"
"v_mfma_f32_16x16x32_bf16 v[64:67], a[164:167], v[248:251], v[64:67]\n"
"s_addk_i32 s82, 7168\n"
"ds_read_b64_tr_b16 a[178:179], v3 offset:26624\n"
"v_mfma_f32_16x16x32_bf16 v[68:71], a[160:163], v[244:247], v[68:71]\n"
"buffer_load_dwordx4 v9, s[64:67], s82 offen offset:0 sc1 lds\n"
"v_mfma_f32_16x16x32_bf16 v[68:71], a[164:167], v[252:255], v[68:71]\n"
"ds_read_b64_tr_b16 a[180:181], v3 offset:19456\n"
"v_mfma_f32_16x16x32_bf16 v[72:75], a[160:163], a[112:115], v[72:75]\n"
"ds_read_b64_tr_b16 a[182:183], v3 offset:27648\n"
"v_mfma_f32_16x16x32_bf16 v[72:75], a[164:167], a[120:123], v[72:75]\n"
"v_exp_f32 v17, v45\n"
"v_mfma_f32_16x16x32_bf16 v[76:79], a[160:163], a[116:119], v[76:79]\n"
"ds_read_b64_tr_b16 a[184:185], v4 offset:18432\n"
"v_add_f32 v17, 1.0, v17\n"
"v_mfma_f32_16x16x32_bf16 v[76:79], a[164:167], a[124:127], v[76:79]\n"
"s_addk_i32 s82, 3072\n"
"ds_read_b64_tr_b16 a[186:187], v4 offset:26624\n"
"v_mfma_f32_16x16x32_bf16 v[80:83], a[168:171], v[240:243], v[80:83]\n"
"buffer_load_dwordx4 v9, s[64:67], s82 offen offset:1024 sc1 lds\n"
"v_mfma_f32_16x16x32_bf16 v[80:83], a[172:175], v[248:251], v[80:83]\n"
"ds_read_b64_tr_b16 a[188:189], v4 offset:19456\n"
"v_mfma_f32_16x16x32_bf16 v[84:87], a[168:171], v[244:247], v[84:87]\n"
"ds_read_b64_tr_b16 a[190:191], v4 offset:27648\n"
"v_mfma_f32_16x16x32_bf16 v[84:87], a[172:175], v[252:255], v[84:87]\n"
"s_addk_i32 s82, 3072\n"
"v_rcp_f32 v16, v16\n"
"v_mfma_f32_16x16x32_bf16 v[88:91], a[168:171], a[112:115], v[88:91]\n"
"buffer_load_dwordx4 v9, s[64:67], s82 offen offset:2048 sc1 lds\n"
"v_mfma_f32_16x16x32_bf16 v[88:91], a[172:175], a[120:123], v[88:91]\n"
"v_exp_f32 v18, v46\n"
"v_mfma_f32_16x16x32_bf16 v[92:95], a[168:171], a[116:119], v[92:95]\n"
"s_addk_i32 s82, 3072\n"
"v_mul_f32 v44, v44, v16\n"
"v_add_f32 v18, 1.0, v18\n"
"v_mfma_f32_16x16x32_bf16 v[92:95], a[172:175], a[124:127], v[92:95]\n"
"s_waitcnt lgkmcnt(0)\n"
"buffer_load_dwordx4 v9, s[64:67], s82 offen offset:3072 sc1 lds\n"
"v_mfma_f32_16x16x32_bf16 v[64:67], a[176:179], a[144:147], v[64:67]\n"
"s_add_u32 m0, s77, 32768\n"
"s_waitcnt vmcnt(4)\n"
"s_barrier\n"
"ds_read_b64_tr_b16 a[160:161], v24 offset:32768\n"
"v_mfma_f32_16x16x32_bf16 v[64:67], a[180:183], a[152:155], v[64:67]\n"
"s_addk_i32 s82, 7168\n"
"ds_read_b64_tr_b16 a[162:163], v24 offset:33792\n"
"v_mfma_f32_16x16x32_bf16 v[68:71], a[176:179], a[148:151], v[68:71]\n"
"buffer_load_dwordx4 v9, s[64:67], s82 offen offset:0 sc1 lds\n"
"v_mfma_f32_16x16x32_bf16 v[68:71], a[180:183], a[156:159], v[68:71]\n"
"ds_read_b64_tr_b16 a[164:165], v24 offset:34816\n"
"v_mfma_f32_16x16x32_bf16 v[72:75], a[176:179], a[128:131], v[72:75]\n"
"ds_read_b64_tr_b16 a[166:167], v24 offset:35840\n"
"v_mul_f32 v32, v32, v64\n"
"v_mfma_f32_16x16x32_bf16 v[72:75], a[180:183], a[136:139], v[72:75]\n"
"v_mul_f32 v33, v33, v65\n"
"v_mul_f32 v34, v34, v66\n"
"v_mfma_f32_16x16x32_bf16 v[76:79], a[176:179], a[132:135], v[76:79]\n"
"ds_read_b64_tr_b16 a[168:169], v25 offset:32768\n"
"v_mul_f32 v35, v35, v67\n"
"v_mfma_f32_16x16x32_bf16 v[76:79], a[180:183], a[140:143], v[76:79]\n"
"s_addk_i32 s82, 3072\n"
"ds_read_b64_tr_b16 a[170:171], v25 offset:33792\n"
"v_cvt_pk_bf16_f32 v64, v32, v33\n"
"v_mfma_f32_16x16x32_bf16 v[80:83], a[184:187], a[144:147], v[80:83]\n"
"buffer_load_dwordx4 v9, s[64:67], s82 offen offset:1024 sc1 lds\n"
"v_mfma_f32_16x16x32_bf16 v[80:83], a[188:191], a[152:155], v[80:83]\n"
"ds_read_b64_tr_b16 a[172:173], v25 offset:34816\n"
"v_cvt_pk_bf16_f32 v65, v34, v35\n"
"v_mfma_f32_16x16x32_bf16 v[84:87], a[184:187], a[148:151], v[84:87]\n"
"ds_read_b64_tr_b16 a[174:175], v25 offset:35840\n"
"v_mul_f32 v36, v36, v68\n"
"v_mfma_f32_16x16x32_bf16 v[84:87], a[188:191], a[156:159], v[84:87]\n"
"s_addk_i32 s82, 3072\n"
"v_mul_f32 v37, v37, v69\n"
"v_mul_f32 v38, v38, v70\n"
"v_mfma_f32_16x16x32_bf16 v[88:91], a[184:187], a[128:131], v[88:91]\n"
"buffer_load_dwordx4 v9, s[64:67], s82 offen offset:2048 sc1 lds\n"
"v_mfma_f32_16x16x32_bf16 v[88:91], a[188:191], a[136:139], v[88:91]\n"
"v_mul_f32 v48, v48, v80\n"
"v_mul_f32 v49, v49, v81\n"
"v_mfma_f32_16x16x32_bf16 v[92:95], a[184:187], a[132:135], v[92:95]\n"
"s_addk_i32 s82, 3072\n"
"v_mul_f32 v50, v50, v82\n"
"v_mul_f32 v51, v51, v83\n"
"v_mfma_f32_16x16x32_bf16 v[92:95], a[188:191], a[140:143], v[92:95]\n"
"s_waitcnt lgkmcnt(0)\n"
"buffer_load_dwordx4 v9, s[64:67], s82 offen offset:3072 sc1 lds\n"
"v_mfma_f32_16x16x32_bf16 v[96:99], v[128:131], a[160:163], 0\n"
"s_addk_i32 m0, 16384\n"
"ds_read_b64_tr_b16 a[176:177], v26 offset:32768\n"
"v_cvt_pk_bf16_f32 v66, v48, v49\n"
"v_mfma_f32_16x16x32_bf16 v[96:99], v[136:139], a[164:167], v[96:99]\n"
"s_addk_i32 s82, 7168\n"
"ds_read_b64_tr_b16 a[178:179], v26 offset:33792\n"
"v_cvt_pk_bf16_f32 v67, v50, v51\n"
"v_mfma_f32_16x16x32_bf16 v[100:103], v[132:135], a[160:163], 0\n"
"buffer_load_dwordx4 v9, s[64:67], s82 offen offset:0 sc1 lds\n"
"v_mfma_f32_16x16x32_bf16 v[100:103], v[140:143], a[164:167], v[100:103]\n"
"ds_read_b64_tr_b16 a[180:181], v26 offset:34816\n"
"v_mul_f32 v39, v39, v71\n"
"v_mfma_f32_16x16x32_bf16 v[104:107], a[0:3], a[160:163], 0\n"
"ds_read_b64_tr_b16 a[182:183], v26 offset:35840\n"
"v_mul_f32 v52, v52, v84\n"
"v_mfma_f32_16x16x32_bf16 v[104:107], a[8:11], a[164:167], v[104:107]\n"
"ds_write_b128 v5, v[64:67] offset:49152\n"
"v_mfma_f32_16x16x32_bf16 v[108:111], a[4:7], a[160:163], 0\n"
"ds_read_b64_tr_b16 a[184:185], v27 offset:32768\n"
"v_mul_f32 v53, v53, v85\n"
"v_mfma_f32_16x16x32_bf16 v[108:111], a[12:15], a[164:167], v[108:111]\n"
"s_addk_i32 s82, 3072\n"
"ds_read_b64_tr_b16 a[186:187], v27 offset:33792\n"
"v_mul_f32 v54, v54, v86\n"
"v_mfma_f32_16x16x32_bf16 v[112:115], v[128:131], a[168:171], 0\n"
"buffer_load_dwordx4 v9, s[64:67], s82 offen offset:1024 sc1 lds\n"
"v_mfma_f32_16x16x32_bf16 v[112:115], v[136:139], a[172:175], v[112:115]\n"
"ds_read_b64_tr_b16 a[188:189], v27 offset:34816\n"
"v_mul_f32 v55, v55, v87\n"
"v_mfma_f32_16x16x32_bf16 v[116:119], v[132:135], a[168:171], 0\n"
"ds_read_b64_tr_b16 a[190:191], v27 offset:35840\n"
"v_cvt_pk_bf16_f32 v68, v36, v37\n"
"v_mfma_f32_16x16x32_bf16 v[116:119], v[140:143], a[172:175], v[116:119]\n"
"s_addk_i32 s82, 3072\n"
"v_rcp_f32 v17, v17\n"
"v_mfma_f32_16x16x32_bf16 v[120:123], a[0:3], a[168:171], 0\n"
"buffer_load_dwordx4 v9, s[64:67], s82 offen offset:2048 sc1 lds\n"
"v_mfma_f32_16x16x32_bf16 v[120:123], a[8:11], a[172:175], v[120:123]\n"
"v_exp_f32 v19, v47\n"
"v_mfma_f32_16x16x32_bf16 v[124:127], a[4:7], a[168:171], 0\n"
"s_addk_i32 s82, 3072\n"
"v_mul_f32 v45, v45, v17\n"
"v_cvt_pk_bf16_f32 v69, v38, v39\n"
"v_mfma_f32_16x16x32_bf16 v[124:127], a[12:15], a[172:175], v[124:127]\n"
"s_waitcnt lgkmcnt(0)\n"
"buffer_load_dwordx4 v9, s[64:67], s82 offen offset:3072 sc1 lds\n"
"v_mfma_f32_16x16x32_bf16 a[192:195], v[128:131], a[176:179], 0\n"
"s_waitcnt vmcnt(8)\n"
"s_barrier\n"
"ds_read_b64_tr_b16 a[160:161], v24 offset:49152\n"
"v_mfma_f32_16x16x32_bf16 a[192:195], v[136:139], a[180:183], a[192:195]\n"
"ds_read_b64_tr_b16 a[162:163], v24 offset:50176\n"
"v_add_f32 v19, 1.0, v19\n"
"v_mfma_f32_16x16x32_bf16 a[196:199], v[132:135], a[176:179], 0\n"
"ds_read_b64_tr_b16 a[164:165], v24 offset:51200\n"
"v_cvt_pk_bf16_f32 v70, v52, v53\n"
"v_mfma_f32_16x16x32_bf16 a[196:199], v[140:143], a[180:183], a[196:199]\n"
"ds_read_b64_tr_b16 a[166:167], v24 offset:52224\n"
"v_cvt_pk_bf16_f32 v71, v54, v55\n"
"v_mfma_f32_16x16x32_bf16 a[200:203], a[0:3], a[176:179], 0\n"
"ds_read_b64_tr_b16 a[168:169], v25 offset:49152\n"
"v_mul_f32 v40, v40, v72\n"
"v_mfma_f32_16x16x32_bf16 a[200:203], a[8:11], a[180:183], a[200:203]\n"
"ds_read_b64_tr_b16 a[170:171], v25 offset:50176\n"
"v_mul_f32 v41, v41, v73\n"
"v_mfma_f32_16x16x32_bf16 a[204:207], a[4:7], a[176:179], 0\n"
"s_addk_i32 m0, 16384\n"
"ds_read_b64_tr_b16 a[172:173], v25 offset:51200\n"
"v_mul_f32 v42, v42, v74\n"
"v_mfma_f32_16x16x32_bf16 a[204:207], a[12:15], a[180:183], a[204:207]\n"
"s_addk_i32 s82, 7168\n"
"ds_read_b64_tr_b16 a[174:175], v25 offset:52224\n"
"v_mul_f32 v43, v43, v75\n"
"v_mfma_f32_16x16x32_bf16 a[208:211], v[128:131], a[184:187], 0\n"
"buffer_load_dwordx4 v9, s[64:67], s82 offen offset:0 sc1 lds\n"
"v_mfma_f32_16x16x32_bf16 a[208:211], v[136:139], a[188:191], a[208:211]\n"
"s_addk_i32 s82, 3072\n"
"v_rcp_f32 v18, v18\n"
"v_mfma_f32_16x16x32_bf16 a[212:215], v[132:135], a[184:187], 0\n"
"buffer_load_dwordx4 v9, s[64:67], s82 offen offset:1024 sc1 lds\n"
"v_mfma_f32_16x16x32_bf16 a[212:215], v[140:143], a[188:191], a[212:215]\n"
"s_waitcnt lgkmcnt(0)\n"
"v_exp_f32 v16, v60\n"
"v_mfma_f32_16x16x32_bf16 v[96:99], v[144:147], a[160:163], v[96:99]\n"
"ds_read_b64_tr_b16 a[176:177], v26 offset:49152\n"
"v_mul_f32 v46, v46, v18\n"
"v_mfma_f32_16x16x32_bf16 v[96:99], v[152:155], a[164:167], v[96:99]\n"
"ds_read_b64_tr_b16 a[178:179], v26 offset:50176\n"
"v_add_f32 v16, 1.0, v16\n"
"v_mfma_f32_16x16x32_bf16 v[100:103], v[148:151], a[160:163], v[100:103]\n"
"buffer_store_dwordx4 v[64:67], v5, s[64:67], s18 offen offset:0 sc1\n"
"v_mfma_f32_16x16x32_bf16 v[100:103], v[156:159], a[164:167], v[100:103]\n"
"ds_read_b64_tr_b16 a[180:181], v26 offset:51200\n"
"v_mul_f32 v56, v56, v88\n"
"v_mfma_f32_16x16x32_bf16 v[104:107], a[16:19], a[160:163], v[104:107]\n"
"s_addk_i32 s82, 3072\n"
"ds_read_b64_tr_b16 a[182:183], v26 offset:52224\n"
"v_mul_f32 v57, v57, v89\n"
"v_mfma_f32_16x16x32_bf16 v[104:107], a[24:27], a[164:167], v[104:107]\n"
"buffer_load_dwordx4 v9, s[64:67], s82 offen offset:2048 sc1 lds\n"
"v_mfma_f32_16x16x32_bf16 v[108:111], a[20:23], a[160:163], v[108:111]\n"
"ds_read_b64_tr_b16 a[184:185], v27 offset:49152\n"
"v_mul_f32 v58, v58, v90\n"
"v_mfma_f32_16x16x32_bf16 v[108:111], a[28:31], a[164:167], v[108:111]\n"
"ds_read_b64_tr_b16 a[186:187], v27 offset:50176\n"
"v_mul_f32 v59, v59, v91\n"
"v_mfma_f32_16x16x32_bf16 v[112:115], v[144:147], a[168:171], v[112:115]\n"
"v_rcp_f32 v19, v19\n"
"v_mfma_f32_16x16x32_bf16 v[112:115], v[152:155], a[172:175], v[112:115]\n"
"ds_read_b64_tr_b16 a[188:189], v27 offset:51200\n"
"v_mul_f32 v47, v47, v19\n"
"v_mfma_f32_16x16x32_bf16 v[116:119], v[148:151], a[168:171], v[116:119]\n"
"s_addk_i32 s82, 3072\n"
"ds_read_b64_tr_b16 a[190:191], v27 offset:52224\n"
"v_cvt_pk_bf16_f32 v72, v40, v41\n"
"v_mfma_f32_16x16x32_bf16 v[116:119], v[156:159], a[172:175], v[116:119]\n"
"buffer_load_dwordx4 v9, s[64:67], s82 offen offset:3072 sc1 lds\n"
"v_mfma_f32_16x16x32_bf16 v[120:123], a[16:19], a[168:171], v[120:123]\n"
"s_add_u32 m0, s77, 0\n"
"ds_write_b128 v5, v[68:71] offset:50176\n"
"v_mfma_f32_16x16x32_bf16 v[120:123], a[24:27], a[172:175], v[120:123]\n"
"s_addk_i32 s82, 7168\n"
"v_exp_f32 v17, v61\n"
"v_mfma_f32_16x16x32_bf16 v[124:127], a[20:23], a[168:171], v[124:127]\n"
"buffer_load_dwordx4 v9, s[64:67], s82 offen offset:0 sc1 lds\n"
"v_mfma_f32_16x16x32_bf16 v[124:127], a[28:31], a[172:175], v[124:127]\n"
"s_waitcnt lgkmcnt(1)\n"
"v_rcp_f32 v16, v16\n"
"v_mfma_f32_16x16x32_bf16 a[192:195], v[144:147], a[176:179], a[192:195]\n"
"s_waitcnt vmcnt(10)\n"
"s_barrier\n"
"ds_read_b64_tr_b16 a[160:161], v20 offset:32768\n"
"v_mfma_f32_16x16x32_bf16 a[192:195], v[152:155], a[180:183], a[192:195]\n"
"ds_read_b64_tr_b16 a[162:163], v20 offset:33792\n"
"v_add_f32 v17, 1.0, v17\n"
"v_mfma_f32_16x16x32_bf16 a[196:199], v[148:151], a[176:179], a[196:199]\n"
"ds_read_b64_tr_b16 a[164:165], v20 offset:34816\n"
"v_mul_f32 v60, v60, v16\n"
"v_mfma_f32_16x16x32_bf16 a[196:199], v[156:159], a[180:183], a[196:199]\n"
"ds_read_b64_tr_b16 a[166:167], v20 offset:35840\n"
"v_cvt_pk_bf16_f32 v73, v42, v43\n"
"v_mfma_f32_16x16x32_bf16 a[200:203], a[16:19], a[176:179], a[200:203]\n"
"ds_read_b64_tr_b16 a[168:169], v21 offset:32768\n"
"v_cvt_pk_bf16_f32 v74, v56, v57\n"
"v_mfma_f32_16x16x32_bf16 a[200:203], a[24:27], a[180:183], a[200:203]\n"
"ds_read_b64_tr_b16 a[170:171], v21 offset:33792\n"
"v_cvt_pk_bf16_f32 v75, v58, v59\n"
"v_mfma_f32_16x16x32_bf16 a[204:207], a[20:23], a[176:179], a[204:207]\n"
"ds_read_b64_tr_b16 a[172:173], v21 offset:34816\n"
"v_mul_f32 v44, v44, v76\n"
"v_mfma_f32_16x16x32_bf16 a[204:207], a[28:31], a[180:183], a[204:207]\n"
"s_addk_i32 s82, 3072\n"
"ds_read_b64_tr_b16 a[174:175], v21 offset:35840\n"
"v_mul_f32 v45, v45, v77\n"
"v_mfma_f32_16x16x32_bf16 a[208:211], v[144:147], a[184:187], a[208:211]\n"
"buffer_load_dwordx4 v9, s[64:67], s82 offen offset:1024 sc1 lds\n"
"v_mfma_f32_16x16x32_bf16 a[208:211], v[152:155], a[188:191], a[208:211]\n"
"s_addk_i32 s82, 3072\n"
"v_exp_f32 v18, v62\n"
"v_mfma_f32_16x16x32_bf16 a[212:215], v[148:151], a[184:187], a[212:215]\n"
"buffer_load_dwordx4 v9, s[64:67], s82 offen offset:2048 sc1 lds\n"
"v_mfma_f32_16x16x32_bf16 a[212:215], v[156:159], a[188:191], a[212:215]\n"
"s_waitcnt lgkmcnt(0)\n"
"v_rcp_f32 v17, v17\n"
"v_mfma_f32_16x16x32_bf16 v[96:99], v[160:163], a[160:163], v[96:99]\n"
"ds_read_b64_tr_b16 a[176:177], v22 offset:32768\n"
"v_add_f32 v18, 1.0, v18\n"
"v_mfma_f32_16x16x32_bf16 v[96:99], v[168:171], a[164:167], v[96:99]\n"
"ds_read_b64_tr_b16 a[178:179], v22 offset:33792\n"
"v_mul_f32 v61, v61, v17\n"
"v_mfma_f32_16x16x32_bf16 v[100:103], v[164:167], a[160:163], v[100:103]\n"
"buffer_store_dwordx4 v[68:71], v5, s[64:67], s18 offen offset:1024 sc1\n"
"v_mfma_f32_16x16x32_bf16 v[100:103], v[172:175], a[164:167], v[100:103]\n"
"ds_read_b64_tr_b16 a[180:181], v22 offset:34816\n"
"v_mul_f32 v46, v46, v78\n"
"v_mfma_f32_16x16x32_bf16 v[104:107], a[32:35], a[160:163], v[104:107]\n"
"s_addk_i32 s82, 3072\n"
"ds_read_b64_tr_b16 a[182:183], v22 offset:35840\n"
"v_mul_f32 v47, v47, v79\n"
"v_mfma_f32_16x16x32_bf16 v[104:107], a[40:43], a[164:167], v[104:107]\n"
"buffer_load_dwordx4 v9, s[64:67], s82 offen offset:3072 sc1 lds\n"
"v_mfma_f32_16x16x32_bf16 v[108:111], a[36:39], a[160:163], v[108:111]\n"
"ds_read_b64_tr_b16 a[184:185], v23 offset:32768\n"
"v_mul_f32 v60, v60, v92\n"
"v_mfma_f32_16x16x32_bf16 v[108:111], a[44:47], a[164:167], v[108:111]\n"
"ds_read_b64_tr_b16 a[186:187], v23 offset:33792\n"
"v_mul_f32 v61, v61, v93\n"
"v_mfma_f32_16x16x32_bf16 v[112:115], v[160:163], a[168:171], v[112:115]\n"
"v_exp_f32 v19, v63\n"
"v_mfma_f32_16x16x32_bf16 v[112:115], v[168:171], a[172:175], v[112:115]\n"
"s_addk_i32 m0, 16384\n"
"ds_read_b64_tr_b16 a[188:189], v23 offset:34816\n"
"v_add_f32 v19, 1.0, v19\n"
"v_mfma_f32_16x16x32_bf16 v[116:119], v[164:167], a[168:171], v[116:119]\n"
"s_addk_i32 s82, 7168\n"
"ds_read_b64_tr_b16 a[190:191], v23 offset:35840\n"
"v_cvt_pk_bf16_f32 v76, v44, v45\n"
"v_mfma_f32_16x16x32_bf16 v[116:119], v[172:175], a[172:175], v[116:119]\n"
"buffer_load_dwordx4 v9, s[64:67], s82 offen offset:0 sc1 lds\n"
"v_mfma_f32_16x16x32_bf16 v[120:123], a[32:35], a[168:171], v[120:123]\n"
"ds_write_b128 v6, v[72:75] offset:49152\n"
"v_mfma_f32_16x16x32_bf16 v[120:123], a[40:43], a[172:175], v[120:123]\n"
"s_addk_i32 s82, 3072\n"
"v_rcp_f32 v18, v18\n"
"v_mfma_f32_16x16x32_bf16 v[124:127], a[36:39], a[168:171], v[124:127]\n"
"buffer_load_dwordx4 v9, s[64:67], s82 offen offset:1024 sc1 lds\n"
"v_mfma_f32_16x16x32_bf16 v[124:127], a[44:47], a[172:175], v[124:127]\n"
"s_waitcnt lgkmcnt(1)\n"
"v_mul_f32 v62, v62, v18\n"
"v_mul_f32 v62, v62, v94\n"
"v_mfma_f32_16x16x32_bf16 a[192:195], v[160:163], a[176:179], a[192:195]\n"
"s_waitcnt vmcnt(12)\n"
"s_barrier\n"
"ds_read_b64_tr_b16 a[160:161], v20 offset:49152\n"
"v_mfma_f32_16x16x32_bf16 a[192:195], v[168:171], a[180:183], a[192:195]\n"
"ds_read_b64_tr_b16 a[162:163], v20 offset:50176\n"
"v_cvt_pk_bf16_f32 v77, v46, v47\n"
"v_mfma_f32_16x16x32_bf16 a[196:199], v[164:167], a[176:179], a[196:199]\n"
"ds_read_b64_tr_b16 a[164:165], v20 offset:51200\n"
"v_cvt_pk_bf16_f32 v78, v60, v61\n"
"v_mfma_f32_16x16x32_bf16 a[196:199], v[172:175], a[180:183], a[196:199]\n"
"ds_read_b64_tr_b16 a[166:167], v20 offset:52224\n"
"v_mfma_f32_16x16x32_bf16 a[200:203], a[32:35], a[176:179], a[200:203]\n"
"ds_read_b64_tr_b16 a[168:169], v21 offset:49152\n"
"v_mfma_f32_16x16x32_bf16 a[200:203], a[40:43], a[180:183], a[200:203]\n"
"ds_read_b64_tr_b16 a[170:171], v21 offset:50176\n"
"v_mfma_f32_16x16x32_bf16 a[204:207], a[36:39], a[176:179], a[204:207]\n"
"ds_read_b64_tr_b16 a[172:173], v21 offset:51200\n"
"v_mfma_f32_16x16x32_bf16 a[204:207], a[44:47], a[180:183], a[204:207]\n"
"s_addk_i32 s82, 3072\n"
"ds_read_b64_tr_b16 a[174:175], v21 offset:52224\n"
"v_mfma_f32_16x16x32_bf16 a[208:211], v[160:163], a[184:187], a[208:211]\n"
"buffer_load_dwordx4 v9, s[64:67], s82 offen offset:2048 sc1 lds\n"
"v_mfma_f32_16x16x32_bf16 a[208:211], v[168:171], a[188:191], a[208:211]\n"
"s_addk_i32 s82, 3072\n"
"v_rcp_f32 v19, v19\n"
"v_mfma_f32_16x16x32_bf16 a[212:215], v[164:167], a[184:187], a[212:215]\n"
"buffer_load_dwordx4 v9, s[64:67], s82 offen offset:3072 sc1 lds\n"
"v_mfma_f32_16x16x32_bf16 a[212:215], v[172:175], a[188:191], a[212:215]\n"
"s_waitcnt lgkmcnt(0)\n"
"v_mul_f32 v63, v63, v19\n"
"v_mul_f32 v63, v63, v95\n"
"v_mfma_f32_16x16x32_bf16 v[96:99], v[176:179], a[160:163], v[96:99]\n"
"ds_read_b64_tr_b16 a[176:177], v22 offset:49152\n"
"v_cvt_pk_bf16_f32 v79, v62, v63\n"
"v_mfma_f32_16x16x32_bf16 v[96:99], v[184:187], a[164:167], v[96:99]\n"
"ds_read_b64_tr_b16 a[178:179], v22 offset:50176\n"
"v_mfma_f32_16x16x32_bf16 v[100:103], v[180:183], a[160:163], v[100:103]\n"
"buffer_store_dwordx4 v[72:75], v6, s[64:67], s18 offen offset:0 sc1\n"
"v_mfma_f32_16x16x32_bf16 v[100:103], v[188:191], a[164:167], v[100:103]\n"
"s_add_u32 m0, s77, 81920\n"
"ds_read_b64_tr_b16 a[180:181], v22 offset:51200\n"
"v_mfma_f32_16x16x32_bf16 v[104:107], a[48:51], a[160:163], v[104:107]\n"
"s_addk_i32 s82, 7168\n"
"ds_read_b64_tr_b16 a[182:183], v22 offset:52224\n"
"v_mfma_f32_16x16x32_bf16 v[104:107], a[56:59], a[164:167], v[104:107]\n"
"buffer_load_dwordx4 v9, s[64:67], s82 offen offset:0 sc1 lds\n"
"v_mfma_f32_16x16x32_bf16 v[108:111], a[52:55], a[160:163], v[108:111]\n"
"ds_read_b64_tr_b16 a[184:185], v23 offset:49152\n"
"v_mfma_f32_16x16x32_bf16 v[108:111], a[60:63], a[164:167], v[108:111]\n"
"ds_read_b64_tr_b16 a[186:187], v23 offset:50176\n"
"v_mfma_f32_16x16x32_bf16 v[112:115], v[176:179], a[168:171], v[112:115]\n"
"v_mfma_f32_16x16x32_bf16 v[112:115], v[184:187], a[172:175], v[112:115]\n"
"ds_read_b64_tr_b16 a[188:189], v23 offset:51200\n"
"v_mfma_f32_16x16x32_bf16 v[116:119], v[180:183], a[168:171], v[116:119]\n"
"s_addk_i32 s82, 3072\n"
"ds_read_b64_tr_b16 a[190:191], v23 offset:52224\n"
"v_mfma_f32_16x16x32_bf16 v[116:119], v[188:191], a[172:175], v[116:119]\n"
"buffer_load_dwordx4 v9, s[64:67], s82 offen offset:1024 sc1 lds\n"
"v_mfma_f32_16x16x32_bf16 v[120:123], a[48:51], a[168:171], v[120:123]\n"
"ds_write_b128 v6, v[76:79] offset:50176\n"
"v_mfma_f32_16x16x32_bf16 v[120:123], a[56:59], a[172:175], v[120:123]\n"
"s_addk_i32 s82, 3072\n"
"v_mfma_f32_16x16x32_bf16 v[124:127], a[52:55], a[168:171], v[124:127]\n"
"buffer_load_dwordx4 v9, s[64:67], s82 offen offset:2048 sc1 lds\n"
"v_mfma_f32_16x16x32_bf16 v[124:127], a[60:63], a[172:175], v[124:127]\n"
"s_waitcnt lgkmcnt(1)\n"
"v_mfma_f32_16x16x32_bf16 a[192:195], v[176:179], a[176:179], a[192:195]\n"
"s_waitcnt vmcnt(13)\n"
"s_barrier\n"
"ds_read_b64_tr_b16 a[160:161], v24 offset:0\n"
"v_mfma_f32_16x16x32_bf16 a[192:195], v[184:187], a[180:183], a[192:195]\n"
"ds_read_b64_tr_b16 a[162:163], v24 offset:1024\n"
"v_mfma_f32_16x16x32_bf16 a[196:199], v[180:183], a[176:179], a[196:199]\n"
"ds_read_b64_tr_b16 a[164:165], v24 offset:2048\n"
"v_mfma_f32_16x16x32_bf16 a[196:199], v[188:191], a[180:183], a[196:199]\n"
"ds_read_b64_tr_b16 a[166:167], v24 offset:3072\n"
"v_mfma_f32_16x16x32_bf16 a[200:203], a[48:51], a[176:179], a[200:203]\n"
"ds_read_b64_tr_b16 a[168:169], v25 offset:0\n"
"v_mfma_f32_16x16x32_bf16 a[200:203], a[56:59], a[180:183], a[200:203]\n"
"ds_read_b64_tr_b16 a[170:171], v25 offset:1024\n"
"v_mfma_f32_16x16x32_bf16 a[204:207], a[52:55], a[176:179], a[204:207]\n"
"ds_read_b64_tr_b16 a[172:173], v25 offset:2048\n"
"v_mfma_f32_16x16x32_bf16 a[204:207], a[60:63], a[180:183], a[204:207]\n"
"s_addk_i32 s82, 3072\n"
"ds_read_b64_tr_b16 a[174:175], v25 offset:3072\n"
"v_mfma_f32_16x16x32_bf16 a[208:211], v[176:179], a[184:187], a[208:211]\n"
"buffer_load_dwordx4 v9, s[64:67], s82 offen offset:3072 sc1 lds\n"
"v_mfma_f32_16x16x32_bf16 a[208:211], v[184:187], a[188:191], a[208:211]\n"
"s_addk_i32 m0, 16384\n"
"s_addk_i32 s82, 7168\n"
"v_mfma_f32_16x16x32_bf16 a[212:215], v[180:183], a[184:187], a[212:215]\n"
"buffer_load_dwordx4 v9, s[64:67], s82 offen offset:0 sc1 lds\n"
"v_mfma_f32_16x16x32_bf16 a[212:215], v[188:191], a[188:191], a[212:215]\n"
"s_waitcnt lgkmcnt(0)\n"
"v_mfma_f32_16x16x32_bf16 v[96:99], v[192:195], a[160:163], v[96:99]\n"
"ds_read_b64_tr_b16 a[176:177], v26 offset:0\n"
"v_mfma_f32_16x16x32_bf16 v[96:99], v[200:203], a[164:167], v[96:99]\n"
"ds_read_b64_tr_b16 a[178:179], v26 offset:1024\n"
"v_mfma_f32_16x16x32_bf16 v[100:103], v[196:199], a[160:163], v[100:103]\n"
"buffer_store_dwordx4 v[76:79], v6, s[64:67], s18 offen offset:1024 sc1\n"
"v_mfma_f32_16x16x32_bf16 v[100:103], v[204:207], a[164:167], v[100:103]\n"
"s_add_u32 s18, s9, 16384\n"
"ds_read_b64_tr_b16 a[180:181], v26 offset:2048\n"
"v_mfma_f32_16x16x32_bf16 v[104:107], a[64:67], a[160:163], v[104:107]\n"
"s_addk_i32 s82, 3072\n"
"ds_read_b64_tr_b16 a[182:183], v26 offset:3072\n"
"v_mfma_f32_16x16x32_bf16 v[104:107], a[72:75], a[164:167], v[104:107]\n"
"buffer_load_dwordx4 v9, s[64:67], s82 offen offset:1024 sc1 lds\n"
"v_mfma_f32_16x16x32_bf16 v[108:111], a[68:71], a[160:163], v[108:111]\n"
"ds_read_b64_tr_b16 a[184:185], v27 offset:0\n"
"v_mfma_f32_16x16x32_bf16 v[108:111], a[76:79], a[164:167], v[108:111]\n"
"ds_read_b64_tr_b16 a[186:187], v27 offset:1024\n"
"v_mfma_f32_16x16x32_bf16 v[112:115], v[192:195], a[168:171], v[112:115]\n"
"v_mfma_f32_16x16x32_bf16 v[112:115], v[200:203], a[172:175], v[112:115]\n"
"ds_read_b64_tr_b16 a[188:189], v27 offset:2048\n"
"v_mfma_f32_16x16x32_bf16 v[116:119], v[196:199], a[168:171], v[116:119]\n"
"s_addk_i32 s82, 3072\n"
"ds_read_b64_tr_b16 a[190:191], v27 offset:3072\n"
"v_mfma_f32_16x16x32_bf16 v[116:119], v[204:207], a[172:175], v[116:119]\n"
"buffer_load_dwordx4 v9, s[64:67], s82 offen offset:2048 sc1 lds\n"
"v_mfma_f32_16x16x32_bf16 v[120:123], a[64:67], a[168:171], v[120:123]\n"
"v_mfma_f32_16x16x32_bf16 v[120:123], a[72:75], a[172:175], v[120:123]\n"
"s_addk_i32 s82, 3072\n"
"v_mfma_f32_16x16x32_bf16 v[124:127], a[68:71], a[168:171], v[124:127]\n"
"buffer_load_dwordx4 v9, s[64:67], s82 offen offset:3072 sc1 lds\n"
"v_mfma_f32_16x16x32_bf16 v[124:127], a[76:79], a[172:175], v[124:127]\n"
"s_waitcnt lgkmcnt(0)\n"
"v_mfma_f32_16x16x32_bf16 a[192:195], v[192:195], a[176:179], a[192:195]\n"
"s_waitcnt vmcnt(10)\n"
"s_barrier\n"
"ds_read_b64_tr_b16 a[160:161], v20 offset:0\n"
"v_mfma_f32_16x16x32_bf16 a[192:195], v[200:203], a[180:183], a[192:195]\n"
"ds_read_b64_tr_b16 a[162:163], v20 offset:1024\n"
"v_mfma_f32_16x16x32_bf16 a[196:199], v[196:199], a[176:179], a[196:199]\n"
"ds_read_b64_tr_b16 a[164:165], v20 offset:2048\n"
"v_mfma_f32_16x16x32_bf16 a[196:199], v[204:207], a[180:183], a[196:199]\n"
"ds_read_b64_tr_b16 a[166:167], v20 offset:3072\n"
"v_mfma_f32_16x16x32_bf16 a[200:203], a[64:67], a[176:179], a[200:203]\n"
"ds_read_b64_tr_b16 a[168:169], v21 offset:0\n"
"v_mfma_f32_16x16x32_bf16 a[200:203], a[72:75], a[180:183], a[200:203]\n"
"ds_read_b64_tr_b16 a[170:171], v21 offset:1024\n"
"v_mfma_f32_16x16x32_bf16 a[204:207], a[68:71], a[176:179], a[204:207]\n"
"s_addk_i32 m0, 16384\n"
"ds_read_b64_tr_b16 a[172:173], v21 offset:2048\n"
"v_mfma_f32_16x16x32_bf16 a[204:207], a[76:79], a[180:183], a[204:207]\n"
"s_add_u32 s82, s79, 4096\n"
"ds_read_b64_tr_b16 a[174:175], v21 offset:3072\n"
"v_mfma_f32_16x16x32_bf16 a[208:211], v[192:195], a[184:187], a[208:211]\n"
"buffer_load_dwordx4 v12, s[52:55], s82 offen offset:0 lds\n"
"v_mfma_f32_16x16x32_bf16 a[208:211], v[200:203], a[188:191], a[208:211]\n"
"s_add_u32 s82, s82, s13\n"
"v_mfma_f32_16x16x32_bf16 a[212:215], v[196:199], a[184:187], a[212:215]\n"
"buffer_load_dwordx4 v12, s[52:55], s82 offen offset:1024 lds\n"
"v_mfma_f32_16x16x32_bf16 a[212:215], v[204:207], a[188:191], a[212:215]\n"
"s_waitcnt lgkmcnt(0)\n"
"v_mfma_f32_16x16x32_bf16 v[96:99], v[208:211], a[160:163], v[96:99]\n"
"s_mov_b32 s88, 0xb5c1a2a0\n"
"ds_read_b64_tr_b16 a[176:177], v22 offset:0\n"
"v_lshrrev_b32 v31, 8, v9\n"
"v_mfma_f32_16x16x32_bf16 v[96:99], v[216:219], a[164:167], v[96:99]\n"
"s_mov_b32 s89, 0xbf317218\n"
"ds_read_b64_tr_b16 a[178:179], v22 offset:1024\n"
"v_mfma_f32_16x16x32_bf16 v[100:103], v[212:215], a[160:163], v[100:103]\n"
"s_mul_i32 s96, s72, 3072\n"
"s_add_u32 s96, s96, 147456\n"
"v_mfma_f32_16x16x32_bf16 v[100:103], v[220:223], a[164:167], v[100:103]\n"
"ds_read_b64_tr_b16 a[180:181], v22 offset:2048\n"
"v_and_b32 v13, 0xf0, v9\n"
"v_mfma_f32_16x16x32_bf16 v[104:107], a[80:83], a[160:163], v[104:107]\n"
"s_add_u32 s82, s82, s13\n"
"ds_read_b64_tr_b16 a[182:183], v22 offset:3072\n"
"v_lshrrev_b32 v13, 1, v13\n"
"v_mfma_f32_16x16x32_bf16 v[104:107], a[88:91], a[164:167], v[104:107]\n"
"buffer_load_dwordx4 v12, s[52:55], s82 offen offset:2048 lds\n"
"v_mfma_f32_16x16x32_bf16 v[108:111], a[84:87], a[160:163], v[108:111]\n"
"ds_read_b64_tr_b16 a[184:185], v23 offset:0\n"
"v_add_u32 v13, s96, v13\n"
"v_mfma_f32_16x16x32_bf16 v[108:111], a[92:95], a[164:167], v[108:111]\n"
"ds_read_b64_tr_b16 a[186:187], v23 offset:1024\n"
"v_lshl_add_u32 v13, v31, 7, v13\n"
"v_mfma_f32_16x16x32_bf16 v[112:115], v[208:211], a[168:171], v[112:115]\n"
"v_mfma_f32_16x16x32_bf16 v[112:115], v[216:219], a[172:175], v[112:115]\n"
"ds_read_b64_tr_b16 a[188:189], v23 offset:2048\n"
"v_mfma_f32_16x16x32_bf16 v[116:119], v[212:215], a[168:171], v[116:119]\n"
"s_add_u32 s82, s82, s13\n"
"ds_read_b64_tr_b16 a[190:191], v23 offset:3072\n"
"v_mfma_f32_16x16x32_bf16 v[116:119], v[220:223], a[172:175], v[116:119]\n"
"buffer_load_dwordx4 v12, s[52:55], s82 offen offset:3072 lds\n"
"v_mfma_f32_16x16x32_bf16 v[120:123], a[80:83], a[168:171], v[120:123]\n"
"s_add_u32 m0, s77, 32768\n"
"v_mfma_f32_16x16x32_bf16 v[120:123], a[88:91], a[172:175], v[120:123]\n"
"s_add_u32 s82, s82, s15\n"
"v_mfma_f32_16x16x32_bf16 v[124:127], a[84:87], a[168:171], v[124:127]\n"
"buffer_load_dwordx4 v12, s[52:55], s82 offen offset:0 lds\n"
"v_mfma_f32_16x16x32_bf16 v[124:127], a[92:95], a[172:175], v[124:127]\n"
"s_waitcnt lgkmcnt(0)\n"
"v_mfma_f32_16x16x32_bf16 a[192:195], v[208:211], a[176:179], a[192:195]\n"
"ds_read_b64_tr_b16 a[160:161], v20 offset:16384\n"
"v_mfma_f32_16x16x32_bf16 a[192:195], v[216:219], a[180:183], a[192:195]\n"
"ds_read_b64_tr_b16 a[162:163], v20 offset:17408\n"
"v_mfma_f32_16x16x32_bf16 a[196:199], v[212:215], a[176:179], a[196:199]\n"
"ds_read_b64_tr_b16 a[164:165], v20 offset:18432\n"
"v_mfma_f32_16x16x32_bf16 a[196:199], v[220:223], a[180:183], a[196:199]\n"
"ds_read_b64_tr_b16 a[166:167], v20 offset:19456\n"
"v_mfma_f32_16x16x32_bf16 a[200:203], a[80:83], a[176:179], a[200:203]\n"
"ds_read_b64_tr_b16 a[168:169], v21 offset:16384\n"
"v_mfma_f32_16x16x32_bf16 a[200:203], a[88:91], a[180:183], a[200:203]\n"
"ds_read_b64_tr_b16 a[170:171], v21 offset:17408\n"
"v_mfma_f32_16x16x32_bf16 a[204:207], a[84:87], a[176:179], a[204:207]\n"
"ds_read_b64_tr_b16 a[172:173], v21 offset:18432\n"
"v_mfma_f32_16x16x32_bf16 a[204:207], a[92:95], a[180:183], a[204:207]\n"
"s_add_u32 s82, s82, s13\n"
"ds_read_b64_tr_b16 a[174:175], v21 offset:19456\n"
"v_mfma_f32_16x16x32_bf16 a[208:211], v[208:211], a[184:187], a[208:211]\n"
"buffer_load_dwordx4 v12, s[52:55], s82 offen offset:1024 lds\n"
"v_mfma_f32_16x16x32_bf16 a[208:211], v[216:219], a[188:191], a[208:211]\n"
"s_add_u32 s82, s82, s13\n"
"v_mfma_f32_16x16x32_bf16 a[212:215], v[212:215], a[184:187], a[212:215]\n"
"buffer_load_dwordx4 v12, s[52:55], s82 offen offset:2048 lds\n"
"v_mfma_f32_16x16x32_bf16 a[212:215], v[220:223], a[188:191], a[212:215]\n"
"s_waitcnt lgkmcnt(0)\n"
"v_mfma_f32_16x16x32_bf16 v[96:99], v[224:227], a[160:163], v[96:99]\n"
"ds_read_b64_tr_b16 a[176:177], v22 offset:16384\n"
"v_mfma_f32_16x16x32_bf16 v[96:99], v[232:235], a[164:167], v[96:99]\n"
"ds_read_b64_tr_b16 a[178:179], v22 offset:17408\n"
"v_mfma_f32_16x16x32_bf16 v[100:103], v[228:231], a[160:163], v[100:103]\n"
"v_mfma_f32_16x16x32_bf16 v[100:103], v[236:239], a[164:167], v[100:103]\n"
"ds_read_b64_tr_b16 a[180:181], v22 offset:18432\n"
"v_mfma_f32_16x16x32_bf16 v[104:107], a[96:99], a[160:163], v[104:107]\n"
"s_add_u32 s82, s82, s13\n"
"ds_read_b64_tr_b16 a[182:183], v22 offset:19456\n"
"v_mfma_f32_16x16x32_bf16 v[104:107], a[104:107], a[164:167], v[104:107]\n"
"buffer_load_dwordx4 v12, s[52:55], s82 offen offset:3072 lds\n"
"v_mfma_f32_16x16x32_bf16 v[108:111], a[100:103], a[160:163], v[108:111]\n"
"ds_read_b64_tr_b16 a[184:185], v23 offset:16384\n"
"v_mfma_f32_16x16x32_bf16 v[108:111], a[108:111], a[164:167], v[108:111]\n"
"ds_read_b64_tr_b16 a[186:187], v23 offset:17408\n"
"v_mfma_f32_16x16x32_bf16 v[112:115], v[224:227], a[168:171], v[112:115]\n"
"v_mfma_f32_16x16x32_bf16 v[112:115], v[232:235], a[172:175], v[112:115]\n"
"s_addk_i32 m0, 16384\n"
"ds_read_b64_tr_b16 a[188:189], v23 offset:18432\n"
"v_mfma_f32_16x16x32_bf16 v[116:119], v[228:231], a[168:171], v[116:119]\n"
"s_add_u32 s82, s82, s15\n"
"ds_read_b64_tr_b16 a[190:191], v23 offset:19456\n"
"v_mfma_f32_16x16x32_bf16 v[116:119], v[236:239], a[172:175], v[116:119]\n"
"buffer_load_dwordx4 v12, s[52:55], s82 offen offset:0 lds\n"
"v_mfma_f32_16x16x32_bf16 v[120:123], a[96:99], a[168:171], v[120:123]\n"
"v_mfma_f32_16x16x32_bf16 v[120:123], a[104:107], a[172:175], v[120:123]\n"
"s_add_u32 s82, s82, s13\n"
"v_mfma_f32_16x16x32_bf16 v[124:127], a[100:103], a[168:171], v[124:127]\n"
"buffer_load_dwordx4 v12, s[52:55], s82 offen offset:1024 lds\n"
"v_mfma_f32_16x16x32_bf16 v[124:127], a[108:111], a[172:175], v[124:127]\n"
"s_waitcnt lgkmcnt(0)\n"
"v_mfma_f32_16x16x32_bf16 a[192:195], v[224:227], a[176:179], a[192:195]\n"
"s_waitcnt vmcnt(10)\n"
"s_barrier\n"
"ds_read_b64_tr_b16 a[160:161], v24 offset:16384\n"
"v_mfma_f32_16x16x32_bf16 a[192:195], v[232:235], a[180:183], a[192:195]\n"
"ds_read_b64_tr_b16 a[162:163], v24 offset:17408\n"
"v_mfma_f32_16x16x32_bf16 a[196:199], v[228:231], a[176:179], a[196:199]\n"
"ds_read_b64_tr_b16 a[164:165], v24 offset:18432\n"
"v_mfma_f32_16x16x32_bf16 a[196:199], v[236:239], a[180:183], a[196:199]\n"
"ds_read_b64_tr_b16 a[166:167], v24 offset:19456\n"
"v_mfma_f32_16x16x32_bf16 a[200:203], a[96:99], a[176:179], a[200:203]\n"
"ds_read_b64_tr_b16 a[168:169], v25 offset:16384\n"
"v_mfma_f32_16x16x32_bf16 a[200:203], a[104:107], a[180:183], a[200:203]\n"
"ds_read_b64_tr_b16 a[170:171], v25 offset:17408\n"
"v_mfma_f32_16x16x32_bf16 a[204:207], a[100:103], a[176:179], a[204:207]\n"
"ds_read_b64_tr_b16 a[172:173], v25 offset:18432\n"
"v_mfma_f32_16x16x32_bf16 a[204:207], a[108:111], a[180:183], a[204:207]\n"
"s_add_u32 s82, s82, s13\n"
"ds_read_b64_tr_b16 a[174:175], v25 offset:19456\n"
"v_mfma_f32_16x16x32_bf16 a[208:211], v[224:227], a[184:187], a[208:211]\n"
"buffer_load_dwordx4 v12, s[52:55], s82 offen offset:2048 lds\n"
"v_mfma_f32_16x16x32_bf16 a[208:211], v[232:235], a[188:191], a[208:211]\n"
"s_add_u32 s82, s82, s13\n"
"v_mfma_f32_16x16x32_bf16 a[212:215], v[228:231], a[184:187], a[212:215]\n"
"buffer_load_dwordx4 v12, s[52:55], s82 offen offset:3072 lds\n"
"v_mfma_f32_16x16x32_bf16 a[212:215], v[236:239], a[188:191], a[212:215]\n"
"s_waitcnt lgkmcnt(0)\n"
"v_mfma_f32_16x16x32_bf16 v[96:99], v[240:243], a[160:163], v[96:99]\n"
"ds_read_b64_tr_b16 a[176:177], v26 offset:16384\n"
"v_mfma_f32_16x16x32_bf16 v[96:99], v[248:251], a[164:167], v[96:99]\n"
"ds_read_b64_tr_b16 a[178:179], v26 offset:17408\n"
"v_mfma_f32_16x16x32_bf16 v[100:103], v[244:247], a[160:163], v[100:103]\n"
"v_mfma_f32_16x16x32_bf16 v[100:103], v[252:255], a[164:167], v[100:103]\n"
"s_addk_i32 m0, 16384\n"
"ds_read_b64_tr_b16 a[180:181], v26 offset:18432\n"
"v_mfma_f32_16x16x32_bf16 v[104:107], a[112:115], a[160:163], v[104:107]\n"
"s_add_u32 s82, s82, s15\n"
"ds_read_b64_tr_b16 a[182:183], v26 offset:19456\n"
"v_mfma_f32_16x16x32_bf16 v[104:107], a[120:123], a[164:167], v[104:107]\n"
"buffer_load_dwordx4 v12, s[52:55], s82 offen offset:0 lds\n"
"v_mfma_f32_16x16x32_bf16 v[108:111], a[116:119], a[160:163], v[108:111]\n"
"ds_read_b64_tr_b16 a[184:185], v27 offset:16384\n"
"v_mfma_f32_16x16x32_bf16 v[108:111], a[124:127], a[164:167], v[108:111]\n"
"ds_read_b64_tr_b16 a[186:187], v27 offset:17408\n"
"v_mfma_f32_16x16x32_bf16 v[112:115], v[240:243], a[168:171], v[112:115]\n"
"v_mfma_f32_16x16x32_bf16 v[112:115], v[248:251], a[172:175], v[112:115]\n"
"ds_read_b64_tr_b16 a[188:189], v27 offset:18432\n"
"v_mfma_f32_16x16x32_bf16 v[116:119], v[244:247], a[168:171], v[116:119]\n"
"s_add_u32 s82, s82, s13\n"
"ds_read_b64_tr_b16 a[190:191], v27 offset:19456\n"
"v_mfma_f32_16x16x32_bf16 v[116:119], v[252:255], a[172:175], v[116:119]\n"
"buffer_load_dwordx4 v12, s[52:55], s82 offen offset:1024 lds\n"
"v_mfma_f32_16x16x32_bf16 v[120:123], a[112:115], a[168:171], v[120:123]\n"
"v_mfma_f32_16x16x32_bf16 v[120:123], a[120:123], a[172:175], v[120:123]\n"
"s_add_u32 s82, s82, s13\n"
"v_mfma_f32_16x16x32_bf16 v[124:127], a[116:119], a[168:171], v[124:127]\n"
"buffer_load_dwordx4 v12, s[52:55], s82 offen offset:2048 lds\n"
"v_mfma_f32_16x16x32_bf16 v[124:127], a[124:127], a[172:175], v[124:127]\n"
"s_waitcnt lgkmcnt(0)\n"
"v_mfma_f32_16x16x32_bf16 a[192:195], v[240:243], a[176:179], a[192:195]\n"
"ds_read_b64_tr_b16 a[160:161], v24 offset:32768\n"
"v_mfma_f32_16x16x32_bf16 a[192:195], v[248:251], a[180:183], a[192:195]\n"
"ds_read_b64_tr_b16 a[162:163], v24 offset:33792\n"
"v_mfma_f32_16x16x32_bf16 a[196:199], v[244:247], a[176:179], a[196:199]\n"
"ds_read_b64_tr_b16 a[164:165], v24 offset:34816\n"
"v_mfma_f32_16x16x32_bf16 a[196:199], v[252:255], a[180:183], a[196:199]\n"
"ds_read_b64_tr_b16 a[166:167], v24 offset:35840\n"
"v_mfma_f32_16x16x32_bf16 a[200:203], a[112:115], a[176:179], a[200:203]\n"
"ds_read_b64_tr_b16 a[168:169], v25 offset:32768\n"
"v_mfma_f32_16x16x32_bf16 a[200:203], a[120:123], a[180:183], a[200:203]\n"
"ds_read_b64_tr_b16 a[170:171], v25 offset:33792\n"
"v_mfma_f32_16x16x32_bf16 a[204:207], a[116:119], a[176:179], a[204:207]\n"
"ds_read_b64_tr_b16 a[172:173], v25 offset:34816\n"
"v_mfma_f32_16x16x32_bf16 a[204:207], a[124:127], a[180:183], a[204:207]\n"
"s_add_u32 s82, s82, s13\n"
"ds_read_b64_tr_b16 a[174:175], v25 offset:35840\n"
"v_mfma_f32_16x16x32_bf16 a[208:211], v[240:243], a[184:187], a[208:211]\n"
"buffer_load_dwordx4 v12, s[52:55], s82 offen offset:3072 lds\n"
"v_mfma_f32_16x16x32_bf16 a[208:211], v[248:251], a[188:191], a[208:211]\n"
"s_add_u32 m0, s77, 0\n"
"s_add_u32 s82, s82, s15\n"
"v_mfma_f32_16x16x32_bf16 a[212:215], v[244:247], a[184:187], a[212:215]\n"
"buffer_load_dwordx4 v12, s[52:55], s82 offen offset:0 lds\n"
"v_mfma_f32_16x16x32_bf16 a[212:215], v[252:255], a[188:191], a[212:215]\n"
"s_waitcnt lgkmcnt(0)\n"
"v_mfma_f32_16x16x32_bf16 v[96:99], a[144:147], a[160:163], v[96:99]\n"
"ds_read_b64_tr_b16 a[176:177], v26 offset:32768\n"
"v_mfma_f32_16x16x32_bf16 v[96:99], a[152:155], a[164:167], v[96:99]\n"
"ds_read_b64_tr_b16 a[178:179], v26 offset:33792\n"
"v_mfma_f32_16x16x32_bf16 v[100:103], a[148:151], a[160:163], v[100:103]\n"
"v_mfma_f32_16x16x32_bf16 v[100:103], a[156:159], a[164:167], v[100:103]\n"
"ds_read_b64_tr_b16 a[180:181], v26 offset:34816\n"
"v_mfma_f32_16x16x32_bf16 v[104:107], a[128:131], a[160:163], v[104:107]\n"
"s_add_u32 s82, s82, s13\n"
"ds_read_b64_tr_b16 a[182:183], v26 offset:35840\n"
"s_nop 0\n"
"v_cvt_pk_bf16_f32 v20, v96, v97\n"
"v_mfma_f32_16x16x32_bf16 v[104:107], a[136:139], a[164:167], v[104:107]\n"
"buffer_load_dwordx4 v12, s[52:55], s82 offen offset:1024 lds\n"
"v_mfma_f32_16x16x32_bf16 v[108:111], a[132:135], a[160:163], v[108:111]\n"
"ds_read_b64_tr_b16 a[184:185], v27 offset:32768\n"
"v_cvt_pk_bf16_f32 v21, v98, v99\n"
"v_mfma_f32_16x16x32_bf16 v[108:111], a[140:143], a[164:167], v[108:111]\n"
"ds_read_b64_tr_b16 a[186:187], v27 offset:33792\n"
"v_cvt_pk_bf16_f32 v22, v100, v101\n"
"v_mfma_f32_16x16x32_bf16 v[112:115], a[144:147], a[168:171], v[112:115]\n"
"v_cvt_pk_bf16_f32 v23, v102, v103\n"
"v_cvt_pk_bf16_f32 v24, v104, v105\n"
"v_mfma_f32_16x16x32_bf16 v[112:115], a[152:155], a[172:175], v[112:115]\n"
"ds_read_b64_tr_b16 a[188:189], v27 offset:34816\n"
"v_cvt_pk_bf16_f32 v25, v106, v107\n"
"v_mfma_f32_16x16x32_bf16 v[116:119], a[148:151], a[168:171], v[116:119]\n"
"s_add_u32 s82, s78, 2176\n"
"ds_read_b64_tr_b16 a[190:191], v27 offset:35840\n"
"v_mfma_f32_16x16x32_bf16 v[116:119], a[156:159], a[172:175], v[116:119]\n"
"buffer_load_dwordx4 v11, s[44:47], s82 offen offset:2048 lds\n"
"v_mfma_f32_16x16x32_bf16 v[120:123], a[128:131], a[168:171], v[120:123]\n"
"v_cvt_pk_bf16_f32 v26, v108, v109\n"
"v_cvt_pk_bf16_f32 v27, v110, v111\n"
"v_mfma_f32_16x16x32_bf16 v[120:123], a[136:139], a[172:175], v[120:123]\n"
"s_add_u32 s82, s82, s13\n"
"v_min_f32 v16, s88, v112\n"
"v_min_f32 v17, s88, v113\n"
"v_mfma_f32_16x16x32_bf16 v[124:127], a[132:135], a[168:171], v[124:127]\n"
"buffer_load_dwordx4 v11, s[44:47], s82 offen offset:3072 lds\n"
"v_mfma_f32_16x16x32_bf16 v[124:127], a[140:143], a[172:175], v[124:127]\n"
"s_waitcnt lgkmcnt(0)\n"
"v_exp_f32 v112, -v112 clamp\n"
"v_mfma_f32_16x16x32_bf16 a[192:195], a[144:147], a[176:179], a[192:195]\n"
"s_waitcnt vmcnt(12)\n"
"s_barrier\n"
"ds_read_b64_tr_b16 a[160:161], v8 offset:49152\n"
"v_mfma_f32_16x16x32_bf16 a[192:195], a[152:155], a[180:183], a[192:195]\n"
"ds_read_b64_tr_b16 a[162:163], v8 offset:53248\n"
"v_fmac_f32 v112, s89, v16\n"
"v_mfma_f32_16x16x32_bf16 a[196:199], a[148:151], a[176:179], a[196:199]\n"
"ds_read_b64_tr_b16 a[164:165], v8 offset:50176\n"
"v_min_f32 v18, s88, v114\n"
"v_mfma_f32_16x16x32_bf16 a[196:199], a[156:159], a[180:183], a[196:199]\n"
"ds_read_b64_tr_b16 a[166:167], v8 offset:54272\n"
"v_min_f32 v19, s88, v115\n"
"v_mfma_f32_16x16x32_bf16 a[200:203], a[128:131], a[176:179], a[200:203]\n"
"ds_read_b64_tr_b16 a[168:169], v8 offset:57344\n"
"v_min_f32 v16, s88, v116\n"
"v_mfma_f32_16x16x32_bf16 a[200:203], a[136:139], a[180:183], a[200:203]\n"
"ds_read_b64_tr_b16 a[170:171], v8 offset:61440\n"
"v_accvgpr_read_b32 v32, a192\n"
"v_mfma_f32_16x16x32_bf16 a[204:207], a[132:135], a[176:179], a[204:207]\n"
"s_addk_i32 m0, 16384\n"
"ds_read_b64_tr_b16 a[172:173], v8 offset:58368\n"
"v_accvgpr_read_b32 v33, a194\n"
"v_mfma_f32_16x16x32_bf16 a[204:207], a[140:143], a[180:183], a[204:207]\n"
"s_add_u32 s82, s82, s15\n"
"ds_read_b64_tr_b16 a[174:175], v8 offset:62464\n"
"v_mfma_f32_16x16x32_bf16 a[208:211], a[144:147], a[184:187], a[208:211]\n"
"buffer_load_dwordx4 v11, s[44:47], s82 offen offset:0 lds\n"
"v_mfma_f32_16x16x32_bf16 a[208:211], a[152:155], a[188:191], a[208:211]\n"
"s_add_u32 s82, s82, s13\n"
"v_exp_f32 v113, -v113 clamp\n"
"v_mfma_f32_16x16x32_bf16 a[212:215], a[148:151], a[184:187], a[212:215]\n"
"buffer_load_dwordx4 v11, s[44:47], s82 offen offset:1024 lds\n"
"v_mfma_f32_16x16x32_bf16 a[212:215], a[156:159], a[188:191], a[212:215]\n"
"s_waitcnt lgkmcnt(0)\n"
"v_fmac_f32 v113, s89, v17\n"
"v_min_f32 v17, s88, v117\n"
"v_mfma_f32_16x16x32_bf16 v[64:67], a[160:163], v[128:131], 0\n"
"ds_read_b64_tr_b16 a[176:177], v8 offset:51200\n"
"v_mfma_f32_16x16x32_bf16 v[64:67], a[164:167], v[136:139], v[64:67]\n"
"ds_read_b64_tr_b16 a[178:179], v8 offset:55296\n"
"v_mfma_f32_16x16x32_bf16 v[68:71], a[160:163], v[132:135], 0\n"
"v_exp_f32 v114, -v114 clamp\n"
"v_mfma_f32_16x16x32_bf16 v[68:71], a[164:167], v[140:143], v[68:71]\n"
"ds_read_b64_tr_b16 a[180:181], v8 offset:52224\n"
"v_fmac_f32 v114, s89, v18\n"
"v_mfma_f32_16x16x32_bf16 v[72:75], a[160:163], a[0:3], 0\n"
"s_add_u32 s82, s82, s13\n"
"ds_read_b64_tr_b16 a[182:183], v8 offset:56320\n"
"v_min_f32 v18, s88, v118\n"
"v_mfma_f32_16x16x32_bf16 v[72:75], a[164:167], a[8:11], v[72:75]\n"
"buffer_load_dwordx4 v11, s[44:47], s82 offen offset:2048 lds\n"
"v_mfma_f32_16x16x32_bf16 v[76:79], a[160:163], a[4:7], 0\n"
"ds_read_b64_tr_b16 a[184:185], v8 offset:59392\n"
"v_mfma_f32_16x16x32_bf16 v[76:79], a[164:167], a[12:15], v[76:79]\n"
"ds_read_b64_tr_b16 a[186:187], v8 offset:63488\n"
"v_mfma_f32_16x16x32_bf16 v[80:83], a[168:171], v[128:131], 0\n"
"v_exp_f32 v115, -v115 clamp\n"
"v_mfma_f32_16x16x32_bf16 v[80:83], a[172:175], v[136:139], v[80:83]\n"
"ds_read_b64_tr_b16 a[188:189], v8 offset:60416\n"
"v_fmac_f32 v115, s89, v19\n"
"v_mfma_f32_16x16x32_bf16 v[84:87], a[168:171], v[132:135], 0\n"
"s_add_u32 s82, s82, s13\n"
"ds_read_b64_tr_b16 a[190:191], v8 offset:64512\n"
"v_min_f32 v19, s88, v119\n"
"v_mfma_f32_16x16x32_bf16 v[84:87], a[172:175], v[140:143], v[84:87]\n"
"buffer_load_dwordx4 v11, s[44:47], s82 offen offset:3072 lds\n"
"v_mfma_f32_16x16x32_bf16 v[88:91], a[168:171], a[0:3], 0\n"
"s_add_u32 m0, s77, 81920\n"
"v_exp_f32 v116, -v116 clamp\n"
"v_mfma_f32_16x16x32_bf16 v[88:91], a[172:175], a[8:11], v[88:91]\n"
"s_add_u32 s82, s82, s15\n"
"v_fmac_f32 v116, s89, v16\n"
"v_min_f32 v16, s88, v120\n"
"v_mfma_f32_16x16x32_bf16 v[92:95], a[168:171], a[4:7], 0\n"
"buffer_load_dwordx4 v11, s[44:47], s82 offen offset:0 lds\n"
"v_mfma_f32_16x16x32_bf16 v[92:95], a[172:175], a[12:15], v[92:95]\n"
"s_waitcnt lgkmcnt(0)\n"
"v_exp_f32 v117, -v117 clamp\n"
"v_mfma_f32_16x16x32_bf16 v[64:67], a[176:179], v[144:147], v[64:67]\n"
"ds_read_b64_tr_b16 a[160:161], v7 offset:32768\n"
"v_fmac_f32 v117, s89, v17\n"
"v_mfma_f32_16x16x32_bf16 v[64:67], a[180:183], v[152:155], v[64:67]\n"
"ds_read_b64_tr_b16 a[162:163], v7 offset:36864\n"
"v_min_f32 v17, s88, v121\n"
"v_mfma_f32_16x16x32_bf16 v[68:71], a[176:179], v[148:151], v[68:71]\n"
"v_exp_f32 v118, -v118 clamp\n"
"v_mfma_f32_16x16x32_bf16 v[68:71], a[180:183], v[156:159], v[68:71]\n"
"ds_read_b64_tr_b16 a[164:165], v7 offset:33792\n"
"v_fmac_f32 v118, s89, v18\n"
"v_mfma_f32_16x16x32_bf16 v[72:75], a[176:179], a[16:19], v[72:75]\n"
"s_add_u32 s82, s82, s13\n"
"ds_read_b64_tr_b16 a[166:167], v7 offset:37888\n"
"v_min_f32 v18, s88, v122\n"
"v_mfma_f32_16x16x32_bf16 v[72:75], a[180:183], a[24:27], v[72:75]\n"
"buffer_load_dwordx4 v11, s[44:47], s82 offen offset:1024 lds\n"
"v_mfma_f32_16x16x32_bf16 v[76:79], a[176:179], a[20:23], v[76:79]\n"
"ds_read_b64_tr_b16 a[168:169], v7 offset:40960\n"
"v_mfma_f32_16x16x32_bf16 v[76:79], a[180:183], a[28:31], v[76:79]\n"
"ds_read_b64_tr_b16 a[170:171], v7 offset:45056\n"
"v_mfma_f32_16x16x32_bf16 v[80:83], a[184:187], v[144:147], v[80:83]\n"
"v_exp_f32 v119, -v119 clamp\n"
"v_mfma_f32_16x16x32_bf16 v[80:83], a[188:191], v[152:155], v[80:83]\n"
"ds_read_b64_tr_b16 a[172:173], v7 offset:41984\n"
"v_fmac_f32 v119, s89, v19\n"
"v_mfma_f32_16x16x32_bf16 v[84:87], a[184:187], v[148:151], v[84:87]\n"
"s_add_u32 s82, s82, s13\n"
"ds_read_b64_tr_b16 a[174:175], v7 offset:46080\n"
"v_min_f32 v19, s88, v123\n"
"v_mfma_f32_16x16x32_bf16 v[84:87], a[188:191], v[156:159], v[84:87]\n"
"buffer_load_dwordx4 v11, s[44:47], s82 offen offset:2048 lds\n"
"v_mfma_f32_16x16x32_bf16 v[88:91], a[184:187], a[16:19], v[88:91]\n"
"v_exp_f32 v120, -v120 clamp\n"
"v_mfma_f32_16x16x32_bf16 v[88:91], a[188:191], a[24:27], v[88:91]\n"
"s_add_u32 s82, s82, s13\n"
"v_fmac_f32 v120, s89, v16\n"
"v_min_f32 v16, s88, v124\n"
"v_mfma_f32_16x16x32_bf16 v[92:95], a[184:187], a[20:23], v[92:95]\n"
"buffer_load_dwordx4 v11, s[44:47], s82 offen offset:3072 lds\n"
"v_mfma_f32_16x16x32_bf16 v[92:95], a[188:191], a[28:31], v[92:95]\n"
"s_waitcnt lgkmcnt(0)\n"
"v_exp_f32 v121, -v121 clamp\n"
"v_mfma_f32_16x16x32_bf16 v[64:67], a[160:163], v[160:163], v[64:67]\n"
"ds_read_b64_tr_b16 a[176:177], v7 offset:34816\n"
"v_fmac_f32 v121, s89, v17\n"
"v_mfma_f32_16x16x32_bf16 v[64:67], a[164:167], v[168:171], v[64:67]\n"
"ds_read_b64_tr_b16 a[178:179], v7 offset:38912\n"
"v_min_f32 v17, s88, v125\n"
"v_mfma_f32_16x16x32_bf16 v[68:71], a[160:163], v[164:167], v[68:71]\n"
"v_exp_f32 v122, -v122 clamp\n"
"v_mfma_f32_16x16x32_bf16 v[68:71], a[164:167], v[172:175], v[68:71]\n"
"s_addk_i32 m0, 16384\n"
"ds_read_b64_tr_b16 a[180:181], v7 offset:35840\n"
"v_fmac_f32 v122, s89, v18\n"
"v_mfma_f32_16x16x32_bf16 v[72:75], a[160:163], a[32:35], v[72:75]\n"
"s_add_u32 s82, s82, s15\n"
"ds_read_b64_tr_b16 a[182:183], v7 offset:39936\n"
"v_min_f32 v18, s88, v126\n"
"v_mfma_f32_16x16x32_bf16 v[72:75], a[164:167], a[40:43], v[72:75]\n"
"buffer_load_dwordx4 v11, s[44:47], s82 offen offset:0 lds\n"
"v_mfma_f32_16x16x32_bf16 v[76:79], a[160:163], a[36:39], v[76:79]\n"
"ds_read_b64_tr_b16 a[184:185], v7 offset:43008\n"
"v_mfma_f32_16x16x32_bf16 v[76:79], a[164:167], a[44:47], v[76:79]\n"
"ds_read_b64_tr_b16 a[186:187], v7 offset:47104\n"
"v_mfma_f32_16x16x32_bf16 v[80:83], a[168:171], v[160:163], v[80:83]\n"
"v_exp_f32 v123, -v123 clamp\n"
"v_mfma_f32_16x16x32_bf16 v[80:83], a[172:175], v[168:171], v[80:83]\n"
"ds_read_b64_tr_b16 a[188:189], v7 offset:44032\n"
"v_fmac_f32 v123, s89, v19\n"
"v_mfma_f32_16x16x32_bf16 v[84:87], a[168:171], v[164:167], v[84:87]\n"
"s_add_u32 s82, s82, s13\n"
"ds_read_b64_tr_b16 a[190:191], v7 offset:48128\n"
"v_min_f32 v19, s88, v127\n"
"v_mfma_f32_16x16x32_bf16 v[84:87], a[172:175], v[172:175], v[84:87]\n"
"buffer_load_dwordx4 v11, s[44:47], s82 offen offset:1024 lds\n"
"v_mfma_f32_16x16x32_bf16 v[88:91], a[168:171], a[32:35], v[88:91]\n"
"v_exp_f32 v124, -v124 clamp\n"
"v_mfma_f32_16x16x32_bf16 v[88:91], a[172:175], a[40:43], v[88:91]\n"
"s_add_u32 s82, s82, s13\n"
"v_fmac_f32 v124, s89, v16\n"
"v_cvt_pk_bf16_f32 v112, v112, v113\n"
"v_mfma_f32_16x16x32_bf16 v[92:95], a[168:171], a[36:39], v[92:95]\n"
"buffer_load_dwordx4 v11, s[44:47], s82 offen offset:2048 lds\n"
"v_mfma_f32_16x16x32_bf16 v[92:95], a[172:175], a[44:47], v[92:95]\n"
"s_waitcnt lgkmcnt(0)\n"
"v_exp_f32 v125, -v125 clamp\n"
"v_mfma_f32_16x16x32_bf16 v[64:67], a[176:179], v[176:179], v[64:67]\n"
"s_waitcnt vmcnt(15)\n"
"s_barrier\n"
"ds_read_b64_tr_b16 a[160:161], v7 offset:49152\n"
"v_mfma_f32_16x16x32_bf16 v[64:67], a[180:183], v[184:187], v[64:67]\n"
"s_lshl_b32 s96, s72, 11\n"
"s_add_u32 s96, s96, 114688\n"
"ds_read_b64_tr_b16 a[162:163], v7 offset:53248\n"
"v_mfma_f32_16x16x32_bf16 v[68:71], a[176:179], v[180:183], v[68:71]\n"
"v_add_u32 v29, s96, v9\n"
"v_accvgpr_read_b32 v56, a208\n"
"v_mfma_f32_16x16x32_bf16 v[68:71], a[180:183], v[188:191], v[68:71]\n"
"ds_read_b64_tr_b16 a[164:165], v7 offset:50176\n"
"v_accvgpr_read_b32 v57, a209\n"
"v_mfma_f32_16x16x32_bf16 v[72:75], a[176:179], a[48:51], v[72:75]\n"
"ds_read_b64_tr_b16 a[166:167], v7 offset:54272\n"
"v_accvgpr_read_b32 v58, a210\n"
"v_mfma_f32_16x16x32_bf16 v[72:75], a[180:183], a[56:59], v[72:75]\n"
"v_accvgpr_read_b32 v59, a211\n"
"v_accvgpr_read_b32 v60, a212\n"
"v_mfma_f32_16x16x32_bf16 v[76:79], a[176:179], a[52:55], v[76:79]\n"
"ds_read_b64_tr_b16 a[168:169], v7 offset:57344\n"
"v_accvgpr_read_b32 v61, a213\n"
"v_mfma_f32_16x16x32_bf16 v[76:79], a[180:183], a[60:63], v[76:79]\n"
"s_add_u32 s82, s82, s13\n"
"ds_read_b64_tr_b16 a[170:171], v7 offset:61440\n"
"v_accvgpr_read_b32 v62, a214\n"
"v_mfma_f32_16x16x32_bf16 v[80:83], a[184:187], v[176:179], v[80:83]\n"
"buffer_load_dwordx4 v11, s[44:47], s82 offen offset:3072 lds\n"
"v_mfma_f32_16x16x32_bf16 v[80:83], a[188:191], v[184:187], v[80:83]\n"
"ds_read_b64_tr_b16 a[172:173], v7 offset:58368\n"
"v_accvgpr_read_b32 v63, a215\n"
"v_mfma_f32_16x16x32_bf16 v[84:87], a[184:187], v[180:183], v[84:87]\n"
"ds_read_b64_tr_b16 a[174:175], v7 offset:62464\n"
"v_cvt_pk_bf16_f32 v40, v56, v57\n"
"v_mfma_f32_16x16x32_bf16 v[84:87], a[188:191], v[188:191], v[84:87]\n"
"s_add_u32 m0, s77, 32768\n"
"v_cvt_pk_bf16_f32 v41, v58, v59\n"
"v_min_f32 v16, s88, v56\n"
"v_mfma_f32_16x16x32_bf16 v[88:91], a[184:187], a[48:51], v[88:91]\n"
"s_add_u32 s82, s82, s15\n"
"v_cvt_pk_bf16_f32 v42, v60, v61\n"
"v_cvt_pk_bf16_f32 v43, v62, v63\n"
"v_mfma_f32_16x16x32_bf16 v[88:91], a[188:191], a[56:59], v[88:91]\n"
"buffer_load_dwordx4 v11, s[44:47], s82 offen offset:0 lds\n"
"v_mfma_f32_16x16x32_bf16 v[92:95], a[184:187], a[52:55], v[92:95]\n"
"s_add_u32 s96, s77, 114688\n"
"ds_write_b128 v29, v[40:43] offset:0\n"
"v_mfma_f32_16x16x32_bf16 v[92:95], a[188:191], a[60:63], v[92:95]\n"
"s_waitcnt lgkmcnt(1)\n"
"v_exp_f32 v56, -v56 clamp\n"
"v_mfma_f32_16x16x32_bf16 v[64:67], a[160:163], v[192:195], v[64:67]\n"
"ds_read_b64_tr_b16 a[176:177], v7 offset:51200\n"
"v_fmac_f32 v125, s89, v17\n"
"v_mfma_f32_16x16x32_bf16 v[64:67], a[164:167], v[200:203], v[64:67]\n"
"ds_read_b64_tr_b16 a[178:179], v7 offset:55296\n"
"v_min_f32 v17, s88, v57\n"
"v_mfma_f32_16x16x32_bf16 v[68:71], a[160:163], v[196:199], v[68:71]\n"
"v_exp_f32 v126, -v126 clamp\n"
"v_mfma_f32_16x16x32_bf16 v[68:71], a[164:167], v[204:207], v[68:71]\n"
"ds_read_b64_tr_b16 a[180:181], v7 offset:52224\n"
"v_fmac_f32 v56, s89, v16\n"
"v_mfma_f32_16x16x32_bf16 v[72:75], a[160:163], a[64:67], v[72:75]\n"
"s_add_u32 s82, s82, s13\n"
"ds_read_b64_tr_b16 a[182:183], v7 offset:56320\n"
"v_fmac_f32 v126, s89, v18\n"
"v_mfma_f32_16x16x32_bf16 v[72:75], a[164:167], a[72:75], v[72:75]\n"
"buffer_load_dwordx4 v11, s[44:47], s82 offen offset:1024 lds\n"
"v_mfma_f32_16x16x32_bf16 v[76:79], a[160:163], a[68:71], v[76:79]\n"
"ds_read_b64_tr_b16 a[184:185], v7 offset:59392\n"
"v_min_f32 v18, s88, v58\n"
"v_mfma_f32_16x16x32_bf16 v[76:79], a[164:167], a[76:79], v[76:79]\n"
"ds_read_b64_tr_b16 a[186:187], v7 offset:63488\n"
"v_min_f32 v16, s88, v60\n"
"v_mfma_f32_16x16x32_bf16 v[80:83], a[168:171], v[192:195], v[80:83]\n"
"v_exp_f32 v57, -v57 clamp\n"
"v_mfma_f32_16x16x32_bf16 v[80:83], a[172:175], v[200:203], v[80:83]\n"
"ds_read_b64_tr_b16 a[188:189], v7 offset:60416\n"
"v_fmac_f32 v57, s89, v17\n"
"v_mfma_f32_16x16x32_bf16 v[84:87], a[168:171], v[196:199], v[84:87]\n"
"s_add_u32 s82, s82, s13\n"
"ds_read_b64_tr_b16 a[190:191], v7 offset:64512\n"
"v_min_f32 v17, s88, v61\n"
"v_mfma_f32_16x16x32_bf16 v[84:87], a[172:175], v[204:207], v[84:87]\n"
"buffer_load_dwordx4 v11, s[44:47], s82 offen offset:2048 lds\n"
"v_mfma_f32_16x16x32_bf16 v[88:91], a[168:171], a[64:67], v[88:91]\n"
"v_exp_f32 v127, -v127 clamp\n"
"v_mfma_f32_16x16x32_bf16 v[88:91], a[172:175], a[72:75], v[88:91]\n"
"s_add_u32 s82, s82, s13\n"
"v_exp_f32 v58, -v58 clamp\n"
"v_mfma_f32_16x16x32_bf16 v[92:95], a[168:171], a[68:71], v[92:95]\n"
"buffer_load_dwordx4 v11, s[44:47], s82 offen offset:3072 lds\n"
"v_mfma_f32_16x16x32_bf16 v[92:95], a[172:175], a[76:79], v[92:95]\n"
"s_waitcnt lgkmcnt(0)\n"
"v_fmac_f32 v127, s89, v19\n"
"v_min_f32 v19, s88, v59\n"
"v_mfma_f32_16x16x32_bf16 v[64:67], a[176:179], v[208:211], v[64:67]\n"
"ds_read_b64_tr_b16 a[160:161], v8 offset:0\n"
"v_fmac_f32 v58, s89, v18\n"
"v_mfma_f32_16x16x32_bf16 v[64:67], a[180:183], v[216:219], v[64:67]\n"
"ds_read_b64_tr_b16 a[162:163], v8 offset:4096\n"
"v_min_f32 v18, s88, v62\n"
"v_mfma_f32_16x16x32_bf16 v[68:71], a[176:179], v[212:215], v[68:71]\n"
"v_exp_f32 v59, -v59 clamp\n"
"v_mfma_f32_16x16x32_bf16 v[68:71], a[180:183], v[220:223], v[68:71]\n"
"ds_read_b64_tr_b16 a[164:165], v8 offset:1024\n"
"v_fmac_f32 v59, s89, v19\n"
"v_mfma_f32_16x16x32_bf16 v[72:75], a[176:179], a[80:83], v[72:75]\n"
"ds_read_b64_tr_b16 a[166:167], v8 offset:5120\n"
"v_min_f32 v19, s88, v63\n"
"v_mfma_f32_16x16x32_bf16 v[72:75], a[180:183], a[88:91], v[72:75]\n"
"v_exp_f32 v60, -v60 clamp\n"
"v_mfma_f32_16x16x32_bf16 v[76:79], a[176:179], a[84:87], v[76:79]\n"
"ds_read_b64_tr_b16 a[168:169], v8 offset:8192\n"
"v_fmac_f32 v60, s89, v16\n"
"v_mfma_f32_16x16x32_bf16 v[76:79], a[180:183], a[92:95], v[76:79]\n"
"ds_read_b64_tr_b16 a[170:171], v8 offset:12288\n"
"v_cvt_pk_bf16_f32 v48, v56, v57\n"
"v_mfma_f32_16x16x32_bf16 v[80:83], a[184:187], v[208:211], v[80:83]\n"
"v_exp_f32 v61, -v61 clamp\n"
"v_mfma_f32_16x16x32_bf16 v[80:83], a[188:191], v[216:219], v[80:83]\n"
"ds_read_b64_tr_b16 a[172:173], v8 offset:9216\n"
"v_fmac_f32 v61, s89, v17\n"
"v_mfma_f32_16x16x32_bf16 v[84:87], a[184:187], v[212:215], v[84:87]\n"
"ds_read_b64_tr_b16 a[174:175], v8 offset:13312\n"
"v_min_f32 v16, s88, v32\n"
"v_mfma_f32_16x16x32_bf16 v[84:87], a[188:191], v[220:223], v[84:87]\n"
"v_exp_f32 v62, -v62 clamp\n"
"v_mfma_f32_16x16x32_bf16 v[88:91], a[184:187], a[80:83], v[88:91]\n"
"v_fmac_f32 v62, s89, v18\n"
"v_accvgpr_read_b32 v56, a193\n"
"v_mfma_f32_16x16x32_bf16 v[88:91], a[188:191], a[88:91], v[88:91]\n"
"v_exp_f32 v63, -v63 clamp\n"
"v_mfma_f32_16x16x32_bf16 v[92:95], a[184:187], a[84:87], v[92:95]\n"
"v_fmac_f32 v63, s89, v19\n"
"v_cvt_pk_bf16_f32 v49, v58, v59\n"
"v_mfma_f32_16x16x32_bf16 v[92:95], a[188:191], a[92:95], v[92:95]\n"
"s_waitcnt lgkmcnt(0)\n"
"v_exp_f32 v32, -v32 clamp\n"
"v_mfma_f32_16x16x32_bf16 v[64:67], a[160:163], v[224:227], v[64:67]\n"
"ds_read_b64_tr_b16 a[176:177], v8 offset:2048\n"
"v_cvt_pk_bf16_f32 v50, v60, v61\n"
"v_mfma_f32_16x16x32_bf16 v[64:67], a[164:167], v[232:235], v[64:67]\n"
"ds_read_b64_tr_b16 a[178:179], v8 offset:6144\n"
"v_min_f32 v17, s88, v56\n"
"v_mfma_f32_16x16x32_bf16 v[68:71], a[160:163], v[228:231], v[68:71]\n"
"v_cvt_pk_bf16_f32 v51, v62, v63\n"
"v_fmac_f32 v32, s89, v16\n"
"v_mfma_f32_16x16x32_bf16 v[68:71], a[164:167], v[236:239], v[68:71]\n"
"ds_read_b64_tr_b16 a[180:181], v8 offset:3072\n"
"v_min_f32 v18, s88, v33\n"
"v_mfma_f32_16x16x32_bf16 v[72:75], a[160:163], a[96:99], v[72:75]\n"
"ds_read_b64_tr_b16 a[182:183], v8 offset:7168\n"
"v_accvgpr_read_b32 v57, a195\n"
"v_mfma_f32_16x16x32_bf16 v[72:75], a[164:167], a[104:107], v[72:75]\n"
"ds_write_b128 v29, v[48:51] offset:1024\n"
"v_mfma_f32_16x16x32_bf16 v[76:79], a[160:163], a[100:103], v[76:79]\n"
"ds_read_b64_tr_b16 a[184:185], v8 offset:10240\n"
"v_xor_b32 v29, 0x800, v29\n"
"v_mfma_f32_16x16x32_bf16 v[76:79], a[164:167], a[108:111], v[76:79]\n"
"ds_read_b64_tr_b16 a[186:187], v8 offset:14336\n"
"v_min_f32 v19, s88, v57\n"
"v_mfma_f32_16x16x32_bf16 v[80:83], a[168:171], v[224:227], v[80:83]\n"
"v_exp_f32 v56, -v56 clamp\n"
"v_mfma_f32_16x16x32_bf16 v[80:83], a[172:175], v[232:235], v[80:83]\n"
"ds_read_b64_tr_b16 a[188:189], v8 offset:11264\n"
"v_fmac_f32 v56, s89, v17\n"
"v_mfma_f32_16x16x32_bf16 v[84:87], a[168:171], v[228:231], v[84:87]\n"
"ds_read_b64_tr_b16 a[190:191], v8 offset:15360\n"
"v_accvgpr_read_b32 v34, a196\n"
"v_mfma_f32_16x16x32_bf16 v[84:87], a[172:175], v[236:239], v[84:87]\n"
"v_exp_f32 v33, -v33 clamp\n"
"v_mfma_f32_16x16x32_bf16 v[88:91], a[168:171], a[96:99], v[88:91]\n"
"v_cvt_pk_bf16_f32 v32, v32, v56\n"
"v_fmac_f32 v33, s89, v18\n"
"v_mfma_f32_16x16x32_bf16 v[88:91], a[172:175], a[104:107], v[88:91]\n"
"v_cvt_pk_bf16_f32 v113, v114, v115\n"
"v_min_f32 v16, s88, v34\n"
"v_mfma_f32_16x16x32_bf16 v[92:95], a[168:171], a[100:103], v[92:95]\n"
"v_exp_f32 v57, -v57 clamp\n"
"v_mfma_f32_16x16x32_bf16 v[92:95], a[172:175], a[108:111], v[92:95]\n"
"s_waitcnt lgkmcnt(0)\n"
"ds_write_b64 v13, v[112:113] offset:0\n"
"v_mfma_f32_16x16x32_bf16 v[64:67], a[176:179], v[240:243], v[64:67]\n"
"s_waitcnt vmcnt(12)\n"
"s_barrier\n"
"ds_read_b64_tr_b16 a[160:161], v7 offset:0\n"
"v_mfma_f32_16x16x32_bf16 v[64:67], a[180:183], v[248:251], v[64:67]\n"
"ds_read_b64_tr_b16 a[162:163], v7 offset:4096\n"
"v_cvt_pk_bf16_f32 v116, v116, v117\n"
"v_mfma_f32_16x16x32_bf16 v[68:71], a[176:179], v[244:247], v[68:71]\n"
"v_cvt_pk_bf16_f32 v117, v118, v119\n"
"v_accvgpr_read_b32 v58, a197\n"
"v_mfma_f32_16x16x32_bf16 v[68:71], a[180:183], v[252:255], v[68:71]\n"
"s_addk_i32 m0, 16384\n"
"ds_read_b64_tr_b16 a[164:165], v7 offset:1024\n"
"v_fmac_f32 v57, s89, v19\n"
"v_mfma_f32_16x16x32_bf16 v[72:75], a[176:179], a[112:115], v[72:75]\n"
"s_add_u32 s82, s78, 4224\n"
"ds_read_b64_tr_b16 a[166:167], v7 offset:5120\n"
"v_min_f32 v17, s88, v58\n"
"v_mfma_f32_16x16x32_bf16 v[72:75], a[180:183], a[120:123], v[72:75]\n"
"buffer_load_dwordx4 v11, s[48:51], s82 offen offset:0 lds\n"
"v_mfma_f32_16x16x32_bf16 v[76:79], a[176:179], a[116:119], v[76:79]\n"
"ds_read_b64_tr_b16 a[168:169], v7 offset:8192\n"
"v_cvt_pk_bf16_f32 v120, v120, v121\n"
"v_mfma_f32_16x16x32_bf16 v[76:79], a[180:183], a[124:127], v[76:79]\n"
"ds_read_b64_tr_b16 a[170:171], v7 offset:12288\n"
"v_cvt_pk_bf16_f32 v121, v122, v123\n"
"v_mfma_f32_16x16x32_bf16 v[80:83], a[184:187], v[240:243], v[80:83]\n"
"v_exp_f32 v34, -v34 clamp\n"
"v_mfma_f32_16x16x32_bf16 v[80:83], a[188:191], v[248:251], v[80:83]\n"
"ds_read_b64_tr_b16 a[172:173], v7 offset:9216\n"
"v_accvgpr_read_b32 v35, a198\n"
"v_mfma_f32_16x16x32_bf16 v[84:87], a[184:187], v[244:247], v[84:87]\n"
"s_add_u32 s82, s82, s13\n"
"ds_read_b64_tr_b16 a[174:175], v7 offset:13312\n"
"v_cvt_pk_bf16_f32 v33, v33, v57\n"
"v_mfma_f32_16x16x32_bf16 v[84:87], a[188:191], v[252:255], v[84:87]\n"
"buffer_load_dwordx4 v11, s[48:51], s82 offen offset:1024 lds\n"
"v_mfma_f32_16x16x32_bf16 v[88:91], a[184:187], a[112:115], v[88:91]\n"
"ds_write_b64 v13, v[116:117] offset:512\n"
"v_mfma_f32_16x16x32_bf16 v[88:91], a[188:191], a[120:123], v[88:91]\n"
"s_add_u32 s82, s82, s13\n"
"v_cvt_pk_bf16_f32 v124, v124, v125\n"
"v_fmac_f32 v34, s89, v16\n"
"v_mfma_f32_16x16x32_bf16 v[92:95], a[184:187], a[116:119], v[92:95]\n"
"buffer_load_dwordx4 v11, s[48:51], s82 offen offset:2048 lds\n"
"v_mfma_f32_16x16x32_bf16 v[92:95], a[188:191], a[124:127], v[92:95]\n"
"s_waitcnt lgkmcnt(1)\n"
"v_exp_f32 v58, -v58 clamp\n"
"v_mfma_f32_16x16x32_bf16 v[64:67], a[160:163], a[144:147], v[64:67]\n"
"ds_read_b64_tr_b16 a[176:177], v1 offset:2048\n"
"v_cvt_pk_bf16_f32 v125, v126, v127\n"
"v_mfma_f32_16x16x32_bf16 v[64:67], a[164:167], a[152:155], v[64:67]\n"
"ds_read_b64_tr_b16 a[178:179], v1 offset:10240\n"
"v_min_f32 v18, s88, v35\n"
"v_mfma_f32_16x16x32_bf16 v[68:71], a[160:163], a[148:151], v[68:71]\n"
"v_accvgpr_read_b32 v59, a199\n"
"v_fmac_f32 v58, s89, v17\n"
"v_mfma_f32_16x16x32_bf16 v[68:71], a[164:167], a[156:159], v[68:71]\n"
"ds_read_b64_tr_b16 a[180:181], v1 offset:3072\n"
"v_min_f32 v19, s88, v59\n"
"v_mfma_f32_16x16x32_bf16 v[72:75], a[160:163], a[128:131], v[72:75]\n"
"s_add_u32 s82, s82, s13\n"
"ds_read_b64_tr_b16 a[182:183], v1 offset:11264\n"
"v_accvgpr_read_b32 v36, a200\n"
"v_mfma_f32_16x16x32_bf16 v[72:75], a[164:167], a[136:139], v[72:75]\n"
"buffer_load_dwordx4 v11, s[48:51], s82 offen offset:3072 lds\n"
"v_mfma_f32_16x16x32_bf16 v[76:79], a[160:163], a[132:135], v[76:79]\n"
"ds_read_b64_tr_b16 a[184:185], v2 offset:2048\n"
"v_cvt_pk_bf16_f32 v34, v34, v58\n"
"v_mfma_f32_16x16x32_bf16 v[76:79], a[164:167], a[140:143], v[76:79]\n"
"ds_read_b64_tr_b16 a[186:187], v2 offset:10240\n"
"v_min_f32 v16, s88, v36\n"
"v_mfma_f32_16x16x32_bf16 v[80:83], a[168:171], a[144:147], v[80:83]\n"
"ds_write_b64 v13, v[120:121] offset:1024\n"
"v_mfma_f32_16x16x32_bf16 v[80:83], a[172:175], a[152:155], v[80:83]\n"
"s_addk_i32 m0, 16384\n"
"ds_read_b64_tr_b16 a[188:189], v2 offset:3072\n"
"v_accvgpr_read_b32 v56, a201\n"
"v_mfma_f32_16x16x32_bf16 v[84:87], a[168:171], a[148:151], v[84:87]\n"
"s_add_u32 s82, s82, s15\n"
"ds_read_b64_tr_b16 a[190:191], v2 offset:11264\n"
"v_min_f32 v17, s88, v56\n"
"v_mfma_f32_16x16x32_bf16 v[84:87], a[172:175], a[156:159], v[84:87]\n"
"buffer_load_dwordx4 v11, s[48:51], s82 offen offset:0 lds\n"
"v_mfma_f32_16x16x32_bf16 v[88:91], a[168:171], a[128:131], v[88:91]\n"
"v_exp_f32 v35, -v35 clamp\n"
"v_mfma_f32_16x16x32_bf16 v[88:91], a[172:175], a[136:139], v[88:91]\n"
"s_add_u32 s82, s82, s13\n"
"v_fmac_f32 v35, s89, v18\n"
"v_accvgpr_read_b32 v37, a202\n"
"v_mfma_f32_16x16x32_bf16 v[92:95], a[168:171], a[132:135], v[92:95]\n"
"buffer_load_dwordx4 v11, s[48:51], s82 offen offset:1024 lds\n"
"v_mfma_f32_16x16x32_bf16 v[92:95], a[172:175], a[140:143], v[92:95]\n"
"s_waitcnt lgkmcnt(0)\n"
"v_exp_f32 v59, -v59 clamp\n"
"v_mfma_f32_16x16x32_bf16 v[96:99], a[176:179], v[128:131], 0\n"
"s_and_b32 s35, s75, 1\n"
"ds_read_b64_tr_b16 a[160:161], v1 offset:16384\n"
"v_min_u32 v0, 0xd0, v9\n"
"v_mfma_f32_16x16x32_bf16 v[96:99], a[180:183], v[136:139], v[96:99]\n"
"s_lshl_b32 s34, s76, 1\n"
"ds_read_b64_tr_b16 a[162:163], v1 offset:24576\n"
"v_fmac_f32 v59, s89, v19\n"
"v_mfma_f32_16x16x32_bf16 v[100:103], a[176:179], v[132:135], 0\n"
"s_add_u32 s34, s34, s35\n"
"ds_write_b64 v13, v[124:125] offset:1536\n"
"v_mfma_f32_16x16x32_bf16 v[100:103], a[180:183], v[140:143], v[100:103]\n"
"s_lshl_b32 s34, s34, 2\n"
"ds_read_b64_tr_b16 a[164:165], v1 offset:17408\n"
"v_lshlrev_b32 v0, 3, v0\n"
"v_mfma_f32_16x16x32_bf16 v[104:107], a[176:179], a[0:3], 0\n"
"s_add_u32 s34, s34, s72\n"
"s_mul_i32 s35, s35, 576\n"
"ds_read_b64_tr_b16 a[166:167], v1 offset:25600\n"
"v_mfma_f32_16x16x32_bf16 v[104:107], a[180:183], a[8:11], v[104:107]\n"
"s_sub_u32 s35, 4096, s35\n"
"v_exp_f32 v36, -v36 clamp\n"
"v_mfma_f32_16x16x32_bf16 v[108:111], a[176:179], a[4:7], 0\n"
"s_mul_i32 s34, s34, 768\n"
"s_add_u32 s35, s35, s34\n"
"ds_read_b64_tr_b16 a[168:169], v2 offset:16384\n"
"v_mfma_f32_16x16x32_bf16 v[108:111], a[180:183], a[12:15], v[108:111]\n"
"s_add_u32 s82, s82, s13\n"
"s_mul_i32 s34, s34, 6\n"
"ds_read_b64_tr_b16 a[170:171], v2 offset:24576\n"
"v_mfma_f32_16x16x32_bf16 v[112:115], a[184:187], v[128:131], 0\n"
"buffer_load_dwordx4 v11, s[48:51], s82 offen offset:2048 lds\n"
"v_mfma_f32_16x16x32_bf16 v[112:115], a[188:191], v[136:139], v[112:115]\n"
"s_add_u32 s34, s34, s35\n"
"ds_read_b64_tr_b16 a[172:173], v2 offset:17408\n"
"v_cvt_pk_bf16_f32 v35, v35, v59\n"
"v_mfma_f32_16x16x32_bf16 v[116:119], a[184:187], v[132:135], 0\n"
"ds_read_b64_tr_b16 a[174:175], v2 offset:25600\n"
"v_fmac_f32 v36, s89, v16\n"
"v_mfma_f32_16x16x32_bf16 v[116:119], a[188:191], v[140:143], v[116:119]\n"
"v_exp_f32 v56, -v56 clamp\n"
"v_mfma_f32_16x16x32_bf16 v[120:123], a[184:187], a[0:3], 0\n"
"s_add_u32 s82, s82, s13\n"
"v_min_f32 v18, s88, v37\n"
"v_accvgpr_read_b32 v57, a203\n"
"v_mfma_f32_16x16x32_bf16 v[120:123], a[188:191], a[8:11], v[120:123]\n"
"buffer_load_dwordx4 v11, s[48:51], s82 offen offset:3072 lds\n"
"v_mfma_f32_16x16x32_bf16 v[124:127], a[184:187], a[4:7], 0\n"
"ds_read_b128 v[44:47], v29 offset:0\n"
"v_fmac_f32 v56, s89, v17\n"
"v_mfma_f32_16x16x32_bf16 v[124:127], a[188:191], a[12:15], v[124:127]\n"
"s_waitcnt lgkmcnt(1)\n"
"v_exp_f32 v37, -v37 clamp\n"
"v_mfma_f32_16x16x32_bf16 v[96:99], a[160:163], v[144:147], v[96:99]\n"
"ds_read_b64_tr_b16 a[176:177], v1 offset:18432\n"
"v_min_f32 v19, s88, v57\n"
"v_mfma_f32_16x16x32_bf16 v[96:99], a[164:167], v[152:155], v[96:99]\n"
"ds_read_b128 v[52:55], v29 offset:1024\n"
"ds_read_b64_tr_b16 a[178:179], v1 offset:26624\n"
"v_mfma_f32_16x16x32_bf16 v[100:103], a[160:163], v[148:151], v[100:103]\n"
"v_accvgpr_read_b32 v38, a204\n"
"v_cvt_pk_bf16_f32 v36, v36, v56\n"
"v_mfma_f32_16x16x32_bf16 v[100:103], a[164:167], v[156:159], v[100:103]\n"
"ds_read_b64_tr_b16 a[180:181], v1 offset:19456\n"
"v_fmac_f32 v37, s89, v18\n"
"v_mfma_f32_16x16x32_bf16 v[104:107], a[160:163], a[16:19], v[104:107]\n"
"ds_read_b64_tr_b16 a[182:183], v1 offset:27648\n"
"v_min_f32 v16, s88, v38\n"
"v_mfma_f32_16x16x32_bf16 v[104:107], a[164:167], a[24:27], v[104:107]\n"
"v_exp_f32 v57, -v57 clamp\n"
"v_mfma_f32_16x16x32_bf16 v[108:111], a[160:163], a[20:23], v[108:111]\n"
"ds_read_b64_tr_b16 a[184:185], v2 offset:18432\n"
"v_accvgpr_read_b32 v58, a205\n"
"v_mfma_f32_16x16x32_bf16 v[108:111], a[164:167], a[28:31], v[108:111]\n"
"ds_read_b64_tr_b16 a[186:187], v2 offset:26624\n"
"v_fmac_f32 v57, s89, v19\n"
"v_mfma_f32_16x16x32_bf16 v[112:115], a[168:171], v[144:147], v[112:115]\n"
"v_exp_f32 v38, -v38 clamp\n"
"v_mfma_f32_16x16x32_bf16 v[112:115], a[172:175], v[152:155], v[112:115]\n"
"ds_read_b64_tr_b16 a[188:189], v2 offset:19456\n"
"v_min_f32 v17, s88, v58\n"
"v_mfma_f32_16x16x32_bf16 v[116:119], a[168:171], v[148:151], v[116:119]\n"
"ds_read_b64_tr_b16 a[190:191], v2 offset:27648\n"
"v_accvgpr_read_b32 v39, a206\n"
"v_mfma_f32_16x16x32_bf16 v[116:119], a[172:175], v[156:159], v[116:119]\n"
"v_cvt_pk_bf16_f32 v37, v37, v57\n"
"v_fmac_f32 v38, s89, v16\n"
"v_mfma_f32_16x16x32_bf16 v[120:123], a[168:171], a[16:19], v[120:123]\n"
"v_exp_f32 v58, -v58 clamp\n"
"v_mfma_f32_16x16x32_bf16 v[120:123], a[172:175], a[24:27], v[120:123]\n"
"v_min_f32 v18, s88, v39\n"
"v_accvgpr_read_b32 v59, a207\n"
"v_mfma_f32_16x16x32_bf16 v[124:127], a[168:171], a[20:23], v[124:127]\n"
"v_fmac_f32 v58, s89, v17\n"
"v_min_f32 v19, s88, v59\n"
"v_mfma_f32_16x16x32_bf16 v[124:127], a[172:175], a[28:31], v[124:127]\n"
"s_waitcnt lgkmcnt(0)\n"
"v_exp_f32 v39, -v39 clamp\n"
"v_mfma_f32_16x16x32_bf16 v[96:99], a[176:179], v[160:163], v[96:99]\n"
"s_waitcnt vmcnt(12)\n"
"s_barrier\n"
"ds_read_b64_tr_b16 a[160:161], v3 offset:16384\n"
"v_mfma_f32_16x16x32_bf16 v[96:99], a[180:183], v[168:171], v[96:99]\n"
"ds_read_b64_tr_b16 a[162:163], v3 offset:24576\n"
"v_cvt_pk_bf16_f32 v38, v38, v58\n"
"v_mfma_f32_16x16x32_bf16 v[100:103], a[176:179], v[164:167], v[100:103]\n"
"buffer_load_dword a236, v0, s[56:59], s34 offen offset:0\n"
"v_mfma_f32_16x16x32_bf16 v[100:103], a[180:183], v[172:175], v[100:103]\n"
"s_add_u32 m0, s77, 0\n"
"ds_read_b64_tr_b16 a[164:165], v3 offset:17408\n"
"v_fmac_f32 v39, s89, v18\n"
"v_mfma_f32_16x16x32_bf16 v[104:107], a[176:179], a[32:35], v[104:107]\n"
"s_add_u32 s82, s82, s15\n"
"ds_read_b64_tr_b16 a[166:167], v3 offset:25600\n"
"v_mov_b32 v28, 0x3f803f80\n"
"v_mfma_f32_16x16x32_bf16 v[104:107], a[180:183], a[40:43], v[104:107]\n"
"buffer_load_dwordx4 v11, s[48:51], s82 offen offset:0 lds\n"
"v_mfma_f32_16x16x32_bf16 v[108:111], a[176:179], a[36:39], v[108:111]\n"
"ds_read_b64_tr_b16 a[168:169], v4 offset:16384\n"
"v_cndmask_b32 v40, v40, v28, s[94:95]\n"
"v_mfma_f32_16x16x32_bf16 v[108:111], a[180:183], a[44:47], v[108:111]\n"
"ds_read_b64_tr_b16 a[170:171], v4 offset:24576\n"
"v_cndmask_b32 v41, v41, v28, s[94:95]\n"
"v_mfma_f32_16x16x32_bf16 v[112:115], a[184:187], v[160:163], v[112:115]\n"
"v_exp_f32 v59, -v59 clamp\n"
"v_mfma_f32_16x16x32_bf16 v[112:115], a[188:191], v[168:171], v[112:115]\n"
"ds_read_b64_tr_b16 a[172:173], v4 offset:17408\n"
"v_fmac_f32 v59, s89, v19\n"
"v_mfma_f32_16x16x32_bf16 v[116:119], a[184:187], v[164:167], v[116:119]\n"
"s_add_u32 s82, s82, s13\n"
"ds_read_b64_tr_b16 a[174:175], v4 offset:25600\n"
"v_cvt_pk_bf16_f32 v39, v39, v59\n"
"v_mfma_f32_16x16x32_bf16 v[116:119], a[188:191], v[172:175], v[116:119]\n"
"buffer_load_dwordx4 v11, s[48:51], s82 offen offset:1024 lds\n"
"v_mfma_f32_16x16x32_bf16 v[120:123], a[184:187], a[32:35], v[120:123]\n"
"v_cndmask_b32 v42, v42, v28, s[94:95]\n"
"v_cndmask_b32 v43, v43, v28, s[94:95]\n"
"v_mfma_f32_16x16x32_bf16 v[120:123], a[188:191], a[40:43], v[120:123]\n"
"s_add_u32 s82, s82, s13\n"
"v_cndmask_b32 v44, v44, v28, s[94:95]\n"
"v_cndmask_b32 v45, v45, v28, s[94:95]\n"
"v_mfma_f32_16x16x32_bf16 v[124:127], a[184:187], a[36:39], v[124:127]\n"
"buffer_load_dwordx4 v11, s[48:51], s82 offen offset:2048 lds\n"
"v_mfma_f32_16x16x32_bf16 v[124:127], a[188:191], a[44:47], v[124:127]\n"
"s_waitcnt lgkmcnt(0)\n"
"v_cndmask_b32 v46, v46, v28, s[94:95]\n"
"v_cndmask_b32 v47, v47, v28, s[94:95]\n"
"v_mfma_f32_16x16x32_bf16 v[96:99], a[160:163], v[176:179], v[96:99]\n"
"ds_read_b64_tr_b16 a[176:177], v3 offset:18432\n"
"v_lshl_add_u32 v7, s74, 10, v9\n"
"v_mfma_f32_16x16x32_bf16 v[96:99], a[164:167], v[184:187], v[96:99]\n"
"ds_read_b64_tr_b16 a[178:179], v3 offset:26624\n"
"v_add_u32 v8, 0x10000, v7\n"
"v_mfma_f32_16x16x32_bf16 v[100:103], a[160:163], v[180:183], v[100:103]\n"
"ds_write_b64 v10, v[48:49] offset:0\n"
"v_mfma_f32_16x16x32_bf16 v[100:103], a[164:167], v[188:191], v[100:103]\n"
"ds_read_b64_tr_b16 a[180:181], v3 offset:19456\n"
"v_mfma_f32_16x16x32_bf16 v[104:107], a[160:163], a[48:51], v[104:107]\n"
"s_add_u32 s82, s82, s13\n"
"ds_read_b64_tr_b16 a[182:183], v3 offset:27648\n"
"v_mfma_f32_16x16x32_bf16 v[104:107], a[164:167], a[56:59], v[104:107]\n"
"buffer_load_dwordx4 v11, s[48:51], s82 offen offset:3072 lds\n"
"v_mfma_f32_16x16x32_bf16 v[108:111], a[160:163], a[52:55], v[108:111]\n"
"ds_read_b64_tr_b16 a[184:185], v4 offset:18432\n"
"v_mfma_f32_16x16x32_bf16 v[108:111], a[164:167], a[60:63], v[108:111]\n"
"ds_read_b64_tr_b16 a[186:187], v4 offset:26624\n"
"v_mfma_f32_16x16x32_bf16 v[112:115], a[168:171], v[176:179], v[112:115]\n"
"v_exp_f32 v16, v64\n"
"v_mfma_f32_16x16x32_bf16 v[112:115], a[172:175], v[184:187], v[112:115]\n"
"s_addk_i32 m0, 16384\n"
"ds_read_b64_tr_b16 a[188:189], v4 offset:19456\n"
"v_add_f32 v16, 1.0, v16\n"
"v_mfma_f32_16x16x32_bf16 v[116:119], a[168:171], v[180:183], v[116:119]\n"
"s_add_u32 s82, s82, s15\n"
"ds_read_b64_tr_b16 a[190:191], v4 offset:27648\n"
"v_mfma_f32_16x16x32_bf16 v[116:119], a[172:175], v[188:191], v[116:119]\n"
"buffer_load_dwordx4 v11, s[48:51], s82 offen offset:0 lds\n"
"v_mfma_f32_16x16x32_bf16 v[120:123], a[168:171], a[48:51], v[120:123]\n"
"ds_write_b64 v10, v[50:51] offset:128\n"
"v_mfma_f32_16x16x32_bf16 v[120:123], a[172:175], a[56:59], v[120:123]\n"
"s_add_u32 s82, s82, s13\n"
"v_exp_f32 v17, v65\n"
"v_mfma_f32_16x16x32_bf16 v[124:127], a[168:171], a[52:55], v[124:127]\n"
"buffer_load_dwordx4 v11, s[48:51], s82 offen offset:1024 lds\n"
"v_mfma_f32_16x16x32_bf16 v[124:127], a[172:175], a[60:63], v[124:127]\n"
"s_waitcnt lgkmcnt(1)\n"
"v_rcp_f32 v16, v16\n"
"v_mfma_f32_16x16x32_bf16 v[96:99], a[176:179], v[192:195], v[96:99]\n"
"ds_read_b64_tr_b16 a[160:161], v3 offset:32768\n"
"v_add_f32 v17, 1.0, v17\n"
"v_mfma_f32_16x16x32_bf16 v[96:99], a[180:183], v[200:203], v[96:99]\n"
"ds_read_b64_tr_b16 a[162:163], v3 offset:40960\n"
"v_mul_f32 v64, v64, v16\n"
"v_mfma_f32_16x16x32_bf16 v[100:103], a[176:179], v[196:199], v[100:103]\n"
"v_exp_f32 v18, v66\n"
"v_mfma_f32_16x16x32_bf16 v[100:103], a[180:183], v[204:207], v[100:103]\n"
"ds_read_b64_tr_b16 a[164:165], v3 offset:33792\n"
"v_add_f32 v18, 1.0, v18\n"
"v_mfma_f32_16x16x32_bf16 v[104:107], a[176:179], a[64:67], v[104:107]\n"
"ds_read_b64_tr_b16 a[166:167], v3 offset:41984\n"
"v_mfma_f32_16x16x32_bf16 v[104:107], a[180:183], a[72:75], v[104:107]\n"
"v_rcp_f32 v17, v17\n"
"v_mfma_f32_16x16x32_bf16 v[108:111], a[176:179], a[68:71], v[108:111]\n"
"ds_read_b64_tr_b16 a[168:169], v4 offset:32768\n"
"v_mul_f32 v65, v65, v17\n"
"v_mfma_f32_16x16x32_bf16 v[108:111], a[180:183], a[76:79], v[108:111]\n"
"s_add_u32 s82, s82, s13\n"
"ds_read_b64_tr_b16 a[170:171], v4 offset:40960\n"
"v_mfma_f32_16x16x32_bf16 v[112:115], a[184:187], v[192:195], v[112:115]\n"
"buffer_load_dwordx4 v11, s[48:51], s82 offen offset:2048 lds\n"
"v_mfma_f32_16x16x32_bf16 v[112:115], a[188:191], v[200:203], v[112:115]\n"
"ds_read_b64_tr_b16 a[172:173], v4 offset:33792\n"
"v_mfma_f32_16x16x32_bf16 v[116:119], a[184:187], v[196:199], v[116:119]\n"
"ds_read_b64_tr_b16 a[174:175], v4 offset:41984\n"
"v_mfma_f32_16x16x32_bf16 v[116:119], a[188:191], v[204:207], v[116:119]\n"
"ds_write_b64 v10, v[52:53] offset:256\n"
"v_mfma_f32_16x16x32_bf16 v[120:123], a[184:187], a[64:67], v[120:123]\n"
"s_add_u32 s82, s82, s13\n"
"v_exp_f32 v19, v67\n"
"v_mfma_f32_16x16x32_bf16 v[120:123], a[188:191], a[72:75], v[120:123]\n"
"buffer_load_dwordx4 v11, s[48:51], s82 offen offset:3072 lds\n"
"v_mfma_f32_16x16x32_bf16 v[124:127], a[184:187], a[68:71], v[124:127]\n"
"v_rcp_f32 v18, v18\n"
"v_mfma_f32_16x16x32_bf16 v[124:127], a[188:191], a[76:79], v[124:127]\n"
"s_waitcnt lgkmcnt(1)\n"
"v_add_f32 v19, 1.0, v19\n"
"v_mul_f32 v66, v66, v18\n"
"v_mfma_f32_16x16x32_bf16 v[96:99], a[160:163], v[208:211], v[96:99]\n"
"ds_read_b64_tr_b16 a[176:177], v3 offset:34816\n"
"v_mfma_f32_16x16x32_bf16 v[96:99], a[164:167], v[216:219], v[96:99]\n"
"ds_read_b64_tr_b16 a[178:179], v3 offset:43008\n"
"v_mfma_f32_16x16x32_bf16 v[100:103], a[160:163], v[212:215], v[100:103]\n"
"buffer_load_dword a237, v0, s[56:59], s34 offen offset:1792\n"
"v_mfma_f32_16x16x32_bf16 v[100:103], a[164:167], v[220:223], v[100:103]\n"
"ds_read_b64_tr_b16 a[180:181], v3 offset:35840\n"
"v_mfma_f32_16x16x32_bf16 v[104:107], a[160:163], a[80:83], v[104:107]\n"
"ds_read_b64_tr_b16 a[182:183], v3 offset:44032\n"
"v_mfma_f32_16x16x32_bf16 v[104:107], a[164:167], a[88:91], v[104:107]\n"
"ds_write_b64 v10, v[54:55] offset:384\n"
"v_mfma_f32_16x16x32_bf16 v[108:111], a[160:163], a[84:87], v[108:111]\n"
"ds_read_b64_tr_b16 a[184:185], v4 offset:34816\n"
"v_add_u32 v10, s96, v9\n"
"v_mfma_f32_16x16x32_bf16 v[108:111], a[164:167], a[92:95], v[108:111]\n"
"ds_read_b64_tr_b16 a[186:187], v4 offset:43008\n"
"v_mfma_f32_16x16x32_bf16 v[112:115], a[168:171], v[208:211], v[112:115]\n"
"v_exp_f32 v16, v68\n"
"v_mfma_f32_16x16x32_bf16 v[112:115], a[172:175], v[216:219], v[112:115]\n"
"ds_read_b64_tr_b16 a[188:189], v4 offset:35840\n"
"v_add_f32 v16, 1.0, v16\n"
"v_mfma_f32_16x16x32_bf16 v[116:119], a[168:171], v[212:215], v[116:119]\n"
"ds_read_b64_tr_b16 a[190:191], v4 offset:44032\n"
"v_mfma_f32_16x16x32_bf16 v[116:119], a[172:175], v[220:223], v[116:119]\n"
"v_rcp_f32 v19, v19\n"
"v_mfma_f32_16x16x32_bf16 v[120:123], a[168:171], a[80:83], v[120:123]\n"
"v_exp_f32 v17, v69\n"
"v_mfma_f32_16x16x32_bf16 v[120:123], a[172:175], a[88:91], v[120:123]\n"
"v_mul_f32 v67, v67, v19\n"
"v_add_f32 v17, 1.0, v17\n"
"v_mfma_f32_16x16x32_bf16 v[124:127], a[168:171], a[84:87], v[124:127]\n"
"v_rcp_f32 v16, v16\n"
"v_mfma_f32_16x16x32_bf16 v[124:127], a[172:175], a[92:95], v[124:127]\n"
"v_exp_f32 v18, v70\n"
"v_mfma_f32_16x16x32_bf16 v[56:59], v[32:35], v[20:23], 0\n"
"v_mul_f32 v68, v68, v16\n"
"v_add_f32 v18, 1.0, v18\n"
"v_mfma_f32_16x16x32_bf16 v[56:59], v[36:39], v[24:27], v[56:59]\n"
"v_rcp_f32 v17, v17\n"
"v_mfma_f32_16x16x32_bf16 v[60:63], v[32:35], v[40:43], 0\n"
"v_exp_f32 v19, v71\n"
"v_mfma_f32_16x16x32_bf16 v[60:63], v[36:39], v[44:47], v[60:63]\n"
"v_mul_f32 v69, v69, v17\n"
"v_add_f32 v19, 1.0, v19\n"
"v_mfma_f32_16x16x32_bf16 v[32:35], v[48:51], v[20:23], 0\n"
"s_nop 0\n"
"ds_write_b128 v10, v[56:59] offset:0\n"
"v_mfma_f32_16x16x32_bf16 v[32:35], v[52:55], v[24:27], v[32:35]\n"
"v_rcp_f32 v18, v18\n"
"v_mfma_f32_16x16x32_bf16 v[36:39], v[48:51], v[40:43], 0\n"
"v_exp_f32 v16, v72\n"
"v_mfma_f32_16x16x32_bf16 v[36:39], v[52:55], v[44:47], v[36:39]\n"
"s_waitcnt lgkmcnt(1)\n"
"v_mul_f32 v70, v70, v18\n"
"v_add_f32 v16, 1.0, v16\n"
"v_mfma_f32_16x16x32_bf16 v[96:99], a[176:179], v[224:227], v[96:99]\n"
"s_waitcnt vmcnt(14)\n"
"s_barrier\n"
"ds_read_b64_tr_b16 a[160:161], v1 offset:32768\n"
"v_mfma_f32_16x16x32_bf16 v[96:99], a[180:183], v[232:235], v[96:99]\n"
"ds_read_b64_tr_b16 a[162:163], v1 offset:40960\n"
"v_mfma_f32_16x16x32_bf16 v[100:103], a[176:179], v[228:231], v[100:103]\n"
"v_rcp_f32 v19, v19\n"
"v_mfma_f32_16x16x32_bf16 v[100:103], a[180:183], v[236:239], v[100:103]\n"
"s_add_u32 m0, s77, 81920\n"
"ds_read_b64_tr_b16 a[164:165], v1 offset:33792\n"
"v_mul_f32 v71, v71, v19\n"
"v_mfma_f32_16x16x32_bf16 v[104:107], a[176:179], a[96:99], v[104:107]\n"
"s_add_u32 s82, s82, s15\n"
"ds_read_b64_tr_b16 a[166:167], v1 offset:41984\n"
"v_mfma_f32_16x16x32_bf16 v[104:107], a[180:183], a[104:107], v[104:107]\n"
"buffer_load_dwordx4 v11, s[48:51], s82 offen offset:0 lds\n"
"v_mfma_f32_16x16x32_bf16 v[108:111], a[176:179], a[100:103], v[108:111]\n"
"ds_read_b64_tr_b16 a[168:169], v2 offset:32768\n"
"ds_write_b128 v10, v[60:63] offset:1024\n"
"v_mfma_f32_16x16x32_bf16 v[108:111], a[180:183], a[108:111], v[108:111]\n"
"ds_read_b64_tr_b16 a[170:171], v2 offset:40960\n"
"v_mfma_f32_16x16x32_bf16 v[112:115], a[184:187], v[224:227], v[112:115]\n"
"ds_write_b128 v10, v[36:39] offset:2048\n"
"v_mfma_f32_16x16x32_bf16 v[112:115], a[188:191], v[232:235], v[112:115]\n"
"ds_read_b64_tr_b16 a[172:173], v2 offset:33792\n"
"v_mfma_f32_16x16x32_bf16 v[116:119], a[184:187], v[228:231], v[116:119]\n"
"s_add_u32 s82, s82, s13\n"
"ds_read_b64_tr_b16 a[174:175], v2 offset:41984\n"
"ds_write_b128 v10, v[32:35] offset:3072\n"
"v_mfma_f32_16x16x32_bf16 v[116:119], a[188:191], v[236:239], v[116:119]\n"
"buffer_load_dwordx4 v11, s[48:51], s82 offen offset:1024 lds\n"
"v_mfma_f32_16x16x32_bf16 v[120:123], a[184:187], a[96:99], v[120:123]\n"
"v_exp_f32 v17, v73\n"
"v_mfma_f32_16x16x32_bf16 v[120:123], a[188:191], a[104:107], v[120:123]\n"
"s_add_u32 s82, s78, 2304\n"
"v_rcp_f32 v16, v16\n"
"v_mfma_f32_16x16x32_bf16 v[124:127], a[184:187], a[100:103], v[124:127]\n"
"buffer_load_dwordx4 v11, s[44:47], s82 offen offset:2048 lds\n"
"v_mfma_f32_16x16x32_bf16 v[124:127], a[188:191], a[108:111], v[124:127]\n"
"s_waitcnt lgkmcnt(1)\n"
"v_add_f32 v17, 1.0, v17\n"
"v_mul_f32 v72, v72, v16\n"
"v_mfma_f32_16x16x32_bf16 v[96:99], a[160:163], v[240:243], v[96:99]\n"
"ds_read_b64_tr_b16 a[176:177], v1 offset:34816\n"
"v_mfma_f32_16x16x32_bf16 v[96:99], a[164:167], v[248:251], v[96:99]\n"
"ds_read_b64_tr_b16 a[178:179], v1 offset:43008\n"
"v_mfma_f32_16x16x32_bf16 v[100:103], a[160:163], v[244:247], v[100:103]\n"
"v_exp_f32 v18, v74\n"
"v_mfma_f32_16x16x32_bf16 v[100:103], a[164:167], v[252:255], v[100:103]\n"
"ds_read_b64_tr_b16 a[180:181], v1 offset:35840\n"
"v_add_f32 v18, 1.0, v18\n"
"v_mfma_f32_16x16x32_bf16 v[104:107], a[160:163], a[112:115], v[104:107]\n"
"ds_read_b64_tr_b16 a[182:183], v1 offset:44032\n"
"v_mfma_f32_16x16x32_bf16 v[104:107], a[164:167], a[120:123], v[104:107]\n"
"v_rcp_f32 v17, v17\n"
"v_mfma_f32_16x16x32_bf16 v[108:111], a[160:163], a[116:119], v[108:111]\n"
"ds_read_b64_tr_b16 a[184:185], v2 offset:34816\n"
"v_mul_f32 v73, v73, v17\n"
"v_mfma_f32_16x16x32_bf16 v[108:111], a[164:167], a[124:127], v[108:111]\n"
"ds_read_b64_tr_b16 a[186:187], v2 offset:43008\n"
"v_mfma_f32_16x16x32_bf16 v[112:115], a[168:171], v[240:243], v[112:115]\n"
"v_exp_f32 v19, v75\n"
"v_mfma_f32_16x16x32_bf16 v[112:115], a[172:175], v[248:251], v[112:115]\n"
"ds_read_b64_tr_b16 a[188:189], v2 offset:35840\n"
"v_add_f32 v19, 1.0, v19\n"
"v_mfma_f32_16x16x32_bf16 v[116:119], a[168:171], v[244:247], v[116:119]\n"
"ds_read_b64_tr_b16 a[190:191], v2 offset:44032\n"
"v_mfma_f32_16x16x32_bf16 v[116:119], a[172:175], v[252:255], v[116:119]\n"
"s_add_u32 s82, s82, s13\n"
"v_rcp_f32 v18, v18\n"
"v_mfma_f32_16x16x32_bf16 v[120:123], a[168:171], a[112:115], v[120:123]\n"
"buffer_load_dwordx4 v11, s[44:47], s82 offen offset:3072 lds\n"
"v_mfma_f32_16x16x32_bf16 v[120:123], a[172:175], a[120:123], v[120:123]\n"
"v_exp_f32 v16, v76\n"
"v_mfma_f32_16x16x32_bf16 v[124:127], a[168:171], a[116:119], v[124:127]\n"
"v_mul_f32 v74, v74, v18\n"
"v_add_f32 v16, 1.0, v16\n"
"v_mfma_f32_16x16x32_bf16 v[124:127], a[172:175], a[124:127], v[124:127]\n"
"s_waitcnt lgkmcnt(0)\n"
"v_rcp_f32 v19, v19\n"
"v_mfma_f32_16x16x32_bf16 v[96:99], a[176:179], a[144:147], v[96:99]\n"
"ds_read_b64_tr_b16 a[160:161], v1 offset:49152\n"
"v_mul_f32 v75, v75, v19\n"
"v_mfma_f32_16x16x32_bf16 v[96:99], a[180:183], a[152:155], v[96:99]\n"
"ds_read_b64_tr_b16 a[162:163], v1 offset:57344\n"
"v_mfma_f32_16x16x32_bf16 v[100:103], a[176:179], a[148:151], v[100:103]\n"
"buffer_load_dword a238, v0, s[56:59], s34 offen offset:3584\n"
"v_mfma_f32_16x16x32_bf16 v[100:103], a[180:183], a[156:159], v[100:103]\n"
"ds_read_b64_tr_b16 a[164:165], v1 offset:50176\n"
"v_mfma_f32_16x16x32_bf16 v[104:107], a[176:179], a[128:131], v[104:107]\n"
"ds_read_b64_tr_b16 a[166:167], v1 offset:58368\n"
"v_mfma_f32_16x16x32_bf16 v[104:107], a[180:183], a[136:139], v[104:107]\n"
"v_exp_f32 v17, v77\n"
"v_mfma_f32_16x16x32_bf16 v[108:111], a[176:179], a[132:135], v[108:111]\n"
"ds_read_b64_tr_b16 a[168:169], v2 offset:49152\n"
"v_add_f32 v17, 1.0, v17\n"
"v_mfma_f32_16x16x32_bf16 v[108:111], a[180:183], a[140:143], v[108:111]\n"
"ds_read_b64_tr_b16 a[170:171], v2 offset:57344\n"
"v_mfma_f32_16x16x32_bf16 v[112:115], a[184:187], a[144:147], v[112:115]\n"
"v_rcp_f32 v16, v16\n"
"v_mfma_f32_16x16x32_bf16 v[112:115], a[188:191], a[152:155], v[112:115]\n"
"ds_read_b64_tr_b16 a[172:173], v2 offset:50176\n"
"v_mul_f32 v76, v76, v16\n"
"v_mfma_f32_16x16x32_bf16 v[116:119], a[184:187], a[148:151], v[116:119]\n"
"ds_read_b64_tr_b16 a[174:175], v2 offset:58368\n"
"v_mfma_f32_16x16x32_bf16 v[116:119], a[188:191], a[156:159], v[116:119]\n"
"v_exp_f32 v18, v78\n"
"v_mfma_f32_16x16x32_bf16 v[120:123], a[184:187], a[128:131], v[120:123]\n"
"v_rcp_f32 v17, v17\n"
"v_mfma_f32_16x16x32_bf16 v[120:123], a[188:191], a[136:139], v[120:123]\n"
"v_add_f32 v18, 1.0, v18\n"
"v_mul_f32 v77, v77, v17\n"
"v_mfma_f32_16x16x32_bf16 v[124:127], a[184:187], a[132:135], v[124:127]\n"
"v_exp_f32 v19, v79\n"
"v_mfma_f32_16x16x32_bf16 v[124:127], a[188:191], a[140:143], v[124:127]\n"
"s_waitcnt lgkmcnt(0)\n"
"v_exp_f32 v16, v96\n"
"v_mfma_f32_16x16x32_bf16 v[32:35], a[160:163], v[128:131], 0\n"
"ds_read_b64_tr_b16 a[176:177], v1 offset:51200\n"
"v_add_f32 v16, 1.0, v16\n"
"v_mfma_f32_16x16x32_bf16 v[32:35], a[164:167], v[136:139], v[32:35]\n"
"ds_read_b64_tr_b16 a[178:179], v1 offset:59392\n"
"v_add_f32 v19, 1.0, v19\n"
"v_mfma_f32_16x16x32_bf16 v[36:39], a[160:163], v[132:135], 0\n"
"s_mul_i32 s99, s74, 8184\n"
"v_exp_f32 v17, v97\n"
"v_mfma_f32_16x16x32_bf16 v[36:39], a[164:167], v[140:143], v[36:39]\n"
"ds_read_b64_tr_b16 a[180:181], v1 offset:52224\n"
"v_add_f32 v17, 1.0, v17\n"
"v_mfma_f32_16x16x32_bf16 v[40:43], a[160:163], a[0:3], 0\n"
"ds_read_b64_tr_b16 a[182:183], v1 offset:60416\n"
"v_mul_f32 v64, v64, v80\n"
"v_mfma_f32_16x16x32_bf16 v[40:43], a[164:167], a[8:11], v[40:43]\n"
"v_rcp_f32 v18, v18\n"
"v_mfma_f32_16x16x32_bf16 v[44:47], a[160:163], a[4:7], 0\n"
"ds_read_b64_tr_b16 a[184:185], v2 offset:51200\n"
"v_mul_f32 v78, v78, v18\n"
"v_mfma_f32_16x16x32_bf16 v[44:47], a[164:167], a[12:15], v[44:47]\n"
"ds_read_b64_tr_b16 a[186:187], v2 offset:59392\n"
"v_mul_f32 v65, v65, v81\n"
"v_mfma_f32_16x16x32_bf16 v[48:51], a[168:171], v[128:131], 0\n"
"v_rcp_f32 v16, v16\n"
"v_mfma_f32_16x16x32_bf16 v[48:51], a[172:175], v[136:139], v[48:51]\n"
"ds_read_b64_tr_b16 a[188:189], v2 offset:52224\n"
"v_mul_f32 v96, v96, v16\n"
"v_mfma_f32_16x16x32_bf16 v[52:55], a[168:171], v[132:135], 0\n"
"ds_read_b64_tr_b16 a[190:191], v2 offset:60416\n"
"v_mul_f32 v66, v66, v82\n"
"v_mfma_f32_16x16x32_bf16 v[52:55], a[172:175], v[140:143], v[52:55]\n"
"v_exp_f32 v18, v98\n"
"v_mfma_f32_16x16x32_bf16 v[56:59], a[168:171], a[0:3], 0\n"
"v_rcp_f32 v19, v19\n"
"v_mfma_f32_16x16x32_bf16 v[56:59], a[172:175], a[8:11], v[56:59]\n"
"v_rcp_f32 v17, v17\n"
"v_mfma_f32_16x16x32_bf16 v[60:63], a[168:171], a[4:7], 0\n"
"v_mul_f32 v79, v79, v19\n"
"v_add_f32 v18, 1.0, v18\n"
"v_mfma_f32_16x16x32_bf16 v[60:63], a[172:175], a[12:15], v[60:63]\n"
"s_waitcnt lgkmcnt(0)\n"
"v_exp_f32 v19, v99\n"
"v_mfma_f32_16x16x32_bf16 v[32:35], a[176:179], v[144:147], v[32:35]\n"
"s_waitcnt vmcnt(10)\n"
"s_barrier\n"
"ds_read_b64_tr_b16 a[160:161], v3 offset:0\n"
"v_mfma_f32_16x16x32_bf16 v[32:35], a[180:183], v[152:155], v[32:35]\n"
"s_lshl_b32 s97, s73, 11\n"
"ds_read_b64_tr_b16 a[162:163], v3 offset:8192\n"
"v_add_u32 v10, s97, v10\n"
"v_xor_b32 v29, 0x2000, v10\n"
"v_mfma_f32_16x16x32_bf16 v[36:39], a[176:179], v[148:151], v[36:39]\n"
"ds_read_b128 v[20:23], v10 offset:0\n"
"ds_read_b128 v[24:27], v10 offset:1024\n"
"ds_read_b128 a[192:195], v29 offset:0\n"
"v_mfma_f32_16x16x32_bf16 v[36:39], a[180:183], v[156:159], v[36:39]\n"
"s_add_u32 m0, s77, 32768\n"
"ds_read_b128 a[196:199], v29 offset:1024\n"
"ds_read_b64_tr_b16 a[164:165], v3 offset:1024\n"
"v_mfma_f32_16x16x32_bf16 v[40:43], a[176:179], a[16:19], v[40:43]\n"
"s_add_u32 s82, s82, s15\n"
"ds_read_b64_tr_b16 a[166:167], v3 offset:9216\n"
"v_mul_f32 v97, v97, v17\n"
"v_mfma_f32_16x16x32_bf16 v[40:43], a[180:183], a[24:27], v[40:43]\n"
"buffer_load_dwordx4 v11, s[44:47], s82 offen offset:0 lds\n"
"v_mfma_f32_16x16x32_bf16 v[44:47], a[176:179], a[20:23], v[44:47]\n"
"ds_read_b64_tr_b16 a[168:169], v4 offset:0\n"
"v_add_f32 v19, 1.0, v19\n"
"v_mfma_f32_16x16x32_bf16 v[44:47], a[180:183], a[28:31], v[44:47]\n"
"ds_read_b64_tr_b16 a[170:171], v4 offset:8192\n"
"v_mul_f32 v67, v67, v83\n"
"v_mfma_f32_16x16x32_bf16 v[48:51], a[184:187], v[144:147], v[48:51]\n"
"s_waitcnt lgkmcnt(5)\n"
"v_accvgpr_read_b32 v28, a192\n"
"v_add_f32 v20, v20, v28\n"
"v_accvgpr_read_b32 v30, a193\n"
"v_mfma_f32_16x16x32_bf16 v[48:51], a[188:191], v[152:155], v[48:51]\n"
"ds_read_b64_tr_b16 a[172:173], v4 offset:1024\n"
"v_add_f32 v21, v21, v30\n"
"v_accvgpr_read_b32 v28, a194\n"
"v_mfma_f32_16x16x32_bf16 v[52:55], a[184:187], v[148:151], v[52:55]\n"
"s_add_u32 s82, s82, s13\n"
"ds_read_b64_tr_b16 a[174:175], v4 offset:9216\n"
"v_add_f32 v22, v22, v28\n"
"v_accvgpr_read_b32 v30, a195\n"
"v_mfma_f32_16x16x32_bf16 v[52:55], a[188:191], v[156:159], v[52:55]\n"
"buffer_load_dwordx4 v11, s[44:47], s82 offen offset:1024 lds\n"
"v_add_f32 v23, v23, v30\n"
"s_waitcnt lgkmcnt(6)\n"
"v_accvgpr_read_b32 v28, a196\n"
"v_mfma_f32_16x16x32_bf16 v[56:59], a[184:187], a[16:19], v[56:59]\n"
"v_add_f32 v24, v24, v28\n"
"v_accvgpr_read_b32 v30, a197\n"
"v_add_f32 v25, v25, v30\n"
"v_mfma_f32_16x16x32_bf16 v[56:59], a[188:191], a[24:27], v[56:59]\n"
"s_add_u32 s82, s82, s13\n"
"v_accvgpr_read_b32 v28, a198\n"
"v_add_f32 v26, v26, v28\n"
"v_accvgpr_read_b32 v30, a199\n"
"v_mfma_f32_16x16x32_bf16 v[60:63], a[184:187], a[20:23], v[60:63]\n"
"buffer_load_dwordx4 v11, s[44:47], s82 offen offset:2048 lds\n"
"v_add_f32 v27, v27, v30\n"
"v_cvt_pk_bf16_f32 v20, v20, v21\n"
"v_mfma_f32_16x16x32_bf16 v[60:63], a[188:191], a[28:31], v[60:63]\n"
"s_waitcnt lgkmcnt(0)\n"
"v_cvt_pk_bf16_f32 v21, v22, v23\n"
"v_cvt_pk_bf16_f32 v22, v24, v25\n"
"v_cvt_pk_bf16_f32 v23, v26, v27\n"
"v_mfma_f32_16x16x32_bf16 v[32:35], a[160:163], v[160:163], v[32:35]\n"
"ds_read_b64_tr_b16 a[176:177], v3 offset:2048\n"
"v_cvt_pk_bf16_f32 v80, v64, v65\n"
"v_mfma_f32_16x16x32_bf16 v[32:35], a[164:167], v[168:171], v[32:35]\n"
"ds_read_b64_tr_b16 a[178:179], v3 offset:10240\n"
"v_cvt_pk_bf16_f32 v81, v66, v67\n"
"v_mfma_f32_16x16x32_bf16 v[36:39], a[160:163], v[164:167], v[36:39]\n"
"buffer_load_dword a239, v0, s[60:63], s35 offen offset:0\n"
"v_mfma_f32_16x16x32_bf16 v[36:39], a[164:167], v[172:175], v[36:39]\n"
"ds_read_b64_tr_b16 a[180:181], v3 offset:3072\n"
"v_mul_f32 v68, v68, v84\n"
"v_mfma_f32_16x16x32_bf16 v[40:43], a[160:163], a[32:35], v[40:43]\n"
"s_add_u32 s82, s82, s13\n"
"ds_read_b64_tr_b16 a[182:183], v3 offset:11264\n"
"v_mul_f32 v69, v69, v85\n"
"v_mfma_f32_16x16x32_bf16 v[40:43], a[164:167], a[40:43], v[40:43]\n"
"buffer_load_dwordx4 v11, s[44:47], s82 offen offset:3072 lds\n"
"v_mfma_f32_16x16x32_bf16 v[44:47], a[160:163], a[36:39], v[44:47]\n"
"ds_read_b64_tr_b16 a[184:185], v4 offset:2048\n"
"v_mul_f32 v70, v70, v86\n"
"v_mfma_f32_16x16x32_bf16 v[44:47], a[164:167], a[44:47], v[44:47]\n"
"s_cmp_eq_u32 s73, 0\n"
"s_cselect_b64 s[16:17], 0, -1\n"
"s_cselect_b32 s98, -1, s86\n"
"s_not_b32 s97, s86\n"
"s_cmp_eq_u32 s73, 0\n"
"s_cselect_b32 s97, -1, s97\n"
"ds_read_b64_tr_b16 a[186:187], v4 offset:10240\n"
"v_and_b32 v20, s97, v20\n"
"v_and_b32 v21, s98, v21\n"
"v_and_b32 v22, s97, v22\n"
"v_and_b32 v23, s98, v23\n"
"v_mov_b32 v24, v22\n"
"v_mov_b32 v25, v23\n"
"v_mov_b32 v26, v22\n"
"v_mov_b32 v27, v23\n"
"s_nop 0\n"
"v_permlane16_swap_b32 v24, v26\n"
"v_permlane16_swap_b32 v25, v27\n"
"v_mov_b32 v28, v20\n"
"v_mov_b32 v29, v21\n"
"v_mov_b32 v30, v20\n"
"v_mov_b32 v31, v21\n"
"s_nop 0\n"
"v_permlane16_swap_b32 v28, v30\n"
"v_permlane16_swap_b32 v29, v31\n"
"v_cndmask_b32 v28, v28, v20, s[16:17]\n"
"v_cndmask_b32 v29, v29, v21, s[16:17]\n"
"v_cndmask_b32 v30, v30, v22, s[16:17]\n"
"v_cndmask_b32 v31, v31, v23, s[16:17]\n"
"v_and_b32 v22, 0xf0, v9\n"
"v_lshrrev_b32 v20, 9, v9\n"
"v_lshl_add_u32 v20, v20, 12, v22\n"
"v_mov_b32 v23, 0x7ffffff0\n"
"v_and_b32 v21, 0x100, v9\n"
"v_cmp_eq_u32 vcc, 0, v21\n"
"s_nop 1\n"
"v_cndmask_b32 v20, v23, v20, vcc\n"
"v_add_u32 v22, 0x4000, v22\n"
"v_cndmask_b32 v22, v23, v22, s[92:93]\n"
"v_cndmask_b32 v21, v20, v22, s[16:17]\n"
"v_mfma_f32_16x16x32_bf16 v[48:51], a[168:171], v[160:163], v[48:51]\n"
"s_lshl_b32 s96, s75, 1\n"
"s_add_u32 s96, s96, s74\n"
"s_mul_i32 s96, s96, 20480\n"
"s_lshl_b32 s97, s76, 8\n"
"s_add_u32 s96, s96, s97\n"
"s_add_u32 s96, s96, 131072\n"
"s_mul_i32 s97, s73, 0x7f000000\n"
"s_add_u32 s97, s97, s96\n"
"s_add_u32 s97, s97, 8192\n"
"buffer_store_dwordx4 v[28:31], v21, s[64:67], s96 offen sc1\n"
"buffer_store_dwordx4 v[24:27], v20, s[64:67], s97 offen sc1\n"
"v_mfma_f32_16x16x32_bf16 v[48:51], a[172:175], v[168:171], v[48:51]\n"
"s_addk_i32 m0, 16384\n"
"ds_read_b64_tr_b16 a[188:189], v4 offset:3072\n"
"v_mov_b32 v26, 0x7ffffff0\n"
"v_mfma_f32_16x16x32_bf16 v[52:55], a[168:171], v[164:167], v[52:55]\n"
"s_add_u32 s82, s82, s15\n"
"ds_read_b64_tr_b16 a[190:191], v4 offset:11264\n"
"v_cmp_eq_u32 vcc, 0, v9\n"
"v_mfma_f32_16x16x32_bf16 v[52:55], a[172:175], v[172:175], v[52:55]\n"
"buffer_load_dwordx4 v11, s[44:47], s82 offen offset:0 lds\n"
"v_mfma_f32_16x16x32_bf16 v[56:59], a[168:171], a[32:35], v[56:59]\n"
"s_lshl_b32 s96, s75, 2\n"
"v_rcp_f32 v18, v18\n"
"v_mfma_f32_16x16x32_bf16 v[56:59], a[172:175], a[40:43], v[56:59]\n"
"s_add_u32 s82, s82, s13\n"
"v_exp_f32 v16, v112\n"
"v_mfma_f32_16x16x32_bf16 v[60:63], a[168:171], a[36:39], v[60:63]\n"
"buffer_load_dwordx4 v11, s[44:47], s82 offen offset:1024 lds\n"
"v_mfma_f32_16x16x32_bf16 v[60:63], a[172:175], a[44:47], v[60:63]\n"
"s_waitcnt lgkmcnt(0)\n"
"v_cndmask_b32 v26, v26, v9, vcc\n"
"v_mov_b32 v20, s84\n"
"v_mfma_f32_16x16x32_bf16 v[32:35], a[176:179], v[176:179], v[32:35]\n"
"s_add_u32 s96, s96, s72\n"
"ds_read_b64_tr_b16 a[160:161], v1 offset:0\n"
"v_mul_f32 v98, v98, v18\n"
"v_mfma_f32_16x16x32_bf16 v[32:35], a[180:183], v[184:187], v[32:35]\n"
"s_lshl_b32 s96, s96, 7\n"
"ds_read_b64_tr_b16 a[162:163], v1 offset:8192\n"
"v_mov_b32 v21, s85\n"
"v_mfma_f32_16x16x32_bf16 v[36:39], a[176:179], v[180:183], v[36:39]\n"
"s_lshl_b32 s97, s76, 3\n"
"v_rcp_f32 v19, v19\n"
"v_mfma_f32_16x16x32_bf16 v[36:39], a[180:183], v[188:191], v[36:39]\n"
"s_add_u32 s96, s96, s97\n"
"s_add_u32 s96, s96, 4096\n"
"ds_read_b64_tr_b16 a[164:165], v1 offset:1024\n"
"v_mfma_f32_16x16x32_bf16 v[40:43], a[176:179], a[48:51], v[40:43]\n"
"s_xor_b32 s98, s75, 1\n"
"ds_read_b64_tr_b16 a[166:167], v1 offset:9216\n"
"v_subrev_u32 v31, s99, v5\n"
"v_mfma_f32_16x16x32_bf16 v[40:43], a[180:183], a[56:59], v[40:43]\n"
"s_lshl_b32 s16, s98, 9\n"
"v_subrev_u32 v30, s99, v6\n"
"v_add_f32 v16, 1.0, v16\n"
"v_mfma_f32_16x16x32_bf16 v[44:47], a[176:179], a[52:55], v[44:47]\n"
"s_add_u32 s16, s16, 4096\n"
"ds_read_b64_tr_b16 a[168:169], v2 offset:0\n"
"v_mul_f32 v99, v99, v19\n"
"v_mfma_f32_16x16x32_bf16 v[44:47], a[180:183], a[60:63], v[44:47]\n"
"s_add_u32 s82, s82, s13\n"
"ds_read_b64_tr_b16 a[170:171], v2 offset:8192\n"
"v_mul_f32 v71, v71, v87\n"
"v_mfma_f32_16x16x32_bf16 v[48:51], a[184:187], v[176:179], v[48:51]\n"
"buffer_load_dwordx4 v11, s[44:47], s82 offen offset:2048 lds\n"
"v_mfma_f32_16x16x32_bf16 v[48:51], a[188:191], v[184:187], v[48:51]\n"
"ds_read_b64_tr_b16 a[172:173], v2 offset:1024\n"
"v_cvt_pk_bf16_f32 v84, v68, v69\n"
"v_mfma_f32_16x16x32_bf16 v[52:55], a[184:187], v[180:183], v[52:55]\n"
"ds_read_b64_tr_b16 a[174:175], v2 offset:9216\n"
"v_cvt_pk_bf16_f32 v85, v70, v71\n"
"v_mfma_f32_16x16x32_bf16 v[52:55], a[188:191], v[188:191], v[52:55]\n"
"s_mul_i32 s98, s98, 40960\n"
"v_exp_f32 v17, v113\n"
"v_mfma_f32_16x16x32_bf16 v[56:59], a[184:187], a[48:51], v[56:59]\n"
"s_add_u32 s82, s82, s13\n"
"v_rcp_f32 v16, v16\n"
"v_mfma_f32_16x16x32_bf16 v[56:59], a[188:191], a[56:59], v[56:59]\n"
"buffer_load_dwordx4 v11, s[44:47], s82 offen offset:3072 lds\n"
"v_mfma_f32_16x16x32_bf16 v[60:63], a[184:187], a[52:55], v[60:63]\n"
"s_add_u32 s83, s83, s98\n"
"v_add_f32 v17, 1.0, v17\n"
"v_mul_f32 v112, v112, v16\n"
"v_mfma_f32_16x16x32_bf16 v[60:63], a[188:191], a[60:63], v[60:63]\n"
"s_waitcnt lgkmcnt(0)\n"
"ds_write_b64 v31, v[80:81] offset:24576\n"
"v_mfma_f32_16x16x32_bf16 v[32:35], a[160:163], v[192:195], v[32:35]\n"
"s_add_u32 s83, s83, 128000\n"
"ds_read_b64_tr_b16 a[176:177], v1 offset:2048\n"
"v_mul_f32 v72, v72, v88\n"
"v_mfma_f32_16x16x32_bf16 v[32:35], a[164:167], v[200:203], v[32:35]\n"
"ds_read_b64_tr_b16 a[178:179], v1 offset:10240\n"
"v_mul_f32 v73, v73, v89\n"
"v_mfma_f32_16x16x32_bf16 v[36:39], a[160:163], v[196:199], v[36:39]\n"
"v_exp_f32 v18, v114\n"
"v_mfma_f32_16x16x32_bf16 v[36:39], a[164:167], v[204:207], v[36:39]\n"
"ds_read_b64_tr_b16 a[180:181], v1 offset:3072\n"
"v_add_f32 v18, 1.0, v18\n"
"v_mfma_f32_16x16x32_bf16 v[40:43], a[160:163], a[64:67], v[40:43]\n"
"ds_read_b64_tr_b16 a[182:183], v1 offset:11264\n"
"v_mul_f32 v74, v74, v90\n"
"v_mfma_f32_16x16x32_bf16 v[40:43], a[164:167], a[72:75], v[40:43]\n"
"v_rcp_f32 v17, v17\n"
"v_mfma_f32_16x16x32_bf16 v[44:47], a[160:163], a[68:71], v[44:47]\n"
"ds_read_b64_tr_b16 a[184:185], v2 offset:2048\n"
"v_mul_f32 v113, v113, v17\n"
"v_mfma_f32_16x16x32_bf16 v[44:47], a[164:167], a[76:79], v[44:47]\n"
"ds_read_b64_tr_b16 a[186:187], v2 offset:10240\n"
"v_mul_f32 v75, v75, v91\n"
"v_mfma_f32_16x16x32_bf16 v[48:51], a[168:171], v[192:195], v[48:51]\n"
"v_exp_f32 v19, v115\n"
"v_mfma_f32_16x16x32_bf16 v[48:51], a[172:175], v[200:203], v[48:51]\n"
"ds_read_b64_tr_b16 a[188:189], v2 offset:3072\n"
"v_add_f32 v19, 1.0, v19\n"
"v_mfma_f32_16x16x32_bf16 v[52:55], a[168:171], v[196:199], v[52:55]\n"
"ds_read_b64_tr_b16 a[190:191], v2 offset:11264\n"
"v_cvt_pk_bf16_f32 v88, v72, v73\n"
"v_mfma_f32_16x16x32_bf16 v[52:55], a[172:175], v[204:207], v[52:55]\n"
"ds_write_b64 v31, v[84:85] offset:25600\n"
"v_mfma_f32_16x16x32_bf16 v[56:59], a[168:171], a[64:67], v[56:59]\n"
"v_rcp_f32 v18, v18\n"
"v_mfma_f32_16x16x32_bf16 v[56:59], a[172:175], a[72:75], v[56:59]\n"
"v_exp_f32 v16, v100\n"
"v_mfma_f32_16x16x32_bf16 v[60:63], a[168:171], a[68:71], v[60:63]\n"
"v_cvt_pk_bf16_f32 v89, v74, v75\n"
"v_mul_f32 v114, v114, v18\n"
"v_mfma_f32_16x16x32_bf16 v[60:63], a[172:175], a[76:79], v[60:63]\n"
"s_waitcnt lgkmcnt(1)\n"
"v_rcp_f32 v19, v19\n"
"v_mfma_f32_16x16x32_bf16 v[32:35], a[176:179], v[208:211], v[32:35]\n"
"s_waitcnt vmcnt(17)\n"
"s_barrier\n"
"ds_read_b64_tr_b16 a[160:161], v1 offset:16384\n"
"s_waitcnt vmcnt(4)\n"
"buffer_store_dwordx2 v[20:21], v26, s[64:67], s96 offen sc1\n"
"v_mfma_f32_16x16x32_bf16 v[32:35], a[180:183], v[216:219], v[32:35]\n"
"ds_read_b64_tr_b16 a[162:163], v1 offset:24576\n"
"v_add_f32 v16, 1.0, v16\n"
"v_mfma_f32_16x16x32_bf16 v[36:39], a[176:179], v[212:215], v[36:39]\n"
"v_mul_f32 v76, v76, v92\n"
"v_mul_f32 v77, v77, v93\n"
"v_mfma_f32_16x16x32_bf16 v[36:39], a[180:183], v[220:223], v[36:39]\n"
"s_add_u32 m0, s77, 0\n"
"ds_read_b64_tr_b16 a[164:165], v1 offset:17408\n"
"v_mul_f32 v78, v78, v94\n"
"v_mfma_f32_16x16x32_bf16 v[40:43], a[176:179], a[80:83], v[40:43]\n"
"s_add_u32 s82, s82, s15\n"
"ds_read_b64_tr_b16 a[166:167], v1 offset:25600\n"
"v_mul_f32 v115, v115, v19\n"
"v_mfma_f32_16x16x32_bf16 v[40:43], a[180:183], a[88:91], v[40:43]\n"
"buffer_load_dwordx4 v11, s[44:47], s82 offen offset:0 lds\n"
"v_mfma_f32_16x16x32_bf16 v[44:47], a[176:179], a[84:87], v[44:47]\n"
"ds_read_b64_tr_b16 a[168:169], v2 offset:16384\n"
"v_mul_f32 v79, v79, v95\n"
"v_mfma_f32_16x16x32_bf16 v[44:47], a[180:183], a[92:95], v[44:47]\n"
"ds_read_b64_tr_b16 a[170:171], v2 offset:24576\n"
"v_cvt_pk_bf16_f32 v92, v76, v77\n"
"v_mfma_f32_16x16x32_bf16 v[48:51], a[184:187], v[208:211], v[48:51]\n"
"v_exp_f32 v17, v101\n"
"v_mfma_f32_16x16x32_bf16 v[48:51], a[188:191], v[216:219], v[48:51]\n"
"ds_read_b64_tr_b16 a[172:173], v2 offset:17408\n"
"v_cvt_pk_bf16_f32 v93, v78, v79\n"
"v_mfma_f32_16x16x32_bf16 v[52:55], a[184:187], v[212:215], v[52:55]\n"
"s_add_u32 s82, s82, s13\n"
"ds_read_b64_tr_b16 a[174:175], v2 offset:25600\n"
"v_add_f32 v17, 1.0, v17\n"
"v_mfma_f32_16x16x32_bf16 v[52:55], a[188:191], v[220:223], v[52:55]\n"
"buffer_load_dwordx4 v11, s[44:47], s82 offen offset:1024 lds\n"
"v_mfma_f32_16x16x32_bf16 v[56:59], a[184:187], a[80:83], v[56:59]\n"
"ds_write_b64 v30, v[88:89] offset:24576\n"
"v_mfma_f32_16x16x32_bf16 v[56:59], a[188:191], a[88:91], v[56:59]\n"
"s_add_u32 s82, s82, s13\n"
"v_rcp_f32 v16, v16\n"
"v_mfma_f32_16x16x32_bf16 v[60:63], a[184:187], a[84:87], v[60:63]\n"
"buffer_load_dwordx4 v11, s[44:47], s82 offen offset:2048 lds\n"
"v_mfma_f32_16x16x32_bf16 v[60:63], a[188:191], a[92:95], v[60:63]\n"
"s_waitcnt lgkmcnt(1)\n"
"v_exp_f32 v18, v102\n"
"v_mfma_f32_16x16x32_bf16 v[32:35], a[160:163], v[224:227], v[32:35]\n"
"ds_read_b64_tr_b16 a[176:177], v1 offset:18432\n"
"v_mul_f32 v100, v100, v16\n"
"v_mfma_f32_16x16x32_bf16 v[32:35], a[164:167], v[232:235], v[32:35]\n"
"ds_read_b64_tr_b16 a[178:179], v1 offset:26624\n"
"v_add_f32 v18, 1.0, v18\n"
"v_mfma_f32_16x16x32_bf16 v[36:39], a[160:163], v[228:231], v[36:39]\n"
"v_rcp_f32 v17, v17\n"
"v_mfma_f32_16x16x32_bf16 v[36:39], a[164:167], v[236:239], v[36:39]\n"
"ds_read_b64_tr_b16 a[180:181], v1 offset:19456\n"
"v_mul_f32 v101, v101, v17\n"
"v_mfma_f32_16x16x32_bf16 v[40:43], a[160:163], a[96:99], v[40:43]\n"
"s_add_u32 s82, s82, s13\n"
"ds_read_b64_tr_b16 a[182:183], v1 offset:27648\n"
"v_lshrrev_b32 v29, 1, v9\n"
"v_mfma_f32_16x16x32_bf16 v[40:43], a[164:167], a[104:107], v[40:43]\n"
"buffer_load_dwordx4 v11, s[44:47], s82 offen offset:3072 lds\n"
"v_mfma_f32_16x16x32_bf16 v[44:47], a[160:163], a[100:103], v[44:47]\n"
"ds_read_b64_tr_b16 a[184:185], v2 offset:18432\n"
"v_subrev_u32 v12, s99, v5\n"
"v_mfma_f32_16x16x32_bf16 v[44:47], a[164:167], a[108:111], v[44:47]\n"
"ds_read_b64_tr_b16 a[186:187], v2 offset:26624\n"
"v_mfma_f32_16x16x32_bf16 v[48:51], a[168:171], v[224:227], v[48:51]\n"
"ds_write_b64 v30, v[92:93] offset:25600\n"
"v_mfma_f32_16x16x32_bf16 v[48:51], a[172:175], v[232:235], v[48:51]\n"
"s_add_u32 m0, s77, 65536\n"
"ds_read_b64_tr_b16 a[188:189], v2 offset:19456\n"
"v_mfma_f32_16x16x32_bf16 v[52:55], a[168:171], v[228:231], v[52:55]\n"
"s_add_u32 s82, s82, s15\n"
"ds_read_b64_tr_b16 a[190:191], v2 offset:27648\n"
"v_mfma_f32_16x16x32_bf16 v[52:55], a[172:175], v[236:239], v[52:55]\n"
"buffer_load_dwordx4 v11, s[44:47], s82 offen offset:0 lds\n"
"v_mfma_f32_16x16x32_bf16 v[56:59], a[168:171], a[96:99], v[56:59]\n"
"v_exp_f32 v19, v103\n"
"v_mfma_f32_16x16x32_bf16 v[56:59], a[172:175], a[104:107], v[56:59]\n"
"s_add_u32 s82, s82, s13\n"
"v_rcp_f32 v18, v18\n"
"v_mfma_f32_16x16x32_bf16 v[60:63], a[168:171], a[100:103], v[60:63]\n"
"buffer_load_dwordx4 v11, s[44:47], s82 offen offset:1024 lds\n"
"v_mfma_f32_16x16x32_bf16 v[60:63], a[172:175], a[108:111], v[60:63]\n"
"s_waitcnt lgkmcnt(0)\n"
"v_add_f32 v19, 1.0, v19\n"
"v_mul_f32 v102, v102, v18\n"
"v_mfma_f32_16x16x32_bf16 v[32:35], a[176:179], v[240:243], v[32:35]\n"
"s_barrier\n"
"ds_read_b64_tr_b16 a[160:161], v3 offset:16384\n"
"v_mfma_f32_16x16x32_bf16 v[32:35], a[180:183], v[248:251], v[32:35]\n"
"s_lshl_b32 s96, s72, 11\n"
"s_add_u32 s97, s96, 98304\n"
"ds_read_b64_tr_b16 a[162:163], v3 offset:24576\n"
"v_mfma_f32_16x16x32_bf16 v[36:39], a[176:179], v[244:247], v[36:39]\n"
"s_add_u32 s96, s96, s9\n"
"v_exp_f32 v16, v116\n"
"v_mfma_f32_16x16x32_bf16 v[36:39], a[180:183], v[252:255], v[36:39]\n"
"ds_read_b64_tr_b16 a[164:165], v3 offset:17408\n"
"v_add_f32 v16, 1.0, v16\n"
"v_mfma_f32_16x16x32_bf16 v[40:43], a[176:179], a[112:115], v[40:43]\n"
"s_add_u32 s82, s82, s13\n"
"ds_read_b64_tr_b16 a[166:167], v3 offset:25600\n"
"v_add_u32 v10, s97, v9\n"
"v_mfma_f32_16x16x32_bf16 v[40:43], a[180:183], a[120:123], v[40:43]\n"
"buffer_load_dwordx4 v11, s[44:47], s82 offen offset:2048 lds\n"
"v_mfma_f32_16x16x32_bf16 v[44:47], a[176:179], a[116:119], v[44:47]\n"
"ds_read_b64_tr_b16 a[168:169], v4 offset:16384\n"
"ds_read_b128 a[240:243], v10 offset:8192\n"
"v_mfma_f32_16x16x32_bf16 v[44:47], a[180:183], a[124:127], v[44:47]\n"
"ds_read_b64_tr_b16 a[170:171], v4 offset:24576\n"
"ds_read_b128 a[244:247], v10 offset:9216\n"
"v_mfma_f32_16x16x32_bf16 v[48:51], a[184:187], v[240:243], v[48:51]\n"
"s_add_u32 s97, s96, 147456\n"
"v_rcp_f32 v19, v19\n"
"v_mfma_f32_16x16x32_bf16 v[48:51], a[188:191], v[248:251], v[48:51]\n"
"s_and_b32 s96, s75, 1\n"
"ds_read_b64_tr_b16 a[172:173], v4 offset:17408\n"
"v_mul_f32 v103, v103, v19\n"
"v_mfma_f32_16x16x32_bf16 v[52:55], a[184:187], v[244:247], v[52:55]\n"
"s_add_u32 s82, s82, s13\n"
"s_mul_i32 s40, s96, 368640\n"
"ds_read_b64_tr_b16 a[174:175], v4 offset:25600\n"
"v_mfma_f32_16x16x32_bf16 v[52:55], a[188:191], v[252:255], v[52:55]\n"
"buffer_load_dwordx4 v11, s[44:47], s82 offen offset:3072 lds\n"
"v_mfma_f32_16x16x32_bf16 v[56:59], a[184:187], a[112:115], v[56:59]\n"
"s_add_u32 m0, s77, 16384\n"
"v_exp_f32 v17, v117\n"
"v_mfma_f32_16x16x32_bf16 v[56:59], a[188:191], a[120:123], v[56:59]\n"
"s_add_u32 s82, s78, 2304\n"
"v_rcp_f32 v16, v16\n"
"v_mfma_f32_16x16x32_bf16 v[60:63], a[184:187], a[116:119], v[60:63]\n"
"buffer_load_dwordx4 v11, s[48:51], s82 offen offset:2048 lds\n"
"v_mfma_f32_16x16x32_bf16 v[60:63], a[188:191], a[124:127], v[60:63]\n"
"s_waitcnt lgkmcnt(0)\n"
"v_add_f32 v17, 1.0, v17\n"
"v_mul_f32 v116, v116, v16\n"
"v_mfma_f32_16x16x32_bf16 v[32:35], a[160:163], a[144:147], v[32:35]\n"
"s_add_u32 s40, s40, s78\n"
"s_mul_i32 s4, s96, 368640\n"
"ds_read_b64_tr_b16 a[176:177], v3 offset:18432\n"
"v_mfma_f32_16x16x32_bf16 v[32:35], a[164:167], a[152:155], v[32:35]\n"
"s_add_u32 s4, s4, s79\n"
"s_mul_i32 s41, s96, 36864\n"
"ds_read_b64_tr_b16 a[178:179], v3 offset:26624\n"
"v_mfma_f32_16x16x32_bf16 v[36:39], a[160:163], a[148:151], v[36:39]\n"
"s_add_u32 s41, s41, s78\n"
"v_exp_f32 v18, v118\n"
"v_mfma_f32_16x16x32_bf16 v[36:39], a[164:167], a[156:159], v[36:39]\n"
"s_add_u32 s41, s41, 294912\n"
"ds_read_b64_tr_b16 a[180:181], v3 offset:19456\n"
"v_add_f32 v18, 1.0, v18\n"
"v_mfma_f32_16x16x32_bf16 v[40:43], a[160:163], a[128:131], v[40:43]\n"
"ds_read_b64_tr_b16 a[182:183], v3 offset:27648\n"
"v_mul_f32 v96, v96, v32\n"
"v_mfma_f32_16x16x32_bf16 v[40:43], a[164:167], a[136:139], v[40:43]\n"
"v_rcp_f32 v17, v17\n"
"v_mfma_f32_16x16x32_bf16 v[44:47], a[160:163], a[132:135], v[44:47]\n"
"ds_read_b64_tr_b16 a[184:185], v4 offset:18432\n"
"v_mul_f32 v117, v117, v17\n"
"v_mfma_f32_16x16x32_bf16 v[44:47], a[164:167], a[140:143], v[44:47]\n"
"ds_read_b64_tr_b16 a[186:187], v4 offset:26624\n"
"v_mul_f32 v97, v97, v33\n"
"v_mfma_f32_16x16x32_bf16 v[48:51], a[168:171], a[144:147], v[48:51]\n"
"v_exp_f32 v19, v119\n"
"v_mfma_f32_16x16x32_bf16 v[48:51], a[172:175], a[152:155], v[48:51]\n"
"ds_read_b64_tr_b16 a[188:189], v4 offset:19456\n"
"v_add_f32 v19, 1.0, v19\n"
"v_mfma_f32_16x16x32_bf16 v[52:55], a[168:171], a[148:151], v[52:55]\n"
"ds_read_b64_tr_b16 a[190:191], v4 offset:27648\n"
"v_mul_f32 v98, v98, v34\n"
"v_mfma_f32_16x16x32_bf16 v[52:55], a[172:175], a[156:159], v[52:55]\n"
"s_add_u32 s82, s82, s13\n"
"v_rcp_f32 v18, v18\n"
"v_mfma_f32_16x16x32_bf16 v[56:59], a[168:171], a[128:131], v[56:59]\n"
"buffer_load_dwordx4 v11, s[48:51], s82 offen offset:3072 lds\n"
"v_mfma_f32_16x16x32_bf16 v[56:59], a[172:175], a[136:139], v[56:59]\n"
"v_exp_f32 v16, v104\n"
"v_mfma_f32_16x16x32_bf16 v[60:63], a[168:171], a[132:135], v[60:63]\n"
"v_mul_f32 v118, v118, v18\n"
"v_add_f32 v16, 1.0, v16\n"
"v_mfma_f32_16x16x32_bf16 v[60:63], a[172:175], a[140:143], v[60:63]\n"
"s_waitcnt lgkmcnt(0)\n"
"v_rcp_f32 v19, v19\n"
"v_mfma_f32_16x16x32_bf16 v[64:67], a[176:179], v[128:131], 0\n"
"s_barrier\n"
"ds_read_b64_tr_b16 a[160:161], v1 offset:32768\n"
"v_mfma_f32_16x16x32_bf16 v[64:67], a[180:183], v[136:139], v[64:67]\n"
"ds_read_b64_tr_b16 a[162:163], v1 offset:40960\n"
"v_mul_f32 v119, v119, v19\n"
"v_mfma_f32_16x16x32_bf16 v[68:71], a[176:179], v[132:135], 0\n"
"v_exp_f32 v17, v105\n"
"v_mfma_f32_16x16x32_bf16 v[68:71], a[180:183], v[140:143], v[68:71]\n"
"ds_read_b64_tr_b16 a[164:165], v1 offset:33792\n"
"v_add_f32 v17, 1.0, v17\n"
"v_mfma_f32_16x16x32_bf16 v[72:75], a[176:179], a[0:3], 0\n"
"ds_read_b64_tr_b16 a[166:167], v1 offset:41984\n"
"v_mul_f32 v99, v99, v35\n"
"v_mfma_f32_16x16x32_bf16 v[72:75], a[180:183], a[8:11], v[72:75]\n"
"v_rcp_f32 v16, v16\n"
"v_mfma_f32_16x16x32_bf16 v[76:79], a[176:179], a[4:7], 0\n"
"s_add_u32 m0, s77, 81920\n"
"ds_read_b64_tr_b16 a[168:169], v2 offset:32768\n"
"v_mul_f32 v104, v104, v16\n"
"v_mfma_f32_16x16x32_bf16 v[76:79], a[180:183], a[12:15], v[76:79]\n"
"s_add_u32 s82, s78, 592128\n"
"ds_read_b64_tr_b16 a[170:171], v2 offset:40960\n"
"v_mul_f32 v112, v112, v48\n"
"v_mfma_f32_16x16x32_bf16 v[80:83], a[184:187], v[128:131], 0\n"
"buffer_load_dwordx4 v11, s[48:51], s82 offen offset:2048 lds\n"
"v_mfma_f32_16x16x32_bf16 v[80:83], a[188:191], v[136:139], v[80:83]\n"
"ds_read_b64_tr_b16 a[172:173], v2 offset:33792\n"
"v_mul_f32 v113, v113, v49\n"
"v_mfma_f32_16x16x32_bf16 v[84:87], a[184:187], v[132:135], 0\n"
"ds_read_b64_tr_b16 a[174:175], v2 offset:41984\n"
"v_mul_f32 v114, v114, v50\n"
"v_mfma_f32_16x16x32_bf16 v[84:87], a[188:191], v[140:143], v[84:87]\n"
"v_exp_f32 v18, v106\n"
"v_mfma_f32_16x16x32_bf16 v[88:91], a[184:187], a[0:3], 0\n"
"s_add_u32 s82, s82, s13\n"
"v_rcp_f32 v17, v17\n"
"v_mfma_f32_16x16x32_bf16 v[88:91], a[188:191], a[8:11], v[88:91]\n"
"buffer_load_dwordx4 v11, s[48:51], s82 offen offset:3072 lds\n"
"v_mfma_f32_16x16x32_bf16 v[92:95], a[184:187], a[4:7], 0\n"
"v_add_f32 v18, 1.0, v18\n"
"v_mul_f32 v105, v105, v17\n"
"v_mfma_f32_16x16x32_bf16 v[92:95], a[188:191], a[12:15], v[92:95]\n"
"s_waitcnt lgkmcnt(0)\n"
"v_exp_f32 v19, v107\n"
"v_mfma_f32_16x16x32_bf16 v[64:67], a[160:163], v[144:147], v[64:67]\n"
"ds_read_b64_tr_b16 a[176:177], v1 offset:34816\n"
"v_add_f32 v19, 1.0, v19\n"
"v_mfma_f32_16x16x32_bf16 v[64:67], a[164:167], v[152:155], v[64:67]\n"
"ds_read_b64_tr_b16 a[178:179], v1 offset:43008\n"
"v_mul_f32 v115, v115, v51\n"
"v_mfma_f32_16x16x32_bf16 v[68:71], a[160:163], v[148:151], v[68:71]\n"
"v_rcp_f32 v18, v18\n"
"v_mfma_f32_16x16x32_bf16 v[68:71], a[164:167], v[156:159], v[68:71]\n"
"ds_read_b64_tr_b16 a[180:181], v1 offset:35840\n"
"v_mul_f32 v106, v106, v18\n"
"v_mfma_f32_16x16x32_bf16 v[72:75], a[160:163], a[16:19], v[72:75]\n"
"ds_read_b64_tr_b16 a[182:183], v1 offset:44032\n"
"v_cvt_pk_bf16_f32 v32, v96, v97\n"
"v_mfma_f32_16x16x32_bf16 v[72:75], a[164:167], a[24:27], v[72:75]\n"
"v_exp_f32 v16, v120\n"
"v_mfma_f32_16x16x32_bf16 v[76:79], a[160:163], a[20:23], v[76:79]\n"
"ds_read_b64_tr_b16 a[184:185], v2 offset:34816\n"
"v_add_f32 v16, 1.0, v16\n"
"v_mfma_f32_16x16x32_bf16 v[76:79], a[164:167], a[28:31], v[76:79]\n"
"ds_read_b64_tr_b16 a[186:187], v2 offset:43008\n"
"v_cvt_pk_bf16_f32 v33, v98, v99\n"
"v_mfma_f32_16x16x32_bf16 v[80:83], a[168:171], v[144:147], v[80:83]\n"
"v_rcp_f32 v19, v19\n"
"v_mfma_f32_16x16x32_bf16 v[80:83], a[172:175], v[152:155], v[80:83]\n"
"ds_read_b64_tr_b16 a[188:189], v2 offset:35840\n"
"v_mul_f32 v107, v107, v19\n"
"v_mfma_f32_16x16x32_bf16 v[84:87], a[168:171], v[148:151], v[84:87]\n"
"ds_read_b64_tr_b16 a[190:191], v2 offset:44032\n"
"v_cvt_pk_bf16_f32 v34, v112, v113\n"
"v_mfma_f32_16x16x32_bf16 v[84:87], a[172:175], v[156:159], v[84:87]\n"
"v_exp_f32 v17, v121\n"
"v_mfma_f32_16x16x32_bf16 v[88:91], a[168:171], a[16:19], v[88:91]\n"
"v_rcp_f32 v16, v16\n"
"v_mfma_f32_16x16x32_bf16 v[88:91], a[172:175], a[24:27], v[88:91]\n"
"v_add_f32 v17, 1.0, v17\n"
"v_mul_f32 v120, v120, v16\n"
"v_mfma_f32_16x16x32_bf16 v[92:95], a[168:171], a[20:23], v[92:95]\n"
"v_exp_f32 v18, v122\n"
"v_mfma_f32_16x16x32_bf16 v[92:95], a[172:175], a[28:31], v[92:95]\n"
"s_waitcnt lgkmcnt(0)\n"
"v_rcp_f32 v17, v17\n"
"v_mfma_f32_16x16x32_bf16 v[64:67], a[176:179], v[160:163], v[64:67]\n"
"s_waitcnt vmcnt(13)\n"
"s_barrier\n"
"ds_read_b64_tr_b16 a[160:161], v1 offset:49152\n"
"v_mfma_f32_16x16x32_bf16 v[64:67], a[180:183], v[168:171], v[64:67]\n"
"ds_read_b64_tr_b16 a[162:163], v1 offset:57344\n"
"v_add_f32 v18, 1.0, v18\n"
"v_mfma_f32_16x16x32_bf16 v[68:71], a[176:179], v[164:167], v[68:71]\n"
"buffer_load_dwordx2 v[30:31], v29, s[64:67], s16 offen sc1\n"
"v_mfma_f32_16x16x32_bf16 v[68:71], a[180:183], v[172:175], v[68:71]\n"
"ds_read_b64_tr_b16 a[164:165], v1 offset:50176\n"
"v_mul_f32 v121, v121, v17\n"
"v_mfma_f32_16x16x32_bf16 v[72:75], a[176:179], a[32:35], v[72:75]\n"
"ds_read_b64_tr_b16 a[166:167], v1 offset:58368\n"
"v_cvt_pk_bf16_f32 v35, v114, v115\n"
"v_mfma_f32_16x16x32_bf16 v[72:75], a[180:183], a[40:43], v[72:75]\n"
"v_exp_f32 v19, v123\n"
"v_mfma_f32_16x16x32_bf16 v[76:79], a[176:179], a[36:39], v[76:79]\n"
"s_add_u32 m0, s77, 32768\n"
"ds_read_b64_tr_b16 a[168:169], v2 offset:49152\n"
"v_add_f32 v19, 1.0, v19\n"
"v_mfma_f32_16x16x32_bf16 v[76:79], a[180:183], a[44:47], v[76:79]\n"
"s_add_u32 s82, s78, 1181952\n"
"ds_read_b64_tr_b16 a[170:171], v2 offset:57344\n"
"v_mul_f32 v100, v100, v36\n"
"v_mfma_f32_16x16x32_bf16 v[80:83], a[184:187], v[160:163], v[80:83]\n"
"buffer_load_dwordx4 v11, s[48:51], s82 offen offset:2048 lds\n"
"v_mfma_f32_16x16x32_bf16 v[80:83], a[188:191], v[168:171], v[80:83]\n"
"ds_read_b64_tr_b16 a[172:173], v2 offset:50176\n"
"v_mul_f32 v101, v101, v37\n"
"v_mfma_f32_16x16x32_bf16 v[84:87], a[184:187], v[164:167], v[84:87]\n"
"ds_read_b64_tr_b16 a[174:175], v2 offset:58368\n"
"v_mul_f32 v102, v102, v38\n"
"v_mfma_f32_16x16x32_bf16 v[84:87], a[188:191], v[172:175], v[84:87]\n"
"v_rcp_f32 v18, v18\n"
"v_mfma_f32_16x16x32_bf16 v[88:91], a[184:187], a[32:35], v[88:91]\n"
"s_add_u32 s82, s82, s13\n"
"v_exp_f32 v16, v108\n"
"v_mfma_f32_16x16x32_bf16 v[88:91], a[188:191], a[40:43], v[88:91]\n"
"buffer_load_dwordx4 v11, s[48:51], s82 offen offset:3072 lds\n"
"v_mfma_f32_16x16x32_bf16 v[92:95], a[184:187], a[36:39], v[92:95]\n"
"v_mul_f32 v122, v122, v18\n"
"v_add_f32 v16, 1.0, v16\n"
"v_mfma_f32_16x16x32_bf16 v[92:95], a[188:191], a[44:47], v[92:95]\n"
"s_waitcnt lgkmcnt(0)\n"
"v_rcp_f32 v19, v19\n"
"v_mfma_f32_16x16x32_bf16 v[64:67], a[160:163], v[176:179], v[64:67]\n"
"ds_read_b64_tr_b16 a[176:177], v1 offset:51200\n"
"v_mul_f32 v123, v123, v19\n"
"v_mfma_f32_16x16x32_bf16 v[64:67], a[164:167], v[184:187], v[64:67]\n"
"ds_read_b64_tr_b16 a[178:179], v1 offset:59392\n"
"v_mul_f32 v103, v103, v39\n"
"v_mfma_f32_16x16x32_bf16 v[68:71], a[160:163], v[180:183], v[68:71]\n"
"v_exp_f32 v17, v109\n"
"v_mfma_f32_16x16x32_bf16 v[68:71], a[164:167], v[188:191], v[68:71]\n"
"ds_read_b64_tr_b16 a[180:181], v1 offset:52224\n"
"v_add_f32 v17, 1.0, v17\n"
"v_mfma_f32_16x16x32_bf16 v[72:75], a[160:163], a[48:51], v[72:75]\n"
"ds_read_b64_tr_b16 a[182:183], v1 offset:60416\n"
"v_mul_f32 v116, v116, v52\n"
"v_mfma_f32_16x16x32_bf16 v[72:75], a[164:167], a[56:59], v[72:75]\n"
"v_rcp_f32 v16, v16\n"
"v_mfma_f32_16x16x32_bf16 v[76:79], a[160:163], a[52:55], v[76:79]\n"
"ds_read_b64_tr_b16 a[184:185], v2 offset:51200\n"
"v_mul_f32 v108, v108, v16\n"
"v_mfma_f32_16x16x32_bf16 v[76:79], a[164:167], a[60:63], v[76:79]\n"
"ds_read_b64_tr_b16 a[186:187], v2 offset:59392\n"
"v_mul_f32 v117, v117, v53\n"
"v_mfma_f32_16x16x32_bf16 v[80:83], a[168:171], v[176:179], v[80:83]\n"
"ds_write_b128 v5, v[32:35] offset:32768\n"
"v_mfma_f32_16x16x32_bf16 v[80:83], a[172:175], v[184:187], v[80:83]\n"
"ds_read_b64_tr_b16 a[188:189], v2 offset:52224\n"
"v_mul_f32 v118, v118, v54\n"
"v_mfma_f32_16x16x32_bf16 v[84:87], a[168:171], v[180:183], v[84:87]\n"
"ds_read_b64_tr_b16 a[190:191], v2 offset:60416\n"
"v_mul_f32 v119, v119, v55\n"
"v_mfma_f32_16x16x32_bf16 v[84:87], a[172:175], v[188:191], v[84:87]\n"
"v_exp_f32 v18, v110\n"
"v_mfma_f32_16x16x32_bf16 v[88:91], a[168:171], a[48:51], v[88:91]\n"
"v_rcp_f32 v17, v17\n"
"v_mfma_f32_16x16x32_bf16 v[88:91], a[172:175], a[56:59], v[88:91]\n"
"v_add_f32 v18, 1.0, v18\n"
"v_mul_f32 v109, v109, v17\n"
"v_mfma_f32_16x16x32_bf16 v[92:95], a[168:171], a[52:55], v[92:95]\n"
"v_exp_f32 v19, v111\n"
"v_mfma_f32_16x16x32_bf16 v[92:95], a[172:175], a[60:63], v[92:95]\n"
"s_waitcnt lgkmcnt(0)\n"
"v_rcp_f32 v18, v18\n"
"v_mfma_f32_16x16x32_bf16 v[64:67], a[176:179], v[192:195], v[64:67]\n"
"s_waitcnt vmcnt(11)\n"
"s_barrier\n"
"ds_read_b64_tr_b16 a[160:161], v1 offset:0\n"
"v_mfma_f32_16x16x32_bf16 v[64:67], a[180:183], v[200:203], v[64:67]\n"
"ds_read_b64_tr_b16 a[162:163], v1 offset:8192\n"
"v_cvt_pk_bf16_f32 v36, v100, v101\n"
"v_mfma_f32_16x16x32_bf16 v[68:71], a[176:179], v[196:199], v[68:71]\n"
"buffer_store_dwordx4 v[32:35], v5, s[64:67], s18 offen offset:0 sc1\n"
"v_mfma_f32_16x16x32_bf16 v[68:71], a[180:183], v[204:207], v[68:71]\n"
"ds_read_b64_tr_b16 a[164:165], v1 offset:1024\n"
"v_cvt_pk_bf16_f32 v37, v102, v103\n"
"v_mfma_f32_16x16x32_bf16 v[72:75], a[176:179], a[64:67], v[72:75]\n"
"ds_read_b64_tr_b16 a[166:167], v1 offset:9216\n"
"v_add_f32 v19, 1.0, v19\n"
"v_mfma_f32_16x16x32_bf16 v[72:75], a[180:183], a[72:75], v[72:75]\n"
"v_cvt_pk_bf16_f32 v38, v116, v117\n"
"v_cvt_pk_bf16_f32 v39, v118, v119\n"
"v_mfma_f32_16x16x32_bf16 v[76:79], a[176:179], a[68:71], v[76:79]\n"
"s_addk_i32 m0, 16384\n"
"ds_read_b64_tr_b16 a[168:169], v2 offset:0\n"
"v_mul_f32 v110, v110, v18\n"
"v_mfma_f32_16x16x32_bf16 v[76:79], a[180:183], a[76:79], v[76:79]\n"
"s_add_u32 s82, s82, s15\n"
"ds_read_b64_tr_b16 a[170:171], v2 offset:8192\n"
"v_mul_f32 v104, v104, v40\n"
"v_mfma_f32_16x16x32_bf16 v[80:83], a[184:187], v[192:195], v[80:83]\n"
"buffer_load_dwordx4 v11, s[48:51], s82 offen offset:0 lds\n"
"v_mfma_f32_16x16x32_bf16 v[80:83], a[188:191], v[200:203], v[80:83]\n"
"ds_read_b64_tr_b16 a[172:173], v2 offset:1024\n"
"v_mul_f32 v105, v105, v41\n"
"v_mfma_f32_16x16x32_bf16 v[84:87], a[184:187], v[196:199], v[84:87]\n"
"ds_read_b64_tr_b16 a[174:175], v2 offset:9216\n"
"v_mul_f32 v106, v106, v42\n"
"v_mfma_f32_16x16x32_bf16 v[84:87], a[188:191], v[204:207], v[84:87]\n"
"v_exp_f32 v16, v124\n"
"v_mfma_f32_16x16x32_bf16 v[88:91], a[184:187], a[64:67], v[88:91]\n"
"s_add_u32 s82, s82, s13\n"
"ds_write_b128 v5, v[36:39] offset:33792\n"
"v_mfma_f32_16x16x32_bf16 v[88:91], a[188:191], a[72:75], v[88:91]\n"
"buffer_load_dwordx4 v11, s[48:51], s82 offen offset:1024 lds\n"
"v_mfma_f32_16x16x32_bf16 v[92:95], a[184:187], a[68:71], v[92:95]\n"
"v_rcp_f32 v19, v19\n"
"v_mfma_f32_16x16x32_bf16 v[92:95], a[188:191], a[76:79], v[92:95]\n"
"s_waitcnt lgkmcnt(1)\n"
"v_add_f32 v16, 1.0, v16\n"
"v_mul_f32 v111, v111, v19\n"
"v_mfma_f32_16x16x32_bf16 v[64:67], a[160:163], v[208:211], v[64:67]\n"
"ds_read_b64_tr_b16 a[176:177], v1 offset:2048\n"
"v_mul_f32 v107, v107, v43\n"
"v_mfma_f32_16x16x32_bf16 v[64:67], a[164:167], v[216:219], v[64:67]\n"
"ds_read_b64_tr_b16 a[178:179], v1 offset:10240\n"
"v_mul_f32 v120, v120, v56\n"
"v_mfma_f32_16x16x32_bf16 v[68:71], a[160:163], v[212:215], v[68:71]\n"
"v_exp_f32 v17, v125\n"
"v_mfma_f32_16x16x32_bf16 v[68:71], a[164:167], v[220:223], v[68:71]\n"
"ds_read_b64_tr_b16 a[180:181], v1 offset:3072\n"
"v_mul_f32 v121, v121, v57\n"
"v_mfma_f32_16x16x32_bf16 v[72:75], a[160:163], a[80:83], v[72:75]\n"
"ds_read_b64_tr_b16 a[182:183], v1 offset:11264\n"
"v_add_f32 v17, 1.0, v17\n"
"v_mfma_f32_16x16x32_bf16 v[72:75], a[164:167], a[88:91], v[72:75]\n"
"v_rcp_f32 v16, v16\n"
"v_mfma_f32_16x16x32_bf16 v[76:79], a[160:163], a[84:87], v[76:79]\n"
"ds_read_b64_tr_b16 a[184:185], v2 offset:2048\n"
"v_mul_f32 v122, v122, v58\n"
"v_mfma_f32_16x16x32_bf16 v[76:79], a[164:167], a[92:95], v[76:79]\n"
"ds_read_b64_tr_b16 a[186:187], v2 offset:10240\n"
"v_mul_f32 v123, v123, v59\n"
"v_mfma_f32_16x16x32_bf16 v[80:83], a[168:171], v[208:211], v[80:83]\n"
"buffer_store_dwordx4 v[36:39], v5, s[64:67], s18 offen offset:1024 sc1\n"
"v_mfma_f32_16x16x32_bf16 v[80:83], a[172:175], v[216:219], v[80:83]\n"
"ds_read_b64_tr_b16 a[188:189], v2 offset:3072\n"
"v_cvt_pk_bf16_f32 v40, v104, v105\n"
"v_mfma_f32_16x16x32_bf16 v[84:87], a[168:171], v[212:215], v[84:87]\n"
"ds_read_b64_tr_b16 a[190:191], v2 offset:11264\n"
"v_mul_f32 v124, v124, v16\n"
"v_mfma_f32_16x16x32_bf16 v[84:87], a[172:175], v[220:223], v[84:87]\n"
"v_exp_f32 v18, v126\n"
"v_mfma_f32_16x16x32_bf16 v[88:91], a[168:171], a[80:83], v[88:91]\n"
"v_cvt_pk_bf16_f32 v41, v106, v107\n"
"v_cvt_pk_bf16_f32 v42, v120, v121\n"
"v_mfma_f32_16x16x32_bf16 v[88:91], a[172:175], a[88:91], v[88:91]\n"
"v_rcp_f32 v17, v17\n"
"v_mfma_f32_16x16x32_bf16 v[92:95], a[168:171], a[84:87], v[92:95]\n"
"v_add_f32 v18, 1.0, v18\n"
"v_cvt_pk_bf16_f32 v43, v122, v123\n"
"v_mfma_f32_16x16x32_bf16 v[92:95], a[172:175], a[92:95], v[92:95]\n"
"s_waitcnt lgkmcnt(0)\n"
"s_waitcnt vmcnt(6)\n"
"v_cmp_ne_u64 vcc, v[30:31], v[14:15]\n"
"s_nop 1\n"
"s_cbranch_vccz L_ok1_%=\n"
"s_mov_b32 s100, 0x4000\n"
"L_rt1_%=:\n"
"s_sleep 2\n"
"buffer_load_dwordx2 v[30:31], v29, s[64:67], s16 offen sc1\n"
"s_waitcnt vmcnt(0)\n"
"v_cmp_ne_u64 vcc, v[30:31], v[14:15]\n"
"s_nop 4\n"
"s_cbranch_vccz L_ok1_%=\n"
"s_sub_u32 s100, s100, 1\n"
"s_cmp_lg_u32 s100, 0\n"
"s_cbranch_scc1 L_rt1_%=\n"
"L_ok1_%=:\n"
"v_mfma_f32_16x16x32_bf16 v[64:67], a[176:179], v[224:227], v[64:67]\n"
"s_add_u32 m0, s77, 16384\n"
"s_barrier\n"
"ds_read_b64_tr_b16 a[160:161], v3 offset:0\n"
"v_mfma_f32_16x16x32_bf16 v[64:67], a[180:183], v[232:235], v[64:67]\n"
"s_add_u32 s82, s83, 3072\n"
"ds_read_b64_tr_b16 a[162:163], v3 offset:8192\n"
"v_mul_f32 v125, v125, v17\n"
"v_mfma_f32_16x16x32_bf16 v[68:71], a[176:179], v[228:231], v[68:71]\n"
"buffer_load_dwordx4 v9, s[64:67], s82 offen offset:0 sc1 lds\n"
"v_mfma_f32_16x16x32_bf16 v[68:71], a[180:183], v[236:239], v[68:71]\n"
"ds_read_b64_tr_b16 a[164:165], v3 offset:1024\n"
"v_mul_f32 v108, v108, v44\n"
"v_mfma_f32_16x16x32_bf16 v[72:75], a[176:179], a[96:99], v[72:75]\n"
"ds_read_b64_tr_b16 a[166:167], v3 offset:9216\n"
"v_mul_f32 v109, v109, v45\n"
"v_mfma_f32_16x16x32_bf16 v[72:75], a[180:183], a[104:107], v[72:75]\n"
"ds_write_b128 v6, v[40:43] offset:32768\n"
"v_mfma_f32_16x16x32_bf16 v[76:79], a[176:179], a[100:103], v[76:79]\n"
"ds_read_b64_tr_b16 a[168:169], v4 offset:0\n"
"v_mul_f32 v110, v110, v46\n"
"v_mfma_f32_16x16x32_bf16 v[76:79], a[180:183], a[108:111], v[76:79]\n"
"s_addk_i32 s82, 19456\n"
"ds_read_b64_tr_b16 a[170:171], v4 offset:8192\n"
"v_mul_f32 v111, v111, v47\n"
"v_mfma_f32_16x16x32_bf16 v[80:83], a[184:187], v[224:227], v[80:83]\n"
"buffer_load_dwordx4 v9, s[64:67], s82 offen offset:1024 sc1 lds\n"
"v_mfma_f32_16x16x32_bf16 v[80:83], a[188:191], v[232:235], v[80:83]\n"
"ds_read_b64_tr_b16 a[172:173], v4 offset:1024\n"
"v_mul_f32 v124, v124, v60\n"
"v_mfma_f32_16x16x32_bf16 v[84:87], a[184:187], v[228:231], v[84:87]\n"
"s_add_u32 m0, s77, 81920\n"
"ds_read_b64_tr_b16 a[174:175], v4 offset:9216\n"
"v_mul_f32 v125, v125, v61\n"
"v_mfma_f32_16x16x32_bf16 v[84:87], a[188:191], v[236:239], v[84:87]\n"
"s_addk_i32 s82, -15360\n"
"v_exp_f32 v19, v127\n"
"v_mfma_f32_16x16x32_bf16 v[88:91], a[184:187], a[96:99], v[88:91]\n"
"buffer_load_dwordx4 v9, s[64:67], s82 offen offset:0 sc1 lds\n"
"v_mfma_f32_16x16x32_bf16 v[88:91], a[188:191], a[104:107], v[88:91]\n"
"v_rcp_f32 v18, v18\n"
"v_mfma_f32_16x16x32_bf16 v[92:95], a[184:187], a[100:103], v[92:95]\n"
"s_addk_i32 s82, 19456\n"
"v_add_f32 v19, 1.0, v19\n"
"v_mul_f32 v126, v126, v18\n"
"v_mfma_f32_16x16x32_bf16 v[92:95], a[188:191], a[108:111], v[92:95]\n"
"s_waitcnt lgkmcnt(0)\n"
"buffer_load_dwordx4 v9, s[64:67], s82 offen offset:1024 sc1 lds\n"
"v_mfma_f32_16x16x32_bf16 v[64:67], a[160:163], v[240:243], v[64:67]\n"
"s_add_u32 m0, s77, 32768\n"
"ds_read_b64_tr_b16 a[176:177], v3 offset:2048\n"
"v_mul_f32 v126, v126, v62\n"
"v_mfma_f32_16x16x32_bf16 v[64:67], a[164:167], v[248:251], v[64:67]\n"
"s_addk_i32 s82, -7168\n"
"ds_read_b64_tr_b16 a[178:179], v3 offset:10240\n"
"v_cvt_pk_bf16_f32 v44, v108, v109\n"
"v_mfma_f32_16x16x32_bf16 v[68:71], a[160:163], v[244:247], v[68:71]\n"
"buffer_load_dwordx4 v9, s[64:67], s82 offen offset:0 sc1 lds\n"
"v_mfma_f32_16x16x32_bf16 v[68:71], a[164:167], v[252:255], v[68:71]\n"
"ds_read_b64_tr_b16 a[180:181], v3 offset:3072\n"
"v_cvt_pk_bf16_f32 v45, v110, v111\n"
"v_mfma_f32_16x16x32_bf16 v[72:75], a[160:163], a[112:115], v[72:75]\n"
"ds_read_b64_tr_b16 a[182:183], v3 offset:11264\n"
"v_cvt_pk_bf16_f32 v46, v124, v125\n"
"v_mfma_f32_16x16x32_bf16 v[72:75], a[164:167], a[120:123], v[72:75]\n"
"buffer_store_dwordx4 v[40:43], v6, s[64:67], s18 offen offset:0 sc1\n"
"v_mfma_f32_16x16x32_bf16 v[76:79], a[160:163], a[116:119], v[76:79]\n"
"ds_read_b64_tr_b16 a[184:185], v4 offset:2048\n"
"v_lshrrev_b32 v15, 1, v12\n"
"v_mfma_f32_16x16x32_bf16 v[76:79], a[164:167], a[124:127], v[76:79]\n"
"s_addk_i32 s82, 19456\n"
"ds_read_b64_tr_b16 a[186:187], v4 offset:10240\n"
"v_add_u32 v15, 118784, v15\n"
"v_mfma_f32_16x16x32_bf16 v[80:83], a[168:171], v[240:243], v[80:83]\n"
"buffer_load_dwordx4 v9, s[64:67], s82 offen offset:1024 sc1 lds\n"
"v_mfma_f32_16x16x32_bf16 v[80:83], a[172:175], v[248:251], v[80:83]\n"
"ds_read_b64_tr_b16 a[188:189], v4 offset:3072\n"
"v_cmp_eq_u32 vcc, 0, v9\n"
"v_mfma_f32_16x16x32_bf16 v[84:87], a[168:171], v[244:247], v[84:87]\n"
"s_addk_i32 m0, 16384\n"
"ds_read_b64_tr_b16 a[190:191], v4 offset:11264\n"
"v_mfma_f32_16x16x32_bf16 v[84:87], a[172:175], v[252:255], v[84:87]\n"
"s_addk_i32 s82, -29696\n"
"v_rcp_f32 v19, v19\n"
"v_mfma_f32_16x16x32_bf16 v[88:91], a[168:171], a[112:115], v[88:91]\n"
"buffer_load_dwordx4 v9, s[64:67], s82 offen offset:2048 sc1 lds\n"
"v_mfma_f32_16x16x32_bf16 v[88:91], a[172:175], a[120:123], v[88:91]\n"
"v_mul_f32 v127, v127, v19\n"
"v_mul_f32 v127, v127, v63\n"
"v_mfma_f32_16x16x32_bf16 v[92:95], a[168:171], a[116:119], v[92:95]\n"
"s_addk_i32 s82, 19456\n"
"v_cvt_pk_bf16_f32 v47, v126, v127\n"
"v_mfma_f32_16x16x32_bf16 v[92:95], a[172:175], a[124:127], v[92:95]\n"
"s_waitcnt lgkmcnt(0)\n"
"buffer_load_dwordx4 v9, s[64:67], s82 offen offset:3072 sc1 lds\n"
"v_mfma_f32_16x16x32_bf16 v[64:67], a[176:179], a[144:147], v[64:67]\n"
"s_add_u32 m0, s77, 0\n"
"s_waitcnt vmcnt(7)\n"
"s_barrier\n"
"ds_read_b64_tr_b16 a[160:161], v1 offset:18432\n"
"v_mfma_f32_16x16x32_bf16 v[64:67], a[180:183], a[152:155], v[64:67]\n"
"s_add_u32 s82, s78, 2363648\n"
"ds_read_b64_tr_b16 a[162:163], v1 offset:26624\n"
"v_mfma_f32_16x16x32_bf16 v[68:71], a[176:179], a[148:151], v[68:71]\n"
"buffer_load_dwordx4 v11, s[48:51], s82 offen offset:0 lds\n"
"v_mfma_f32_16x16x32_bf16 v[68:71], a[180:183], a[156:159], v[68:71]\n"
"ds_read_b128 a[192:195], v7 offset:16384\n"
"ds_read_b64_tr_b16 a[164:165], v1 offset:19456\n"
"v_mfma_f32_16x16x32_bf16 v[72:75], a[176:179], a[128:131], v[72:75]\n"
"ds_read_b128 a[196:199], v7 offset:20480\n"
"ds_read_b64_tr_b16 a[166:167], v1 offset:27648\n"
"v_mfma_f32_16x16x32_bf16 v[72:75], a[180:183], a[136:139], v[72:75]\n"
"ds_read_b128 a[200:203], v7 offset:24576\n"
"ds_read_b128 a[204:207], v7 offset:28672\n"
"v_mfma_f32_16x16x32_bf16 v[76:79], a[176:179], a[132:135], v[76:79]\n"
"ds_read_b64_tr_b16 a[168:169], v2 offset:18432\n"
"v_mfma_f32_16x16x32_bf16 v[76:79], a[180:183], a[140:143], v[76:79]\n"
"s_add_u32 s82, s82, s13\n"
"ds_read_b64_tr_b16 a[170:171], v2 offset:26624\n"
"v_mfma_f32_16x16x32_bf16 v[80:83], a[184:187], a[144:147], v[80:83]\n"
"buffer_load_dwordx4 v11, s[48:51], s82 offen offset:1024 lds\n"
"v_mfma_f32_16x16x32_bf16 v[80:83], a[188:191], a[152:155], v[80:83]\n"
"ds_read_b64_tr_b16 a[172:173], v2 offset:19456\n"
"v_mfma_f32_16x16x32_bf16 v[84:87], a[184:187], a[148:151], v[84:87]\n"
"ds_read_b64_tr_b16 a[174:175], v2 offset:27648\n"
"v_mfma_f32_16x16x32_bf16 v[84:87], a[188:191], a[156:159], v[84:87]\n"
"s_add_u32 s82, s83, 13312\n"
"v_exp_f32 v16, v64\n"
"v_mfma_f32_16x16x32_bf16 v[88:91], a[184:187], a[128:131], v[88:91]\n"
"buffer_load_dwordx4 v9, s[64:67], s82 offen offset:2048 sc1 lds\n"
"v_mfma_f32_16x16x32_bf16 v[88:91], a[188:191], a[136:139], v[88:91]\n"
"v_add_f32 v16, 1.0, v16\n"
"v_mfma_f32_16x16x32_bf16 v[92:95], a[184:187], a[132:135], v[92:95]\n"
"s_addk_i32 s82, 19456\n"
"v_exp_f32 v17, v65\n"
"v_mfma_f32_16x16x32_bf16 v[92:95], a[188:191], a[140:143], v[92:95]\n"
"s_waitcnt lgkmcnt(0)\n"
"buffer_load_dwordx4 v9, s[64:67], s82 offen offset:3072 sc1 lds\n"
"v_mfma_f32_16x16x32_bf16 v[96:99], a[160:163], v[128:131], 0\n"
"s_add_u32 m0, s77, 65536\n"
"s_waitcnt vmcnt(9)\n"
"s_barrier\n"
"ds_read_b64_tr_b16 a[176:177], v3 offset:18432\n"
"v_mfma_f32_16x16x32_bf16 v[96:99], a[164:167], v[136:139], v[96:99]\n"
"s_add_u32 s82, s78, 2953472\n"
"ds_read_b64_tr_b16 a[178:179], v3 offset:26624\n"
"v_add_f32 v17, 1.0, v17\n"
"v_mfma_f32_16x16x32_bf16 v[100:103], a[160:163], v[132:135], 0\n"
"buffer_load_dwordx4 v11, s[48:51], s82 offen offset:0 lds\n"
"v_mfma_f32_16x16x32_bf16 v[100:103], a[164:167], v[140:143], v[100:103]\n"
"ds_read_b128 a[208:211], v8 offset:16384\n"
"ds_read_b64_tr_b16 a[180:181], v3 offset:19456\n"
"v_mfma_f32_16x16x32_bf16 v[104:107], a[160:163], a[0:3], 0\n"
"ds_read_b128 a[212:215], v8 offset:20480\n"
"ds_read_b64_tr_b16 a[182:183], v3 offset:27648\n"
"v_mfma_f32_16x16x32_bf16 v[104:107], a[164:167], a[8:11], v[104:107]\n"
"ds_read_b128 a[216:219], v8 offset:24576\n"
"ds_read_b128 a[220:223], v8 offset:28672\n"
"v_mfma_f32_16x16x32_bf16 v[108:111], a[160:163], a[4:7], 0\n"
"ds_read_b64_tr_b16 a[184:185], v4 offset:18432\n"
"v_mfma_f32_16x16x32_bf16 v[108:111], a[164:167], a[12:15], v[108:111]\n"
"s_add_u32 s82, s82, s13\n"
"ds_read_b64_tr_b16 a[186:187], v4 offset:26624\n"
"v_mfma_f32_16x16x32_bf16 v[112:115], a[168:171], v[128:131], 0\n"
"buffer_load_dwordx4 v11, s[48:51], s82 offen offset:1024 lds\n"
"v_mfma_f32_16x16x32_bf16 v[112:115], a[172:175], v[136:139], v[112:115]\n"
"ds_read_b64_tr_b16 a[188:189], v4 offset:19456\n"
"v_mfma_f32_16x16x32_bf16 v[116:119], a[168:171], v[132:135], 0\n"
"ds_read_b64_tr_b16 a[190:191], v4 offset:27648\n"
"v_mfma_f32_16x16x32_bf16 v[116:119], a[172:175], v[140:143], v[116:119]\n"
"s_add_u32 s82, s82, s13\n"
"v_rcp_f32 v16, v16\n"
"v_mfma_f32_16x16x32_bf16 v[120:123], a[168:171], a[0:3], 0\n"
"buffer_load_dwordx4 v11, s[48:51], s82 offen offset:2048 lds\n"
"v_mfma_f32_16x16x32_bf16 v[120:123], a[172:175], a[8:11], v[120:123]\n"
"v_exp_f32 v18, v66\n"
"v_mfma_f32_16x16x32_bf16 v[124:127], a[168:171], a[4:7], 0\n"
"s_add_u32 s82, s82, s13\n"
"v_mul_f32 v64, v64, v16\n"
"v_add_f32 v18, 1.0, v18\n"
"v_mfma_f32_16x16x32_bf16 v[124:127], a[172:175], a[12:15], v[124:127]\n"
"s_waitcnt lgkmcnt(0)\n"
"buffer_load_dwordx4 v11, s[48:51], s82 offen offset:3072 lds\n"
"v_mfma_f32_16x16x32_bf16 v[96:99], a[176:179], v[144:147], v[96:99]\n"
"s_add_u32 m0, s77, 16384\n"
"s_waitcnt vmcnt(10)\n"
"s_barrier\n"
"ds_read_b64_tr_b16 a[160:161], v1 offset:34816\n"
"v_mfma_f32_16x16x32_bf16 v[96:99], a[180:183], v[152:155], v[96:99]\n"
"s_add_u32 s82, s82, s15\n"
"ds_read_b128 a[224:227], v7 offset:32768\n"
"ds_read_b64_tr_b16 a[162:163], v1 offset:43008\n"
"v_mfma_f32_16x16x32_bf16 v[100:103], a[176:179], v[148:151], v[100:103]\n"
"buffer_load_dwordx4 v11, s[48:51], s82 offen offset:0 lds\n"
"v_mfma_f32_16x16x32_bf16 v[100:103], a[180:183], v[156:159], v[100:103]\n"
"ds_read_b128 a[228:231], v7 offset:36864\n"
"ds_read_b64_tr_b16 a[164:165], v1 offset:35840\n"
"v_mfma_f32_16x16x32_bf16 v[104:107], a[176:179], a[16:19], v[104:107]\n"
"ds_read_b128 a[232:235], v7 offset:40960\n"
"ds_read_b64_tr_b16 a[166:167], v1 offset:44032\n"
"v_mfma_f32_16x16x32_bf16 v[104:107], a[180:183], a[24:27], v[104:107]\n"
"ds_read_b128 a[236:239], v7 offset:45056\n"
"v_mfma_f32_16x16x32_bf16 v[108:111], a[176:179], a[20:23], v[108:111]\n"
"ds_read_b64_tr_b16 a[168:169], v2 offset:34816\n"
"v_mfma_f32_16x16x32_bf16 v[108:111], a[180:183], a[28:31], v[108:111]\n"
"s_add_u32 s82, s82, s13\n"
"ds_read_b64_tr_b16 a[170:171], v2 offset:43008\n"
"v_mfma_f32_16x16x32_bf16 v[112:115], a[184:187], v[144:147], v[112:115]\n"
"buffer_load_dwordx4 v11, s[48:51], s82 offen offset:1024 lds\n"
"v_mfma_f32_16x16x32_bf16 v[112:115], a[188:191], v[152:155], v[112:115]\n"
"ds_read_b64_tr_b16 a[172:173], v2 offset:35840\n"
"v_mfma_f32_16x16x32_bf16 v[116:119], a[184:187], v[148:151], v[116:119]\n"
"ds_read_b64_tr_b16 a[174:175], v2 offset:44032\n"
"v_mfma_f32_16x16x32_bf16 v[116:119], a[188:191], v[156:159], v[116:119]\n"
"s_add_u32 s82, s82, s13\n"
"v_rcp_f32 v17, v17\n"
"v_mfma_f32_16x16x32_bf16 v[120:123], a[184:187], a[16:19], v[120:123]\n"
"buffer_load_dwordx4 v11, s[48:51], s82 offen offset:2048 lds\n"
"v_mfma_f32_16x16x32_bf16 v[120:123], a[188:191], a[24:27], v[120:123]\n"
"v_exp_f32 v19, v67\n"
"v_mfma_f32_16x16x32_bf16 v[124:127], a[184:187], a[20:23], v[124:127]\n"
"s_add_u32 s82, s82, s13\n"
"v_mul_f32 v65, v65, v17\n"
"v_add_f32 v19, 1.0, v19\n"
"v_mfma_f32_16x16x32_bf16 v[124:127], a[188:191], a[28:31], v[124:127]\n"
"s_waitcnt lgkmcnt(0)\n"
"buffer_load_dwordx4 v11, s[48:51], s82 offen offset:3072 lds\n"
"v_mfma_f32_16x16x32_bf16 v[96:99], a[160:163], v[160:163], v[96:99]\n"
"s_waitcnt vmcnt(12)\n"
"s_barrier\n"
"ds_read_b64_tr_b16 a[176:177], v1 offset:49152\n"
"v_mfma_f32_16x16x32_bf16 v[96:99], a[164:167], v[168:171], v[96:99]\n"
"ds_read_b64_tr_b16 a[178:179], v1 offset:57344\n"
"v_mfma_f32_16x16x32_bf16 v[100:103], a[160:163], v[164:167], v[100:103]\n"
"v_rcp_f32 v18, v18\n"
"v_mfma_f32_16x16x32_bf16 v[100:103], a[164:167], v[172:175], v[100:103]\n"
"ds_read_b64_tr_b16 a[180:181], v1 offset:50176\n"
"v_mul_f32 v66, v66, v18\n"
"v_mfma_f32_16x16x32_bf16 v[104:107], a[160:163], a[32:35], v[104:107]\n"
"ds_read_b64_tr_b16 a[182:183], v1 offset:58368\n"
"v_mfma_f32_16x16x32_bf16 v[104:107], a[164:167], a[40:43], v[104:107]\n"
"v_exp_f32 v16, v80\n"
"v_mfma_f32_16x16x32_bf16 v[108:111], a[160:163], a[36:39], v[108:111]\n"
"ds_read_b64_tr_b16 a[184:185], v2 offset:49152\n"
"v_add_f32 v16, 1.0, v16\n"
"v_mfma_f32_16x16x32_bf16 v[108:111], a[164:167], a[44:47], v[108:111]\n"
"ds_read_b64_tr_b16 a[186:187], v2 offset:57344\n"
"v_mfma_f32_16x16x32_bf16 v[112:115], a[168:171], v[160:163], v[112:115]\n"
"v_rcp_f32 v19, v19\n"
"v_mfma_f32_16x16x32_bf16 v[112:115], a[172:175], v[168:171], v[112:115]\n"
"ds_read_b64_tr_b16 a[188:189], v2 offset:50176\n"
"v_mul_f32 v67, v67, v19\n"
"v_mfma_f32_16x16x32_bf16 v[116:119], a[168:171], v[164:167], v[116:119]\n"
"ds_read_b64_tr_b16 a[190:191], v2 offset:58368\n"
"v_mfma_f32_16x16x32_bf16 v[116:119], a[172:175], v[172:175], v[116:119]\n"
"v_exp_f32 v17, v81\n"
"v_mfma_f32_16x16x32_bf16 v[120:123], a[168:171], a[32:35], v[120:123]\n"
"v_rcp_f32 v16, v16\n"
"v_mfma_f32_16x16x32_bf16 v[120:123], a[172:175], a[40:43], v[120:123]\n"
"v_add_f32 v17, 1.0, v17\n"
"v_mul_f32 v80, v80, v16\n"
"v_mfma_f32_16x16x32_bf16 v[124:127], a[168:171], a[36:39], v[124:127]\n"
"v_exp_f32 v18, v82\n"
"v_mfma_f32_16x16x32_bf16 v[124:127], a[172:175], a[44:47], v[124:127]\n"
"s_waitcnt lgkmcnt(0)\n"
"v_rcp_f32 v17, v17\n"
"v_mfma_f32_16x16x32_bf16 v[96:99], a[176:179], v[176:179], v[96:99]\n"
"s_addk_i32 m0, 16384\n"
"s_waitcnt vmcnt(8)\n"
"s_barrier\n"
"ds_read_b64_tr_b16 a[160:161], v1 offset:0\n"
"v_mfma_f32_16x16x32_bf16 v[96:99], a[180:183], v[184:187], v[96:99]\n"
"s_add_u32 s82, s78, 4480\n"
"ds_read_b64_tr_b16 a[162:163], v1 offset:8192\n"
"v_add_f32 v18, 1.0, v18\n"
"v_mfma_f32_16x16x32_bf16 v[100:103], a[176:179], v[180:183], v[100:103]\n"
"buffer_load_dwordx4 v11, s[44:47], s82 offen offset:0 lds\n"
"v_mfma_f32_16x16x32_bf16 v[100:103], a[180:183], v[188:191], v[100:103]\n"
"ds_read_b64_tr_b16 a[164:165], v1 offset:1024\n"
"v_mul_f32 v81, v81, v17\n"
"v_mfma_f32_16x16x32_bf16 v[104:107], a[176:179], a[48:51], v[104:107]\n"
"ds_read_b64_tr_b16 a[166:167], v1 offset:9216\n"
"v_mfma_f32_16x16x32_bf16 v[104:107], a[180:183], a[56:59], v[104:107]\n"
"v_exp_f32 v19, v83\n"
"v_mfma_f32_16x16x32_bf16 v[108:111], a[176:179], a[52:55], v[108:111]\n"
"ds_read_b64_tr_b16 a[168:169], v2 offset:0\n"
"v_add_f32 v19, 1.0, v19\n"
"v_mfma_f32_16x16x32_bf16 v[108:111], a[180:183], a[60:63], v[108:111]\n"
"s_add_u32 s82, s82, s13\n"
"ds_read_b64_tr_b16 a[170:171], v2 offset:8192\n"
"v_mfma_f32_16x16x32_bf16 v[112:115], a[184:187], v[176:179], v[112:115]\n"
"buffer_load_dwordx4 v11, s[44:47], s82 offen offset:1024 lds\n"
"v_mfma_f32_16x16x32_bf16 v[112:115], a[188:191], v[184:187], v[112:115]\n"
"ds_read_b64_tr_b16 a[172:173], v2 offset:1024\n"
"v_mfma_f32_16x16x32_bf16 v[116:119], a[184:187], v[180:183], v[116:119]\n"
"ds_read_b64_tr_b16 a[174:175], v2 offset:9216\n"
"v_mfma_f32_16x16x32_bf16 v[116:119], a[188:191], v[188:191], v[116:119]\n"
"s_add_u32 s82, s82, s13\n"
"v_rcp_f32 v18, v18\n"
"v_mfma_f32_16x16x32_bf16 v[120:123], a[184:187], a[48:51], v[120:123]\n"
"buffer_load_dwordx4 v11, s[44:47], s82 offen offset:2048 lds\n"
"v_mfma_f32_16x16x32_bf16 v[120:123], a[188:191], a[56:59], v[120:123]\n"
"v_exp_f32 v16, v68\n"
"v_mfma_f32_16x16x32_bf16 v[124:127], a[184:187], a[52:55], v[124:127]\n"
"s_add_u32 s82, s82, s13\n"
"v_mul_f32 v82, v82, v18\n"
"v_add_f32 v16, 1.0, v16\n"
"v_mfma_f32_16x16x32_bf16 v[124:127], a[188:191], a[60:63], v[124:127]\n"
"s_waitcnt lgkmcnt(0)\n"
"buffer_load_dwordx4 v11, s[44:47], s82 offen offset:3072 lds\n"
"v_mfma_f32_16x16x32_bf16 v[96:99], a[160:163], v[192:195], v[96:99]\n"
"s_waitcnt vmcnt(8)\n"
"s_barrier\n"
"ds_read_b64_tr_b16 a[176:177], v3 offset:0\n"
"v_mfma_f32_16x16x32_bf16 v[96:99], a[164:167], v[200:203], v[96:99]\n"
"ds_read_b64_tr_b16 a[178:179], v3 offset:8192\n"
"v_mfma_f32_16x16x32_bf16 v[100:103], a[160:163], v[196:199], v[100:103]\n"
"v_rcp_f32 v19, v19\n"
"v_mfma_f32_16x16x32_bf16 v[100:103], a[164:167], v[204:207], v[100:103]\n"
"ds_read_b64_tr_b16 a[180:181], v3 offset:1024\n"
"v_mul_f32 v83, v83, v19\n"
"v_mfma_f32_16x16x32_bf16 v[104:107], a[160:163], a[64:67], v[104:107]\n"
"ds_read_b64_tr_b16 a[182:183], v3 offset:9216\n"
"v_mfma_f32_16x16x32_bf16 v[104:107], a[164:167], a[72:75], v[104:107]\n"
"v_exp_f32 v17, v69\n"
"v_mfma_f32_16x16x32_bf16 v[108:111], a[160:163], a[68:71], v[108:111]\n"
"ds_read_b64_tr_b16 a[184:185], v4 offset:0\n"
"v_add_f32 v17, 1.0, v17\n"
"v_mfma_f32_16x16x32_bf16 v[108:111], a[164:167], a[76:79], v[108:111]\n"
"ds_read_b64_tr_b16 a[186:187], v4 offset:8192\n"
"v_mfma_f32_16x16x32_bf16 v[112:115], a[168:171], v[192:195], v[112:115]\n"
"v_rcp_f32 v16, v16\n"
"v_mfma_f32_16x16x32_bf16 v[112:115], a[172:175], v[200:203], v[112:115]\n"
"ds_read_b64_tr_b16 a[188:189], v4 offset:1024\n"
"v_mul_f32 v68, v68, v16\n"
"v_mfma_f32_16x16x32_bf16 v[116:119], a[168:171], v[196:199], v[116:119]\n"
"ds_read_b64_tr_b16 a[190:191], v4 offset:9216\n"
"v_mfma_f32_16x16x32_bf16 v[116:119], a[172:175], v[204:207], v[116:119]\n"
"v_exp_f32 v18, v70\n"
"v_mfma_f32_16x16x32_bf16 v[120:123], a[168:171], a[64:67], v[120:123]\n"
"buffer_store_dwordx4 a[240:243], v9, s[64:67], s97 offen offset:0 sc1\n"
"v_mfma_f32_16x16x32_bf16 v[120:123], a[172:175], a[72:75], v[120:123]\n"
"v_rcp_f32 v17, v17\n"
"v_mfma_f32_16x16x32_bf16 v[124:127], a[168:171], a[68:71], v[124:127]\n"
"v_add_f32 v18, 1.0, v18\n"
"v_mul_f32 v69, v69, v17\n"
"v_mfma_f32_16x16x32_bf16 v[124:127], a[172:175], a[76:79], v[124:127]\n"
"v_exp_f32 v19, v71\n"
"v_mfma_f32_16x16x32_bf16 v[28:31], a[248:251], a[192:195], 0\n"
"ds_read_b128 a[192:195], v7 offset:51200\n"
"v_add_f32 v19, 1.0, v19\n"
"v_mfma_f32_16x16x32_bf16 v[28:31], a[248:251], a[196:199], v[28:31]\n"
"ds_read_b128 a[196:199], v7 offset:55296\n"
"v_mfma_f32_16x16x32_bf16 v[28:31], a[248:251], a[200:203], v[28:31]\n"
"ds_read_b128 a[200:203], v7 offset:59392\n"
"v_mfma_f32_16x16x32_bf16 v[28:31], a[248:251], a[204:207], v[28:31]\n"
"ds_read_b128 a[204:207], v7 offset:63488\n"
"v_mfma_f32_16x16x32_bf16 v[28:31], a[252:255], a[208:211], v[28:31]\n"
"ds_read_b128 a[208:211], v7 offset:2048\n"
"v_mfma_f32_16x16x32_bf16 v[28:31], a[252:255], a[212:215], v[28:31]\n"
"ds_read_b128 a[212:215], v7 offset:6144\n"
"v_mfma_f32_16x16x32_bf16 v[28:31], a[252:255], a[216:219], v[28:31]\n"
"ds_read_b128 a[216:219], v7 offset:10240\n"
"v_mfma_f32_16x16x32_bf16 v[28:31], a[252:255], a[220:223], v[28:31]\n"
"ds_read_b128 a[220:223], v7 offset:14336\n"
"v_mfma_f32_16x16x32_bf16 v[24:27], a[248:251], a[224:227], 0\n"
"buffer_store_dwordx4 a[244:247], v9, s[64:67], s97 offen offset:1024 sc1\n"
"v_mfma_f32_16x16x32_bf16 v[24:27], a[248:251], a[228:231], v[24:27]\n"
"s_xor_b32 s97, s96, 1\n"
"s_mul_i32 s42, s97, 368640\n"
"s_add_u32 s42, s42, s78\n"
"v_mfma_f32_16x16x32_bf16 v[24:27], a[248:251], a[232:235], v[24:27]\n"
"s_sub_u32 s98, s79, s78\n"
"v_cvt_pk_bf16_f32 v20, v28, v29\n"
"v_cvt_pk_bf16_f32 v21, v30, v31\n"
"v_mfma_f32_16x16x32_bf16 v[24:27], a[248:251], a[236:239], v[24:27]\n"
"s_waitcnt lgkmcnt(8)\n"
"s_mul_i32 s5, s97, 368640\n"
"s_add_u32 s5, s5, s79\n"
"v_mfma_f32_16x16x32_bf16 v[96:99], a[176:179], v[208:211], v[96:99]\n"
"s_mul_i32 s43, s97, 36864\n"
"s_add_u32 s43, s43, s78\n"
"ds_read_b64_tr_b16 a[160:161], v3 offset:2048\n"
"v_mfma_f32_16x16x32_bf16 v[96:99], a[180:183], v[216:219], v[96:99]\n"
"s_add_u32 s43, s43, 294912\n"
"s_and_b32 s17, s73, s74\n"
"ds_read_b64_tr_b16 a[162:163], v3 offset:10240\n"
"v_mfma_f32_16x16x32_bf16 v[100:103], a[176:179], v[212:215], v[100:103]\n"
"s_mul_i32 s80, s97, 41472\n"
"s_add_u32 s80, s80, s78\n"
"s_mul_i32 s81, s96, 41472\n"
"v_mfma_f32_16x16x32_bf16 v[100:103], a[180:183], v[220:223], v[100:103]\n"
"s_add_u32 s81, s81, s78\n"
"s_mul_i32 s17, s17, 4608\n"
"ds_read_b64_tr_b16 a[164:165], v3 offset:3072\n"
"v_mfma_f32_16x16x32_bf16 v[104:107], a[176:179], a[80:83], v[104:107]\n"
"s_add_u32 s98, s98, s17\n"
"ds_read_b64_tr_b16 a[166:167], v3 offset:11264\n"
"v_cvt_pk_bf16_f32 v0, v24, v25\n"
"v_mfma_f32_16x16x32_bf16 v[104:107], a[180:183], a[88:91], v[104:107]\n"
"s_cmp_eq_u32 s73, 0\n"
"s_cselect_b32 s6, s41, s80\n"
"s_cselect_b32 s7, s43, s81\n"
"s_add_u32 s6, s6, s98\n"
"v_mfma_f32_16x16x32_bf16 v[108:111], a[176:179], a[84:87], v[108:111]\n"
"s_add_u32 s7, s7, s98\n"
"ds_read_b64_tr_b16 a[168:169], v4 offset:2048\n"
"v_cvt_pk_bf16_f32 v10, v26, v27\n"
"v_mfma_f32_16x16x32_bf16 v[108:111], a[180:183], a[92:95], v[108:111]\n"
"s_add_u32 s17, s14, 64\n"
"ds_read_b64_tr_b16 a[170:171], v4 offset:10240\n"
"v_cndmask_b32 v22, 0, v0, s[92:93]\n"
"v_mfma_f32_16x16x32_bf16 v[112:115], a[184:187], v[208:211], v[112:115]\n"
"s_mul_i32 s96, s72, 3072\n"
"v_rcp_f32 v18, v18\n"
"v_mfma_f32_16x16x32_bf16 v[112:115], a[188:191], v[216:219], v[112:115]\n"
"s_add_u32 s98, s14, 32\n"
"ds_read_b64_tr_b16 a[172:173], v4 offset:3072\n"
"v_cndmask_b32 v23, 0, v10, s[92:93]\n"
"v_mfma_f32_16x16x32_bf16 v[116:119], a[184:187], v[212:215], v[116:119]\n"
"ds_read_b64_tr_b16 a[174:175], v4 offset:11264\n"
"v_cndmask_b32 v26, 0, v0, s[90:91]\n"
"v_mfma_f32_16x16x32_bf16 v[116:119], a[188:191], v[220:223], v[116:119]\n"
"s_add_u32 s96, s96, 147456\n"
"v_exp_f32 v16, v84\n"
"v_mfma_f32_16x16x32_bf16 v[120:123], a[184:187], a[80:83], v[120:123]\n"
"v_bfe_u32 v0, v9, 4, 2\n"
"v_lshl_add_u32 v7, v0, 7, s96\n"
"v_mfma_f32_16x16x32_bf16 v[120:123], a[188:191], a[88:91], v[120:123]\n"
"v_lshl_add_u32 v8, v0, 5, s96\n"
"v_mul_f32 v70, v70, v18\n"
"v_mfma_f32_16x16x32_bf16 v[124:127], a[184:187], a[84:87], v[124:127]\n"
"s_mul_i32 s96, s72, 9216\n"
"v_rcp_f32 v19, v19\n"
"v_mfma_f32_16x16x32_bf16 v[124:127], a[188:191], a[92:95], v[124:127]\n"
"s_waitcnt lgkmcnt(0)\n"
"v_bfe_u32 v0, v9, 6, 2\n"
"v_lshl_add_u32 v8, v0, 3, v8\n"
"v_mfma_f32_16x16x32_bf16 v[96:99], a[160:163], v[224:227], v[96:99]\n"
"s_waitcnt vmcnt(6)\n"
"s_barrier\n"
"ds_read_b64_tr_b16 a[176:177], v1 offset:16384\n"
"v_mfma_f32_16x16x32_bf16 v[96:99], a[164:167], v[232:235], v[96:99]\n"
"ds_read_b64_tr_b16 a[178:179], v1 offset:24576\n"
"v_add_f32 v16, 1.0, v16\n"
"v_mfma_f32_16x16x32_bf16 v[100:103], a[160:163], v[228:231], v[100:103]\n"
"v_lshrrev_b32 v0, 6, v9\n"
"v_lshl_add_u32 v7, v0, 3, v7\n"
"v_mfma_f32_16x16x32_bf16 v[100:103], a[164:167], v[236:239], v[100:103]\n"
"s_add_u32 m0, s77, 0\n"
"ds_read_b64_tr_b16 a[180:181], v1 offset:17408\n"
"v_cndmask_b32 v27, 0, v10, s[90:91]\n"
"v_mfma_f32_16x16x32_bf16 v[104:107], a[160:163], a[96:99], v[104:107]\n"
"s_add_u32 s82, s82, s15\n"
"ds_read_b64_tr_b16 a[182:183], v1 offset:25600\n"
"v_mul_f32 v71, v71, v19\n"
"v_mfma_f32_16x16x32_bf16 v[104:107], a[164:167], a[104:107], v[104:107]\n"
"buffer_load_dwordx4 v11, s[44:47], s82 offen offset:0 lds\n"
"v_mfma_f32_16x16x32_bf16 v[108:111], a[160:163], a[100:103], v[108:111]\n"
"ds_read_b64_tr_b16 a[184:185], v2 offset:16384\n"
"ds_read_b64_tr_b16 a[244:245], v7 offset:0\n"
"v_mfma_f32_16x16x32_bf16 v[108:111], a[164:167], a[108:111], v[108:111]\n"
"ds_read_b64_tr_b16 a[186:187], v2 offset:24576\n"
"ds_read_b64_tr_b16 a[246:247], v8 offset:2048\n"
"v_mfma_f32_16x16x32_bf16 v[112:115], a[168:171], v[224:227], v[112:115]\n"
"v_exp_f32 v17, v85\n"
"v_mfma_f32_16x16x32_bf16 v[112:115], a[172:175], v[232:235], v[112:115]\n"
"ds_read_b64_tr_b16 a[188:189], v2 offset:17408\n"
"v_add_f32 v17, 1.0, v17\n"
"v_mfma_f32_16x16x32_bf16 v[116:119], a[168:171], v[228:231], v[116:119]\n"
"s_add_u32 s82, s82, s13\n"
"ds_read_b64_tr_b16 a[190:191], v2 offset:25600\n"
"v_mfma_f32_16x16x32_bf16 v[116:119], a[172:175], v[236:239], v[116:119]\n"
"buffer_load_dwordx4 v11, s[44:47], s82 offen offset:1024 lds\n"
"v_mfma_f32_16x16x32_bf16 v[120:123], a[168:171], a[96:99], v[120:123]\n"
"ds_write_b128 v6, v[44:47] offset:33792\n"
"v_mfma_f32_16x16x32_bf16 v[120:123], a[172:175], a[104:107], v[120:123]\n"
"s_add_u32 s82, s82, s13\n"
"v_rcp_f32 v16, v16\n"
"v_mfma_f32_16x16x32_bf16 v[124:127], a[168:171], a[100:103], v[124:127]\n"
"buffer_load_dwordx4 v11, s[44:47], s82 offen offset:2048 lds\n"
"v_mfma_f32_16x16x32_bf16 v[124:127], a[172:175], a[108:111], v[124:127]\n"
"s_waitcnt lgkmcnt(1)\n"
"v_exp_f32 v18, v86\n"
"v_mfma_f32_16x16x32_bf16 v[96:99], a[176:179], v[240:243], v[96:99]\n"
"ds_read_b64_tr_b16 a[160:161], v1 offset:18432\n"
"v_mul_f32 v84, v84, v16\n"
"v_mfma_f32_16x16x32_bf16 v[96:99], a[180:183], v[248:251], v[96:99]\n"
"ds_read_b64_tr_b16 a[162:163], v1 offset:26624\n"
"v_add_f32 v18, 1.0, v18\n"
"v_mfma_f32_16x16x32_bf16 v[100:103], a[176:179], v[244:247], v[100:103]\n"
"v_rcp_f32 v17, v17\n"
"v_mfma_f32_16x16x32_bf16 v[100:103], a[180:183], v[252:255], v[100:103]\n"
"ds_read_b64_tr_b16 a[164:165], v1 offset:19456\n"
"v_mul_f32 v85, v85, v17\n"
"v_mfma_f32_16x16x32_bf16 v[104:107], a[176:179], a[112:115], v[104:107]\n"
"s_add_u32 s82, s82, s13\n"
"ds_read_b64_tr_b16 a[166:167], v1 offset:27648\n"
"v_mfma_f32_16x16x32_bf16 v[104:107], a[180:183], a[120:123], v[104:107]\n"
"buffer_load_dwordx4 v11, s[44:47], s82 offen offset:3072 lds\n"
"v_mfma_f32_16x16x32_bf16 v[108:111], a[176:179], a[116:119], v[108:111]\n"
"ds_read_b64_tr_b16 a[168:169], v2 offset:18432\n"
"v_mfma_f32_16x16x32_bf16 v[108:111], a[180:183], a[124:127], v[108:111]\n"
"ds_read_b64_tr_b16 a[170:171], v2 offset:26624\n"
"v_mfma_f32_16x16x32_bf16 v[112:115], a[184:187], v[240:243], v[112:115]\n"
"buffer_store_dwordx4 v[44:47], v6, s[64:67], s18 offen offset:1024 sc1\n"
"v_mfma_f32_16x16x32_bf16 v[112:115], a[188:191], v[248:251], v[112:115]\n"
"s_add_u32 m0, s77, 49152\n"
"ds_read_b64_tr_b16 a[172:173], v2 offset:19456\n"
"v_mfma_f32_16x16x32_bf16 v[116:119], a[184:187], v[244:247], v[116:119]\n"
"s_add_u32 s82, s82, s15\n"
"s_add_u32 s18, s9, 32768\n"
"ds_read_b64_tr_b16 a[174:175], v2 offset:27648\n"
"v_mfma_f32_16x16x32_bf16 v[116:119], a[188:191], v[252:255], v[116:119]\n"
"buffer_load_dwordx4 v11, s[44:47], s82 offen offset:0 lds\n"
"v_mfma_f32_16x16x32_bf16 v[120:123], a[184:187], a[112:115], v[120:123]\n"
"v_exp_f32 v19, v87\n"
"v_mfma_f32_16x16x32_bf16 v[120:123], a[188:191], a[120:123], v[120:123]\n"
"s_add_u32 s82, s82, s13\n"
"v_rcp_f32 v18, v18\n"
"v_mfma_f32_16x16x32_bf16 v[124:127], a[184:187], a[116:119], v[124:127]\n"
"buffer_load_dwordx4 v11, s[44:47], s82 offen offset:1024 lds\n"
"v_mfma_f32_16x16x32_bf16 v[124:127], a[188:191], a[124:127], v[124:127]\n"
"v_add_f32 v19, 1.0, v19\n"
"v_mul_f32 v86, v86, v18\n"
"v_mfma_f32_16x16x32_bf16 v[28:31], a[248:251], a[192:195], 0\n"
"v_exp_f32 v16, v72\n"
"v_mfma_f32_16x16x32_bf16 v[28:31], a[248:251], a[196:199], v[28:31]\n"
"v_rcp_f32 v19, v19\n"
"v_mfma_f32_16x16x32_bf16 v[28:31], a[248:251], a[200:203], v[28:31]\n"
"v_add_f32 v16, 1.0, v16\n"
"v_mul_f32 v87, v87, v19\n"
"v_mfma_f32_16x16x32_bf16 v[28:31], a[248:251], a[204:207], v[28:31]\n"
"v_exp_f32 v17, v73\n"
"v_mfma_f32_16x16x32_bf16 v[28:31], a[252:255], a[208:211], v[28:31]\n"
"v_rcp_f32 v16, v16\n"
"v_mfma_f32_16x16x32_bf16 v[28:31], a[252:255], a[212:215], v[28:31]\n"
"v_add_f32 v17, 1.0, v17\n"
"v_mul_f32 v72, v72, v16\n"
"v_mfma_f32_16x16x32_bf16 v[28:31], a[252:255], a[216:219], v[28:31]\n"
"v_exp_f32 v18, v74\n"
"v_mfma_f32_16x16x32_bf16 v[28:31], a[252:255], a[220:223], v[28:31]\n"
"s_waitcnt lgkmcnt(0)\n"
"v_rcp_f32 v17, v17\n"
"v_mfma_f32_16x16x32_bf16 v[96:99], a[160:163], a[144:147], v[96:99]\n"
"s_sub_u32 s52, s60, 36864\n"
"s_subb_u32 s53, s61, 0\n"
"s_add_u32 s54, s62, 36864\n"
"s_cmp_eq_u32 s73, 0\n"
"s_cselect_b32 s52, s56, s52\n"
"s_cselect_b32 s53, s57, s53\n"
"s_cselect_b32 s54, s58, s54\n"
"s_waitcnt vmcnt(9)\n"
"s_barrier\n"
"ds_read_b64_tr_b16 a[176:177], v1 offset:32768\n"
"v_mfma_f32_16x16x32_bf16 v[96:99], a[164:167], a[152:155], v[96:99]\n"
"ds_read_b64_tr_b16 a[178:179], v1 offset:40960\n"
"v_add_f32 v18, 1.0, v18\n"
"v_mfma_f32_16x16x32_bf16 v[100:103], a[160:163], a[148:151], v[100:103]\n"
"v_cvt_pk_bf16_f32 v24, v28, v29\n"
"v_cvt_pk_bf16_f32 v25, v30, v31\n"
"v_mfma_f32_16x16x32_bf16 v[100:103], a[164:167], a[156:159], v[100:103]\n"
"ds_read_b64_tr_b16 a[180:181], v1 offset:33792\n"
"v_mul_f32 v73, v73, v17\n"
"v_mfma_f32_16x16x32_bf16 v[104:107], a[160:163], a[128:131], v[104:107]\n"
"s_add_u32 s82, s82, s13\n"
"ds_read_b64_tr_b16 a[182:183], v1 offset:41984\n"
"v_mul_f32 v64, v64, v96\n"
"v_mfma_f32_16x16x32_bf16 v[104:107], a[164:167], a[136:139], v[104:107]\n"
"buffer_load_dwordx4 v11, s[44:47], s82 offen offset:2048 lds\n"
"v_mfma_f32_16x16x32_bf16 v[108:111], a[160:163], a[132:135], v[108:111]\n"
"ds_read_b64_tr_b16 a[184:185], v2 offset:32768\n"
"v_mul_f32 v65, v65, v97\n"
"v_mfma_f32_16x16x32_bf16 v[108:111], a[164:167], a[140:143], v[108:111]\n"
"ds_read_b64_tr_b16 a[186:187], v2 offset:40960\n"
"v_mul_f32 v66, v66, v98\n"
"v_mfma_f32_16x16x32_bf16 v[112:115], a[168:171], a[144:147], v[112:115]\n"
"v_exp_f32 v19, v75\n"
"v_mfma_f32_16x16x32_bf16 v[112:115], a[172:175], a[152:155], v[112:115]\n"
"ds_read_b64_tr_b16 a[188:189], v2 offset:33792\n"
"v_add_f32 v19, 1.0, v19\n"
"v_mfma_f32_16x16x32_bf16 v[116:119], a[168:171], a[148:151], v[116:119]\n"
"s_add_u32 s82, s82, s13\n"
"ds_read_b64_tr_b16 a[190:191], v2 offset:41984\n"
"v_mul_f32 v67, v67, v99\n"
"v_mfma_f32_16x16x32_bf16 v[116:119], a[172:175], a[156:159], v[116:119]\n"
"buffer_load_dwordx4 v11, s[44:47], s82 offen offset:3072 lds\n"
"v_mfma_f32_16x16x32_bf16 v[120:123], a[168:171], a[128:131], v[120:123]\n"
"s_addk_i32 m0, 16384\n"
"v_rcp_f32 v18, v18\n"
"v_mfma_f32_16x16x32_bf16 v[120:123], a[172:175], a[136:139], v[120:123]\n"
"s_add_u32 s82, s82, s15\n"
"v_exp_f32 v16, v88\n"
"v_mfma_f32_16x16x32_bf16 v[124:127], a[168:171], a[132:135], v[124:127]\n"
"buffer_load_dwordx4 v11, s[44:47], s82 offen offset:0 lds\n"
"v_mfma_f32_16x16x32_bf16 v[124:127], a[172:175], a[140:143], v[124:127]\n"
"s_waitcnt lgkmcnt(0)\n"
"v_mul_f32 v74, v74, v18\n"
"v_add_f32 v16, 1.0, v16\n"
"v_mfma_f32_16x16x32_bf16 v[32:35], a[176:179], v[128:131], 0\n"
"ds_read_b64_tr_b16 a[160:161], v1 offset:34816\n"
"v_mul_f32 v80, v80, v112\n"
"v_mfma_f32_16x16x32_bf16 v[32:35], a[180:183], v[136:139], v[32:35]\n"
"ds_read_b64_tr_b16 a[162:163], v1 offset:43008\n"
"v_mul_f32 v81, v81, v113\n"
"v_mfma_f32_16x16x32_bf16 v[36:39], a[176:179], v[132:135], 0\n"
"v_rcp_f32 v19, v19\n"
"v_mfma_f32_16x16x32_bf16 v[36:39], a[180:183], v[140:143], v[36:39]\n"
"ds_read_b64_tr_b16 a[164:165], v1 offset:35840\n"
"v_mul_f32 v75, v75, v19\n"
"v_mfma_f32_16x16x32_bf16 v[40:43], a[176:179], a[0:3], 0\n"
"s_add_u32 s82, s82, s13\n"
"ds_read_b64_tr_b16 a[166:167], v1 offset:44032\n"
"v_mul_f32 v82, v82, v114\n"
"v_mfma_f32_16x16x32_bf16 v[40:43], a[180:183], a[8:11], v[40:43]\n"
"buffer_load_dwordx4 v11, s[44:47], s82 offen offset:1024 lds\n"
"v_mfma_f32_16x16x32_bf16 v[44:47], a[176:179], a[4:7], 0\n"
"ds_read_b64_tr_b16 a[168:169], v2 offset:34816\n"
"v_mul_f32 v83, v83, v115\n"
"v_mfma_f32_16x16x32_bf16 v[44:47], a[180:183], a[12:15], v[44:47]\n"
"ds_read_b64_tr_b16 a[170:171], v2 offset:43008\n"
"v_cvt_pk_bf16_f32 v96, v64, v65\n"
"v_mfma_f32_16x16x32_bf16 v[48:51], a[184:187], v[128:131], 0\n"
"v_exp_f32 v17, v89\n"
"v_mfma_f32_16x16x32_bf16 v[48:51], a[188:191], v[136:139], v[48:51]\n"
"ds_read_b64_tr_b16 a[172:173], v2 offset:35840\n"
"v_add_f32 v17, 1.0, v17\n"
"v_mfma_f32_16x16x32_bf16 v[52:55], a[184:187], v[132:135], 0\n"
"s_add_u32 s82, s82, s13\n"
"ds_read_b64_tr_b16 a[174:175], v2 offset:44032\n"
"v_cvt_pk_bf16_f32 v97, v66, v67\n"
"v_mfma_f32_16x16x32_bf16 v[52:55], a[188:191], v[140:143], v[52:55]\n"
"buffer_load_dwordx4 v11, s[44:47], s82 offen offset:2048 lds\n"
"v_mfma_f32_16x16x32_bf16 v[56:59], a[184:187], a[0:3], 0\n"
"v_rcp_f32 v16, v16\n"
"v_mfma_f32_16x16x32_bf16 v[56:59], a[188:191], a[8:11], v[56:59]\n"
"s_add_u32 s82, s82, s13\n"
"v_exp_f32 v18, v90\n"
"v_mfma_f32_16x16x32_bf16 v[60:63], a[184:187], a[4:7], 0\n"
"buffer_load_dwordx4 v11, s[44:47], s82 offen offset:3072 lds\n"
"v_mfma_f32_16x16x32_bf16 v[60:63], a[188:191], a[12:15], v[60:63]\n"
"v_mul_f32 v88, v88, v16\n"
"v_add_f32 v18, 1.0, v18\n"
"v_mfma_f32_16x16x32_bf16 v[28:31], v[20:23], a[244:247], 0\n"
"v_rcp_f32 v17, v17\n"
"v_mfma_f32_16x16x32_bf16 a[240:243], v[24:27], a[244:247], 0\n"
"s_waitcnt lgkmcnt(0)\n"
"ds_read_b64_tr_b16 a[244:245], v7 offset:512\n"
"ds_read_b64_tr_b16 a[246:247], v8 offset:2176\n"
"v_mfma_f32_16x16x32_bf16 v[32:35], a[160:163], v[144:147], v[32:35]\n"
"s_waitcnt vmcnt(9)\n"
"s_barrier\n"
"ds_read_b64_tr_b16 a[176:177], v1 offset:0\n"
"v_mfma_f32_16x16x32_bf16 v[32:35], a[164:167], v[152:155], v[32:35]\n"
"ds_read_b64_tr_b16 a[178:179], v1 offset:8192\n"
"v_mul_f32 v89, v89, v17\n"
"v_mfma_f32_16x16x32_bf16 v[36:39], a[160:163], v[148:151], v[36:39]\n"
"v_accvgpr_read_b32 v0, a240\n"
"v_accvgpr_read_b32 v10, a241\n"
"v_accvgpr_read_b32 v13, a242\n"
"v_mfma_f32_16x16x32_bf16 v[36:39], a[164:167], v[156:159], v[36:39]\n"
"s_add_u32 m0, s77, 16384\n"
"ds_read_b64_tr_b16 a[180:181], v1 offset:1024\n"
"v_accvgpr_read_b32 v14, a243\n"
"v_cndmask_b32 v10, v10, v14, s[86:87]\n"
"v_mfma_f32_16x16x32_bf16 v[40:43], a[160:163], a[16:19], v[40:43]\n"
"s_add_u32 s82, s82, s15\n"
"ds_read_b64_tr_b16 a[182:183], v1 offset:9216\n"
"v_cndmask_b32 v14, v13, v0, s[86:87]\n"
"v_cndmask_b32 v0, v0, v13, s[86:87]\n"
"v_mfma_f32_16x16x32_bf16 v[40:43], a[164:167], a[24:27], v[40:43]\n"
"buffer_load_dwordx4 v11, s[44:47], s82 offen offset:0 lds\n"
"v_add_f32 v14, 0x358637bd, v14\n"
"v_mfma_f32_16x16x32_bf16 v[44:47], a[160:163], a[20:23], v[44:47]\n"
"ds_read_b64_tr_b16 a[184:185], v2 offset:0\n"
"v_rcp_f32 v14, v14\n"
"v_mfma_f32_16x16x32_bf16 v[44:47], a[164:167], a[28:31], v[44:47]\n"
"ds_read_b64_tr_b16 a[186:187], v2 offset:8192\n"
"v_mul_f32 v14, 0xbfb8aa3b, v14\n"
"v_mul_f32 v28, v28, v14\n"
"v_mfma_f32_16x16x32_bf16 v[48:51], a[168:171], v[144:147], v[48:51]\n"
"v_mul_f32 v29, v29, v14\n"
"v_mul_f32 v30, v30, v14\n"
"v_mul_f32 v31, v31, v14\n"
"v_mfma_f32_16x16x32_bf16 v[48:51], a[172:175], v[152:155], v[48:51]\n"
"ds_read_b64_tr_b16 a[188:189], v2 offset:1024\n"
"v_mul_f32 v0, v0, v14\n"
"v_mul_f32 v10, v10, v14\n"
"v_mfma_f32_16x16x32_bf16 v[52:55], a[168:171], v[148:151], v[52:55]\n"
"s_add_u32 s82, s82, s13\n"
"ds_read_b64_tr_b16 a[190:191], v2 offset:9216\n"
"v_cvt_pk_bf16_f32 v28, v28, v29\n"
"v_mfma_f32_16x16x32_bf16 v[52:55], a[172:175], v[156:159], v[52:55]\n"
"buffer_load_dwordx4 v11, s[44:47], s82 offen offset:1024 lds\n"
"v_cvt_pk_bf16_f32 v29, v30, v31\n"
"v_mfma_f32_16x16x32_bf16 v[56:59], a[168:171], a[16:19], v[56:59]\n"
"v_cvt_pk_bf16_f32 v0, v0, v10\n"
"v_cndmask_b32 v0, 0, v0, s[90:91]\n"
"v_mfma_f32_16x16x32_bf16 v[56:59], a[172:175], a[24:27], v[56:59]\n"
"s_add_u32 s82, s78, 2432\n"
"ds_write_b64 v12, v[28:29] offset:16384\n"
"v_mfma_f32_16x16x32_bf16 v[60:63], a[168:171], a[20:23], v[60:63]\n"
"buffer_load_dwordx4 v11, s[48:51], s82 offen offset:2048 lds\n"
"v_mfma_f32_16x16x32_bf16 v[60:63], a[172:175], a[28:31], v[60:63]\n"
"s_waitcnt lgkmcnt(1)\n"
"v_exp_f32 v19, v91\n"
"v_mfma_f32_16x16x32_bf16 v[32:35], a[176:179], v[160:163], v[32:35]\n"
"ds_read_b64_tr_b16 a[160:161], v1 offset:2048\n"
"v_add_f32 v19, 1.0, v19\n"
"v_mfma_f32_16x16x32_bf16 v[32:35], a[180:183], v[168:171], v[32:35]\n"
"ds_read_b64_tr_b16 a[162:163], v1 offset:10240\n"
"ds_write_b32 v15, v0 offset:0\n"
"v_mfma_f32_16x16x32_bf16 v[36:39], a[176:179], v[164:167], v[36:39]\n"
"v_rcp_f32 v18, v18\n"
"v_mfma_f32_16x16x32_bf16 v[36:39], a[180:183], v[172:175], v[36:39]\n"
"ds_read_b64_tr_b16 a[164:165], v1 offset:3072\n"
"v_mul_f32 v90, v90, v18\n"
"v_mfma_f32_16x16x32_bf16 v[40:43], a[176:179], a[32:35], v[40:43]\n"
"s_add_u32 s82, s82, s13\n"
"ds_read_b64_tr_b16 a[166:167], v1 offset:11264\n"
"v_cvt_pk_bf16_f32 v98, v80, v81\n"
"v_mfma_f32_16x16x32_bf16 v[40:43], a[180:183], a[40:43], v[40:43]\n"
"buffer_load_dwordx4 v11, s[48:51], s82 offen offset:3072 lds\n"
"v_mfma_f32_16x16x32_bf16 v[44:47], a[176:179], a[36:39], v[44:47]\n"
"ds_read_b64_tr_b16 a[168:169], v2 offset:2048\n"
"v_cvt_pk_bf16_f32 v99, v82, v83\n"
"v_mfma_f32_16x16x32_bf16 v[44:47], a[180:183], a[44:47], v[44:47]\n"
"ds_read_b64_tr_b16 a[170:171], v2 offset:10240\n"
"v_mul_f32 v68, v68, v100\n"
"v_mfma_f32_16x16x32_bf16 v[48:51], a[184:187], v[160:163], v[48:51]\n"
"v_exp_f32 v16, v76\n"
"v_mfma_f32_16x16x32_bf16 v[48:51], a[188:191], v[168:171], v[48:51]\n"
"s_addk_i32 m0, 16384\n"
"ds_read_b64_tr_b16 a[172:173], v2 offset:3072\n"
"v_add_f32 v16, 1.0, v16\n"
"v_mfma_f32_16x16x32_bf16 v[52:55], a[184:187], v[164:167], v[52:55]\n"
"s_add_u32 s82, s82, s15\n"
"ds_read_b64_tr_b16 a[174:175], v2 offset:11264\n"
"v_mul_f32 v69, v69, v101\n"
"v_mfma_f32_16x16x32_bf16 v[52:55], a[188:191], v[172:175], v[52:55]\n"
"buffer_load_dwordx4 v11, s[48:51], s82 offen offset:0 lds\n"
"v_mfma_f32_16x16x32_bf16 v[56:59], a[184:187], a[32:35], v[56:59]\n"
"v_rcp_f32 v19, v19\n"
"v_mfma_f32_16x16x32_bf16 v[56:59], a[188:191], a[40:43], v[56:59]\n"
"s_add_u32 s82, s82, s13\n"
"v_exp_f32 v17, v77\n"
"v_mfma_f32_16x16x32_bf16 v[60:63], a[184:187], a[36:39], v[60:63]\n"
"buffer_load_dwordx4 v11, s[48:51], s82 offen offset:1024 lds\n"
"v_mfma_f32_16x16x32_bf16 v[60:63], a[188:191], a[44:47], v[60:63]\n"
"v_mul_f32 v91, v91, v19\n"
"v_add_f32 v17, 1.0, v17\n"
"v_mfma_f32_16x16x32_bf16 v[28:31], v[20:23], a[244:247], 0\n"
"v_rcp_f32 v16, v16\n"
"v_mfma_f32_16x16x32_bf16 a[240:243], v[24:27], a[244:247], 0\n"
"s_waitcnt lgkmcnt(0)\n"
"ds_read_b64_tr_b16 a[244:245], v7 offset:1024\n"
"ds_read_b64_tr_b16 a[246:247], v8 offset:2304\n"
"v_mfma_f32_16x16x32_bf16 v[32:35], a[160:163], v[176:179], v[32:35]\n"
"s_waitcnt vmcnt(10)\n"
"s_barrier\n"
"ds_read_b64_tr_b16 a[176:177], v1 offset:49152\n"
"v_mfma_f32_16x16x32_bf16 v[32:35], a[164:167], v[184:187], v[32:35]\n"
"ds_read_b64_tr_b16 a[178:179], v1 offset:57344\n"
"v_mul_f32 v76, v76, v16\n"
"v_mfma_f32_16x16x32_bf16 v[36:39], a[160:163], v[180:183], v[36:39]\n"
"v_accvgpr_read_b32 v0, a240\n"
"v_accvgpr_read_b32 v10, a241\n"
"v_accvgpr_read_b32 v13, a242\n"
"v_mfma_f32_16x16x32_bf16 v[36:39], a[164:167], v[188:191], v[36:39]\n"
"ds_read_b64_tr_b16 a[180:181], v1 offset:50176\n"
"v_accvgpr_read_b32 v14, a243\n"
"v_cndmask_b32 v10, v10, v14, s[86:87]\n"
"v_mfma_f32_16x16x32_bf16 v[40:43], a[160:163], a[48:51], v[40:43]\n"
"s_add_u32 s82, s82, s13\n"
"ds_read_b64_tr_b16 a[182:183], v1 offset:58368\n"
"v_cndmask_b32 v14, v13, v0, s[86:87]\n"
"v_cndmask_b32 v0, v0, v13, s[86:87]\n"
"v_mfma_f32_16x16x32_bf16 v[40:43], a[164:167], a[56:59], v[40:43]\n"
"buffer_load_dwordx4 v11, s[48:51], s82 offen offset:2048 lds\n"
"v_add_f32 v14, 0x358637bd, v14\n"
"v_mfma_f32_16x16x32_bf16 v[44:47], a[160:163], a[52:55], v[44:47]\n"
"ds_read_b64_tr_b16 a[184:185], v2 offset:49152\n"
"v_rcp_f32 v14, v14\n"
"v_mfma_f32_16x16x32_bf16 v[44:47], a[164:167], a[60:63], v[44:47]\n"
"ds_read_b64_tr_b16 a[186:187], v2 offset:57344\n"
"v_mul_f32 v14, 0xbfb8aa3b, v14\n"
"v_mul_f32 v28, v28, v14\n"
"v_mfma_f32_16x16x32_bf16 v[48:51], a[168:171], v[176:179], v[48:51]\n"
"v_mul_f32 v29, v29, v14\n"
"v_mul_f32 v30, v30, v14\n"
"v_mul_f32 v31, v31, v14\n"
"v_mfma_f32_16x16x32_bf16 v[48:51], a[172:175], v[184:187], v[48:51]\n"
"ds_read_b64_tr_b16 a[188:189], v2 offset:50176\n"
"v_mul_f32 v0, v0, v14\n"
"v_mul_f32 v10, v10, v14\n"
"v_mfma_f32_16x16x32_bf16 v[52:55], a[168:171], v[180:183], v[52:55]\n"
"s_add_u32 s82, s82, s13\n"
"ds_read_b64_tr_b16 a[190:191], v2 offset:58368\n"
"v_cvt_pk_bf16_f32 v28, v28, v29\n"
"v_mfma_f32_16x16x32_bf16 v[52:55], a[172:175], v[188:191], v[52:55]\n"
"buffer_load_dwordx4 v11, s[48:51], s82 offen offset:3072 lds\n"
"v_cvt_pk_bf16_f32 v29, v30, v31\n"
"v_mfma_f32_16x16x32_bf16 v[56:59], a[168:171], a[48:51], v[56:59]\n"
"s_add_u32 m0, s77, 0\n"
"v_cvt_pk_bf16_f32 v0, v0, v10\n"
"v_cndmask_b32 v0, 0, v0, s[90:91]\n"
"v_mfma_f32_16x16x32_bf16 v[56:59], a[172:175], a[56:59], v[56:59]\n"
"s_add_u32 s82, s82, s15\n"
"ds_write_b64 v12, v[28:29] offset:17408\n"
"v_mfma_f32_16x16x32_bf16 v[60:63], a[168:171], a[52:55], v[60:63]\n"
"buffer_load_dwordx4 v11, s[48:51], s82 offen offset:0 lds\n"
"v_mfma_f32_16x16x32_bf16 v[60:63], a[172:175], a[60:63], v[60:63]\n"
"s_waitcnt lgkmcnt(1)\n"
"ds_write_b32 v15, v0 offset:512\n"
"v_mfma_f32_16x16x32_bf16 v[32:35], a[176:179], v[192:195], v[32:35]\n"
"ds_read_b64_tr_b16 a[160:161], v1 offset:51200\n"
"v_subrev_u32 v12, s99, v6\n"
"v_mfma_f32_16x16x32_bf16 v[32:35], a[180:183], v[200:203], v[32:35]\n"
"ds_read_b64_tr_b16 a[162:163], v1 offset:59392\n"
"v_lshrrev_b32 v15, 1, v12\n"
"v_mfma_f32_16x16x32_bf16 v[36:39], a[176:179], v[196:199], v[36:39]\n"
"v_mul_f32 v70, v70, v102\n"
"v_add_u32 v15, 118784, v15\n"
"v_mfma_f32_16x16x32_bf16 v[36:39], a[180:183], v[204:207], v[36:39]\n"
"ds_read_b64_tr_b16 a[164:165], v1 offset:52224\n"
"v_mul_f32 v71, v71, v103\n"
"v_mfma_f32_16x16x32_bf16 v[40:43], a[176:179], a[64:67], v[40:43]\n"
"s_add_u32 s82, s82, s13\n"
"ds_read_b64_tr_b16 a[166:167], v1 offset:60416\n"
"v_mul_f32 v84, v84, v116\n"
"v_mfma_f32_16x16x32_bf16 v[40:43], a[180:183], a[72:75], v[40:43]\n"
"buffer_load_dwordx4 v11, s[48:51], s82 offen offset:1024 lds\n"
"v_mfma_f32_16x16x32_bf16 v[44:47], a[176:179], a[68:71], v[44:47]\n"
"ds_read_b64_tr_b16 a[168:169], v2 offset:51200\n"
"v_mul_f32 v85, v85, v117\n"
"v_mfma_f32_16x16x32_bf16 v[44:47], a[180:183], a[76:79], v[44:47]\n"
"ds_read_b64_tr_b16 a[170:171], v2 offset:59392\n"
"v_mul_f32 v86, v86, v118\n"
"v_mfma_f32_16x16x32_bf16 v[48:51], a[184:187], v[192:195], v[48:51]\n"
"v_exp_f32 v18, v78\n"
"v_mfma_f32_16x16x32_bf16 v[48:51], a[188:191], v[200:203], v[48:51]\n"
"ds_read_b64_tr_b16 a[172:173], v2 offset:52224\n"
"v_add_f32 v18, 1.0, v18\n"
"v_mfma_f32_16x16x32_bf16 v[52:55], a[184:187], v[196:199], v[52:55]\n"
"s_add_u32 s82, s82, s13\n"
"ds_read_b64_tr_b16 a[174:175], v2 offset:60416\n"
"v_mul_f32 v87, v87, v119\n"
"v_mfma_f32_16x16x32_bf16 v[52:55], a[188:191], v[204:207], v[52:55]\n"
"buffer_load_dwordx4 v11, s[48:51], s82 offen offset:2048 lds\n"
"v_mfma_f32_16x16x32_bf16 v[56:59], a[184:187], a[64:67], v[56:59]\n"
"ds_write_b128 v5, v[96:99] offset:0\n"
"v_mfma_f32_16x16x32_bf16 v[56:59], a[188:191], a[72:75], v[56:59]\n"
"s_add_u32 s82, s82, s13\n"
"v_rcp_f32 v17, v17\n"
"v_mfma_f32_16x16x32_bf16 v[60:63], a[184:187], a[68:71], v[60:63]\n"
"buffer_load_dwordx4 v11, s[48:51], s82 offen offset:3072 lds\n"
"v_mfma_f32_16x16x32_bf16 v[60:63], a[188:191], a[76:79], v[60:63]\n"
"v_exp_f32 v19, v79\n"
"v_mfma_f32_16x16x32_bf16 v[28:31], v[20:23], a[244:247], 0\n"
"v_mul_f32 v77, v77, v17\n"
"v_add_f32 v19, 1.0, v19\n"
"v_mfma_f32_16x16x32_bf16 a[240:243], v[24:27], a[244:247], 0\n"
"s_waitcnt lgkmcnt(1)\n"
"v_rcp_f32 v18, v18\n"
"v_mfma_f32_16x16x32_bf16 v[32:35], a[160:163], v[208:211], v[32:35]\n"
"s_waitcnt vmcnt(12)\n"
"s_barrier\n"
"ds_read_b64_tr_b16 a[176:177], v3 offset:0\n"
"v_mfma_f32_16x16x32_bf16 v[32:35], a[164:167], v[216:219], v[32:35]\n"
"ds_read_b64_tr_b16 a[244:245], v7 offset:1536\n"
"ds_read_b64_tr_b16 a[178:179], v3 offset:8192\n"
"v_mfma_f32_16x16x32_bf16 v[36:39], a[160:163], v[212:215], v[36:39]\n"
"v_accvgpr_read_b32 v0, a240\n"
"v_accvgpr_read_b32 v10, a241\n"
"v_accvgpr_read_b32 v13, a242\n"
"v_mfma_f32_16x16x32_bf16 v[36:39], a[164:167], v[220:223], v[36:39]\n"
"s_add_u32 m0, s77, 49152\n"
"ds_read_b64_tr_b16 a[180:181], v3 offset:1024\n"
"v_accvgpr_read_b32 v14, a243\n"
"v_cndmask_b32 v10, v10, v14, s[86:87]\n"
"v_mfma_f32_16x16x32_bf16 v[40:43], a[160:163], a[80:83], v[40:43]\n"
"s_add_u32 s82, s82, s15\n"
"ds_read_b64_tr_b16 a[182:183], v3 offset:9216\n"
"v_cndmask_b32 v14, v13, v0, s[86:87]\n"
"v_cndmask_b32 v0, v0, v13, s[86:87]\n"
"v_mfma_f32_16x16x32_bf16 v[40:43], a[164:167], a[88:91], v[40:43]\n"
"buffer_load_dwordx4 v11, s[48:51], s82 offen offset:0 lds\n"
"v_add_f32 v14, 0x358637bd, v14\n"
"v_mfma_f32_16x16x32_bf16 v[44:47], a[160:163], a[84:87], v[44:47]\n"
"ds_read_b64_tr_b16 a[184:185], v4 offset:0\n"
"v_rcp_f32 v14, v14\n"
"v_mfma_f32_16x16x32_bf16 v[44:47], a[164:167], a[92:95], v[44:47]\n"
"ds_read_b64_tr_b16 a[186:187], v4 offset:8192\n"
"v_mul_f32 v14, 0xbfb8aa3b, v14\n"
"v_mul_f32 v28, v28, v14\n"
"v_mfma_f32_16x16x32_bf16 v[48:51], a[168:171], v[208:211], v[48:51]\n"
"v_mul_f32 v29, v29, v14\n"
"v_mul_f32 v30, v30, v14\n"
"v_mul_f32 v31, v31, v14\n"
"v_mfma_f32_16x16x32_bf16 v[48:51], a[172:175], v[216:219], v[48:51]\n"
"ds_read_b64_tr_b16 a[188:189], v4 offset:1024\n"
"v_mul_f32 v0, v0, v14\n"
"v_mul_f32 v10, v10, v14\n"
"v_mfma_f32_16x16x32_bf16 v[52:55], a[168:171], v[212:215], v[52:55]\n"
"s_add_u32 s82, s82, s13\n"
"ds_read_b64_tr_b16 a[190:191], v4 offset:9216\n"
"v_cvt_pk_bf16_f32 v28, v28, v29\n"
"v_mfma_f32_16x16x32_bf16 v[52:55], a[172:175], v[220:223], v[52:55]\n"
"buffer_load_dwordx4 v11, s[48:51], s82 offen offset:1024 lds\n"
"v_cvt_pk_bf16_f32 v29, v30, v31\n"
"v_mfma_f32_16x16x32_bf16 v[56:59], a[168:171], a[80:83], v[56:59]\n"
"v_cvt_pk_bf16_f32 v0, v0, v10\n"
"v_cndmask_b32 v0, 0, v0, s[90:91]\n"
"v_mfma_f32_16x16x32_bf16 v[56:59], a[172:175], a[88:91], v[56:59]\n"
"s_add_u32 s82, s82, s13\n"
"ds_write_b64 v12, v[28:29] offset:16384\n"
"v_mfma_f32_16x16x32_bf16 v[60:63], a[168:171], a[84:87], v[60:63]\n"
"buffer_load_dwordx4 v11, s[48:51], s82 offen offset:2048 lds\n"
"v_mfma_f32_16x16x32_bf16 v[60:63], a[172:175], a[92:95], v[60:63]\n"
"s_waitcnt lgkmcnt(1)\n"
"ds_read_b64_tr_b16 a[246:247], v8 offset:2432\n"
"v_mul_f32 v78, v78, v18\n"
"v_mfma_f32_16x16x32_bf16 v[32:35], a[176:179], v[224:227], v[32:35]\n"
"ds_read_b64_tr_b16 a[160:161], v3 offset:2048\n"
"ds_write_b32 v15, v0 offset:0\n"
"v_mfma_f32_16x16x32_bf16 v[32:35], a[180:183], v[232:235], v[32:35]\n"
"ds_read_b64_tr_b16 a[162:163], v3 offset:10240\n"
"v_cvt_pk_bf16_f32 v100, v68, v69\n"
"v_mfma_f32_16x16x32_bf16 v[36:39], a[176:179], v[228:231], v[36:39]\n"
"buffer_store_dwordx4 v[96:99], v5, s[64:67], s18 offen offset:0 sc1\n"
"v_mfma_f32_16x16x32_bf16 v[36:39], a[180:183], v[236:239], v[36:39]\n"
"ds_read_b64_tr_b16 a[164:165], v3 offset:3072\n"
"v_cvt_pk_bf16_f32 v101, v70, v71\n"
"v_mfma_f32_16x16x32_bf16 v[40:43], a[176:179], a[96:99], v[40:43]\n"
"ds_read_b64_tr_b16 a[166:167], v3 offset:11264\n"
"v_cvt_pk_bf16_f32 v102, v84, v85\n"
"v_mfma_f32_16x16x32_bf16 v[40:43], a[180:183], a[104:107], v[40:43]\n"
"v_exp_f32 v16, v92\n"
"v_mfma_f32_16x16x32_bf16 v[44:47], a[176:179], a[100:103], v[44:47]\n"
"ds_read_b64_tr_b16 a[168:169], v4 offset:2048\n"
"v_add_f32 v16, 1.0, v16\n"
"v_mfma_f32_16x16x32_bf16 v[44:47], a[180:183], a[108:111], v[44:47]\n"
"ds_read_b64_tr_b16 a[170:171], v4 offset:10240\n"
"v_cvt_pk_bf16_f32 v103, v86, v87\n"
"v_mfma_f32_16x16x32_bf16 v[48:51], a[184:187], v[224:227], v[48:51]\n"
"v_rcp_f32 v19, v19\n"
"v_mfma_f32_16x16x32_bf16 v[48:51], a[188:191], v[232:235], v[48:51]\n"
"ds_read_b64_tr_b16 a[172:173], v4 offset:3072\n"
"v_mul_f32 v79, v79, v19\n"
"v_mfma_f32_16x16x32_bf16 v[52:55], a[184:187], v[228:231], v[52:55]\n"
"ds_read_b64_tr_b16 a[174:175], v4 offset:11264\n"
"v_mul_f32 v72, v72, v104\n"
"v_mfma_f32_16x16x32_bf16 v[52:55], a[188:191], v[236:239], v[52:55]\n"
"s_add_u32 s82, s82, s13\n"
"v_exp_f32 v17, v93\n"
"v_mfma_f32_16x16x32_bf16 v[56:59], a[184:187], a[96:99], v[56:59]\n"
"buffer_load_dwordx4 v11, s[48:51], s82 offen offset:3072 lds\n"
"v_mfma_f32_16x16x32_bf16 v[56:59], a[188:191], a[104:107], v[56:59]\n"
"ds_write_b128 v5, v[100:103] offset:1024\n"
"v_mfma_f32_16x16x32_bf16 v[60:63], a[184:187], a[100:103], v[60:63]\n"
"v_rcp_f32 v16, v16\n"
"v_mfma_f32_16x16x32_bf16 v[60:63], a[188:191], a[108:111], v[60:63]\n"
"v_add_f32 v17, 1.0, v17\n"
"v_mul_f32 v73, v73, v105\n"
"s_waitcnt lgkmcnt(10)\n"
"v_mfma_f32_16x16x32_bf16 v[28:31], v[20:23], a[244:247], 0\n"
"v_mul_f32 v74, v74, v106\n"
"v_mov_b32 v20, s84\n"
"v_mfma_f32_16x16x32_bf16 a[240:243], v[24:27], a[244:247], 0\n"
"s_waitcnt lgkmcnt(1)\n"
"v_exp_f32 v18, v94\n"
"v_mfma_f32_16x16x32_bf16 v[32:35], a[160:163], v[240:243], v[32:35]\n"
"s_waitcnt vmcnt(13)\n"
"s_barrier\n"
"ds_read_b64_tr_b16 a[176:177], v1 offset:16384\n"
"v_mfma_f32_16x16x32_bf16 v[32:35], a[164:167], v[248:251], v[32:35]\n"
"ds_read_b64_tr_b16 a[178:179], v1 offset:24576\n"
"v_mov_b32 v21, s85\n"
"v_mfma_f32_16x16x32_bf16 v[36:39], a[160:163], v[244:247], v[36:39]\n"
"v_accvgpr_read_b32 v0, a240\n"
"v_accvgpr_read_b32 v10, a241\n"
"v_accvgpr_read_b32 v13, a242\n"
"v_mfma_f32_16x16x32_bf16 v[36:39], a[164:167], v[252:255], v[36:39]\n"
"s_addk_i32 m0, 16384\n"
"ds_read_b64_tr_b16 a[180:181], v1 offset:17408\n"
"v_accvgpr_read_b32 v14, a243\n"
"v_cndmask_b32 v10, v10, v14, s[86:87]\n"
"v_mfma_f32_16x16x32_bf16 v[40:43], a[160:163], a[112:115], v[40:43]\n"
"s_add_u32 s82, s82, s15\n"
"ds_read_b64_tr_b16 a[182:183], v1 offset:25600\n"
"v_cndmask_b32 v14, v13, v0, s[86:87]\n"
"v_cndmask_b32 v0, v0, v13, s[86:87]\n"
"v_mfma_f32_16x16x32_bf16 v[40:43], a[164:167], a[120:123], v[40:43]\n"
"buffer_load_dwordx4 v11, s[48:51], s82 offen offset:0 lds\n"
"v_add_f32 v14, 0x358637bd, v14\n"
"v_mfma_f32_16x16x32_bf16 v[44:47], a[160:163], a[116:119], v[44:47]\n"
"ds_read_b64_tr_b16 a[184:185], v2 offset:16384\n"
"v_rcp_f32 v14, v14\n"
"v_mfma_f32_16x16x32_bf16 v[44:47], a[164:167], a[124:127], v[44:47]\n"
"ds_read_b64_tr_b16 a[186:187], v2 offset:24576\n"
"v_mul_f32 v14, 0xbfb8aa3b, v14\n"
"v_mul_f32 v28, v28, v14\n"
"v_mfma_f32_16x16x32_bf16 v[48:51], a[168:171], v[240:243], v[48:51]\n"
"v_mul_f32 v29, v29, v14\n"
"v_mul_f32 v30, v30, v14\n"
"v_mul_f32 v31, v31, v14\n"
"v_mfma_f32_16x16x32_bf16 v[48:51], a[172:175], v[248:251], v[48:51]\n"
"ds_read_b64_tr_b16 a[188:189], v2 offset:17408\n"
"v_mul_f32 v0, v0, v14\n"
"v_mul_f32 v10, v10, v14\n"
"v_mfma_f32_16x16x32_bf16 v[52:55], a[168:171], v[244:247], v[52:55]\n"
"s_add_u32 s82, s82, s13\n"
"ds_read_b64_tr_b16 a[190:191], v2 offset:25600\n"
"buffer_store_dwordx4 v[100:103], v5, s[64:67], s18 offen offset:1024 sc1\n"
"v_mfma_f32_16x16x32_bf16 v[52:55], a[172:175], v[252:255], v[52:55]\n"
"buffer_load_dwordx4 v11, s[48:51], s82 offen offset:1024 lds\n"
"v_cvt_pk_bf16_f32 v28, v28, v29\n"
"v_mfma_f32_16x16x32_bf16 v[56:59], a[168:171], a[112:115], v[56:59]\n"
"v_mul_f32 v75, v75, v107\n"
"v_cvt_pk_bf16_f32 v29, v30, v31\n"
"v_cvt_pk_bf16_f32 v0, v0, v10\n"
"v_mfma_f32_16x16x32_bf16 v[56:59], a[172:175], a[120:123], v[56:59]\n"
"s_add_u32 s82, s82, s13\n"
"ds_write_b64 v12, v[28:29] offset:17408\n"
"v_cndmask_b32 v0, 0, v0, s[90:91]\n"
"v_mfma_f32_16x16x32_bf16 v[60:63], a[168:171], a[116:119], v[60:63]\n"
"buffer_load_dwordx4 v11, s[48:51], s82 offen offset:2048 lds\n"
"v_mov_b32 v22, s10\n"
"v_mfma_f32_16x16x32_bf16 v[60:63], a[172:175], a[124:127], v[60:63]\n"
"s_waitcnt lgkmcnt(1)\n"
"v_mul_f32 v92, v92, v16\n"
"v_mul_f32 v88, v88, v120\n"
"v_mov_b32 v23, s11\n"
"v_mov_b32 v24, 0x7ffffff0\n"
"v_cndmask_b32 v24, v24, v9, vcc\n"
"v_mfma_f32_16x16x32_bf16 v[32:35], a[176:179], a[144:147], v[32:35]\n"
"ds_read_b64_tr_b16 a[160:161], v1 offset:18432\n"
"buffer_store_dwordx2 v[20:21], v24, s[64:67], s17 offen sc1\n"
"v_rcp_f32 v17, v17\n"
"v_mfma_f32_16x16x32_bf16 v[32:35], a[180:183], a[152:155], v[32:35]\n"
"ds_read_b64_tr_b16 a[162:163], v1 offset:26624\n"
"v_mul_f32 v89, v89, v121\n"
"v_mul_f32 v90, v90, v122\n"
"v_mfma_f32_16x16x32_bf16 v[36:39], a[176:179], a[148:151], v[36:39]\n"
"ds_write_b32 v15, v0 offset:512\n"
"v_add_f32 v18, 1.0, v18\n"
"v_mfma_f32_16x16x32_bf16 v[36:39], a[180:183], a[156:159], v[36:39]\n"
"ds_read_b64_tr_b16 a[164:165], v1 offset:19456\n"
"v_mul_f32 v91, v91, v123\n"
"v_lshrrev_b32 v0, 7, v9\n"
"v_mfma_f32_16x16x32_bf16 v[40:43], a[176:179], a[128:131], v[40:43]\n"
"ds_read_b64_tr_b16 a[166:167], v1 offset:27648\n"
"v_exp_f32 v19, v95\n"
"v_mfma_f32_16x16x32_bf16 v[40:43], a[180:183], a[136:139], v[40:43]\n"
"v_cvt_pk_bf16_f32 v104, v72, v73\n"
"v_exp_f32 v16, v32\n"
"v_mfma_f32_16x16x32_bf16 v[44:47], a[176:179], a[132:135], v[44:47]\n"
"ds_read_b64_tr_b16 a[168:169], v2 offset:18432\n"
"v_mul_f32 v93, v93, v17\n"
"v_mul_u32_u24 v0, 1152, v0\n"
"v_mfma_f32_16x16x32_bf16 v[44:47], a[180:183], a[140:143], v[44:47]\n"
"ds_read_b64_tr_b16 a[170:171], v2 offset:26624\n"
"v_cvt_pk_bf16_f32 v105, v74, v75\n"
"v_add_f32 v16, 1.0, v16\n"
"v_mfma_f32_16x16x32_bf16 v[48:51], a[184:187], a[144:147], v[48:51]\n"
"v_cvt_pk_bf16_f32 v106, v88, v89\n"
"v_bfe_u32 v10, v9, 8, 2\n"
"v_lshlrev_b32 v10, 5, v10\n"
"v_mfma_f32_16x16x32_bf16 v[48:51], a[188:191], a[152:155], v[48:51]\n"
"ds_read_b64_tr_b16 a[172:173], v2 offset:19456\n"
"v_exp_f32 v17, v33\n"
"v_mfma_f32_16x16x32_bf16 v[52:55], a[184:187], a[148:151], v[52:55]\n"
"ds_read_b64_tr_b16 a[174:175], v2 offset:27648\n"
"v_cvt_pk_bf16_f32 v107, v90, v91\n"
"v_and_b32 v8, 0x70, v9\n"
"v_mfma_f32_16x16x32_bf16 v[52:55], a[188:191], a[156:159], v[52:55]\n"
"s_add_u32 s82, s82, s13\n"
"ds_write_b128 v6, v[104:107] offset:0\n"
"v_rcp_f32 v18, v18\n"
"v_mfma_f32_16x16x32_bf16 v[56:59], a[184:187], a[128:131], v[56:59]\n"
"buffer_load_dwordx4 v11, s[48:51], s82 offen offset:3072 lds\n"
"v_xor_b32 v10, v10, v8\n"
"v_mfma_f32_16x16x32_bf16 v[56:59], a[188:191], a[136:139], v[56:59]\n"
"v_add_f32 v19, 1.0, v19\n"
"v_add_u32 v0, v0, v10\n"
"v_rcp_f32 v16, v16\n"
"v_mfma_f32_16x16x32_bf16 v[60:63], a[184:187], a[132:135], v[60:63]\n"
"buffer_store_dwordx4 v[104:107], v6, s[64:67], s18 offen offset:0 sc1\n"
"v_add_u32 v12, s96, v0\n"
"v_mfma_f32_16x16x32_bf16 v[60:63], a[188:191], a[140:143], v[60:63]\n"
"s_waitcnt lgkmcnt(1)\n"
"s_mul_i32 s96, s73, 4608\n"
"v_mul_f32 v94, v94, v18\n"
"v_add_f32 v17, 1.0, v17\n"
"v_mfma_f32_16x16x32_bf16 v[64:67], a[160:163], v[128:131], 0\n"
"s_waitcnt vmcnt(16)\n"
"s_barrier\n"
"ds_read_b64_tr_b16 a[176:177], v1 offset:32768\n"
"v_mul_f32 v76, v76, v108\n"
"v_add_u32 v13, s96, v12\n"
"v_mfma_f32_16x16x32_bf16 v[64:67], a[164:167], v[136:139], v[64:67]\n"
"s_lshl_b32 s96, s73, 12\n"
"ds_read_b64_tr_b16 a[178:179], v1 offset:40960\n"
"v_exp_f32 v18, v34\n"
"v_mfma_f32_16x16x32_bf16 v[68:71], a[160:163], v[132:135], 0\n"
"s_add_u32 s96, s96, 81920\n"
"v_mul_f32 v77, v77, v109\n"
"v_add_u32 v7, s96, v9\n"
"v_lshrrev_b32 v8, 6, v9\n"
"v_mfma_f32_16x16x32_bf16 v[68:71], a[164:167], v[140:143], v[68:71]\n"
"s_add_u32 m0, s77, 16384\n"
"ds_read_b64_tr_b16 a[180:181], v1 offset:33792\n"
"v_mul_f32 v78, v78, v110\n"
"v_mul_f32 v32, v32, v16\n"
"v_mfma_f32_16x16x32_bf16 v[72:75], a[160:163], a[0:3], 0\n"
"s_mul_i32 s96, s72, 18432\n"
"s_add_u32 s82, s40, 4096\n"
"ds_read_b64_tr_b16 a[182:183], v1 offset:41984\n"
"v_mul_f32 v79, v79, v111\n"
"v_mul_u32_u24 v8, 1152, v8\n"
"v_bfe_u32 v10, v9, 8, 1\n"
"v_lshlrev_b32 v10, 5, v10\n"
"v_and_b32 v14, 0x30, v9\n"
"v_xor_b32 v10, v10, v14\n"
"v_add_u32 v8, v8, v10\n"
"v_add_u32 v10, s96, v8\n"
"v_mfma_f32_16x16x32_bf16 v[72:75], a[164:167], a[8:11], v[72:75]\n"
"s_nop 3\n"
"buffer_load_dwordx4 v12, s[56:59], s82 offen offset:0 lds\n"
"v_rcp_f32 v19, v19\n"
"v_cvt_pk_bf16_f32 v108, v76, v77\n"
"v_cvt_pk_bf16_f32 v109, v78, v79\n"
"v_mfma_f32_16x16x32_bf16 v[76:79], a[160:163], a[4:7], 0\n"
"ds_read_b64_tr_b16 a[184:185], v2 offset:32768\n"
"v_mul_f32 v92, v92, v124\n"
"v_rcp_f32 v17, v17\n"
"v_mfma_f32_16x16x32_bf16 v[76:79], a[164:167], a[12:15], v[76:79]\n"
"ds_read_b64_tr_b16 a[186:187], v2 offset:40960\n"
"v_mul_f32 v95, v95, v19\n"
"v_mul_f32 v93, v93, v125\n"
"v_add_f32 v18, 1.0, v18\n"
"v_mfma_f32_16x16x32_bf16 v[80:83], a[168:171], v[128:131], 0\n"
"v_mul_f32 v94, v94, v126\n"
"v_exp_f32 v19, v35\n"
"v_mfma_f32_16x16x32_bf16 v[80:83], a[172:175], v[136:139], v[80:83]\n"
"ds_read_b64_tr_b16 a[188:189], v2 offset:33792\n"
"v_mul_f32 v95, v95, v127\n"
"v_mul_f32 v33, v33, v17\n"
"v_mfma_f32_16x16x32_bf16 v[84:87], a[168:171], v[132:135], 0\n"
"s_add_u32 s82, s40, 39936\n"
"ds_read_b64_tr_b16 a[190:191], v2 offset:41984\n"
"v_rcp_f32 v18, v18\n"
"v_mfma_f32_16x16x32_bf16 v[84:87], a[172:175], v[140:143], v[84:87]\n"
"buffer_load_dwordx4 v12, s[56:59], s82 offen offset:1024 lds\n"
"v_add_f32 v19, 1.0, v19\n"
"v_mfma_f32_16x16x32_bf16 v[88:91], a[168:171], a[0:3], 0\n"
"v_exp_f32 v16, v48\n"
"v_mul_f32 v34, v34, v18\n"
"v_mfma_f32_16x16x32_bf16 v[88:91], a[172:175], a[8:11], v[88:91]\n"
"s_add_u32 s82, s40, 2176\n"
"v_cvt_pk_bf16_f32 v110, v92, v93\n"
"v_cvt_pk_bf16_f32 v111, v94, v95\n"
"v_rcp_f32 v19, v19\n"
"v_mfma_f32_16x16x32_bf16 v[92:95], a[168:171], a[4:7], 0\n"
"buffer_load_dwordx4 v12, s[56:59], s82 offen offset:2048 lds\n"
"v_mfma_f32_16x16x32_bf16 v[92:95], a[172:175], a[12:15], v[92:95]\n"
"s_waitcnt lgkmcnt(0)\n"
"v_add_f32 v16, 1.0, v16\n"
"v_mul_f32 v35, v35, v19\n"
"v_mfma_f32_16x16x32_bf16 v[64:67], a[176:179], v[144:147], v[64:67]\n"
"ds_read_b64_tr_b16 a[160:161], v1 offset:34816\n"
"v_exp_f32 v17, v49\n"
"v_mfma_f32_16x16x32_bf16 v[64:67], a[180:183], v[152:155], v[64:67]\n"
"s_lshl_b32 s96, s72, 11\n"
"ds_read_b64_tr_b16 a[162:163], v1 offset:43008\n"
"v_add_f32 v17, 1.0, v17\n"
"v_mfma_f32_16x16x32_bf16 v[68:71], a[176:179], v[148:151], v[68:71]\n"
"s_add_u32 s97, s96, 98304\n"
"v_rcp_f32 v16, v16\n"
"v_mfma_f32_16x16x32_bf16 v[68:71], a[180:183], v[156:159], v[68:71]\n"
"s_add_u32 s96, s96, s9\n"
"ds_read_b64_tr_b16 a[164:165], v1 offset:35840\n"
"v_mul_f32 v48, v48, v16\n"
"v_mfma_f32_16x16x32_bf16 v[72:75], a[176:179], a[16:19], v[72:75]\n"
"ds_read_b64_tr_b16 a[166:167], v1 offset:44032\n"
"v_and_b32 v25, 0x30, v9\n"
"v_mfma_f32_16x16x32_bf16 v[72:75], a[180:183], a[24:27], v[72:75]\n"
"v_exp_f32 v18, v50\n"
"v_mfma_f32_16x16x32_bf16 v[76:79], a[176:179], a[20:23], v[76:79]\n"
"ds_read_b64_tr_b16 a[168:169], v2 offset:34816\n"
"v_add_f32 v18, 1.0, v18\n"
"v_mfma_f32_16x16x32_bf16 v[76:79], a[180:183], a[28:31], v[76:79]\n"
"ds_read_b64_tr_b16 a[170:171], v2 offset:43008\n"
"v_lshrrev_b32 v25, 1, v25\n"
"v_mfma_f32_16x16x32_bf16 v[80:83], a[184:187], v[144:147], v[80:83]\n"
"v_rcp_f32 v17, v17\n"
"v_mfma_f32_16x16x32_bf16 v[80:83], a[188:191], v[152:155], v[80:83]\n"
"ds_read_b64_tr_b16 a[172:173], v2 offset:35840\n"
"v_mul_f32 v49, v49, v17\n"
"v_mfma_f32_16x16x32_bf16 v[84:87], a[184:187], v[148:151], v[84:87]\n"
"ds_read_b64_tr_b16 a[174:175], v2 offset:44032\n"
"v_add_u32 v15, s97, v9\n"
"v_mfma_f32_16x16x32_bf16 v[84:87], a[188:191], v[156:159], v[84:87]\n"
"s_add_u32 s82, s40, 38016\n"
"v_exp_f32 v19, v51\n"
"v_mfma_f32_16x16x32_bf16 v[88:91], a[184:187], a[16:19], v[88:91]\n"
"buffer_load_dwordx4 v12, s[56:59], s82 offen offset:3072 lds\n"
"v_mfma_f32_16x16x32_bf16 v[88:91], a[188:191], a[24:27], v[88:91]\n"
"s_add_u32 s97, s96, 155648\n"
"v_rcp_f32 v18, v18\n"
"v_mfma_f32_16x16x32_bf16 v[92:95], a[184:187], a[20:23], v[92:95]\n"
"ds_read_b128 a[224:227], v15 offset:0\n"
"v_add_f32 v19, 1.0, v19\n"
"v_mfma_f32_16x16x32_bf16 v[92:95], a[188:191], a[28:31], v[92:95]\n"
"s_waitcnt lgkmcnt(1)\n"
"ds_read_b128 a[228:231], v15 offset:1024\n"
"ds_read_b128 a[232:235], v15 offset:61440\n"
"v_mfma_f32_16x16x32_bf16 v[64:67], a[160:163], v[160:163], v[64:67]\n"
"s_add_u32 s16, s19, 64\n"
"s_waitcnt vmcnt(16)\n"
"s_barrier\n"
"ds_read_b64_tr_b16 a[176:177], v1 offset:0\n"
"v_mfma_f32_16x16x32_bf16 v[64:67], a[164:167], v[168:171], v[64:67]\n"
"ds_read_b128 a[236:239], v15 offset:62464\n"
"ds_read_b64_tr_b16 a[178:179], v1 offset:8192\n"
"v_mfma_f32_16x16x32_bf16 v[68:71], a[160:163], v[164:167], v[68:71]\n"
"buffer_load_dwordx2 v[26:27], v25, s[64:67], s16 offen sc1\n"
"v_mfma_f32_16x16x32_bf16 v[68:71], a[164:167], v[172:175], v[68:71]\n"
"ds_read_b64_tr_b16 a[180:181], v1 offset:1024\n"
"v_mul_f32 v50, v50, v18\n"
"v_mfma_f32_16x16x32_bf16 v[72:75], a[160:163], a[32:35], v[72:75]\n"
"ds_read_b64_tr_b16 a[182:183], v1 offset:9216\n"
"v_lshrrev_b32 v14, 6, v9\n"
"v_mfma_f32_16x16x32_bf16 v[72:75], a[164:167], a[40:43], v[72:75]\n"
"v_exp_f32 v16, v36\n"
"v_mfma_f32_16x16x32_bf16 v[76:79], a[160:163], a[36:39], v[76:79]\n"
"ds_read_b64_tr_b16 a[184:185], v2 offset:0\n"
"v_add_f32 v16, 1.0, v16\n"
"v_mfma_f32_16x16x32_bf16 v[76:79], a[164:167], a[44:47], v[76:79]\n"
"ds_read_b64_tr_b16 a[186:187], v2 offset:8192\n"
"v_lshlrev_b32 v14, 6, v14\n"
"v_mfma_f32_16x16x32_bf16 v[80:83], a[168:171], v[160:163], v[80:83]\n"
"s_waitcnt lgkmcnt(9)\n"
"buffer_store_dwordx4 a[224:227], v9, s[64:67], s97 offen offset:0 sc1\n"
"v_mfma_f32_16x16x32_bf16 v[80:83], a[172:175], v[168:171], v[80:83]\n"
"ds_read_b64_tr_b16 a[188:189], v2 offset:1024\n"
"v_add_u32 v14, v14, v25\n"
"v_mfma_f32_16x16x32_bf16 v[84:87], a[168:171], v[164:167], v[84:87]\n"
"ds_read_b64_tr_b16 a[190:191], v2 offset:9216\n"
"v_bfe_u32 v96, v9, 8, 1\n"
"v_mfma_f32_16x16x32_bf16 v[84:87], a[172:175], v[172:175], v[84:87]\n"
"v_rcp_f32 v19, v19\n"
"v_mfma_f32_16x16x32_bf16 v[88:91], a[168:171], a[32:35], v[88:91]\n"
"v_exp_f32 v17, v37\n"
"v_mfma_f32_16x16x32_bf16 v[88:91], a[172:175], a[40:43], v[88:91]\n"
"s_waitcnt lgkmcnt(10)\n"
"buffer_store_dwordx4 a[228:231], v9, s[64:67], s97 offen offset:1024 sc1\n"
"v_mfma_f32_16x16x32_bf16 v[92:95], a[168:171], a[36:39], v[92:95]\n"
"s_add_u32 s97, s96, 163840\n"
"v_mul_f32 v51, v51, v19\n"
"v_add_f32 v17, 1.0, v17\n"
"v_mfma_f32_16x16x32_bf16 v[92:95], a[172:175], a[44:47], v[92:95]\n"
"s_waitcnt lgkmcnt(0)\n"
"s_cmp_lt_u32 s72, 2\n"
"s_cselect_b32 s97, s97, 0x7ff00000\n"
"v_mfma_f32_16x16x32_bf16 v[64:67], a[176:179], v[176:179], v[64:67]\n"
"s_lshl_b32 s96, s72, 11\n"
"s_add_u32 m0, s96, 147456\n"
"s_lshl_b32 s96, s72, 7\n"
"ds_read_b64_tr_b16 a[160:161], v1 offset:2048\n"
"v_mfma_f32_16x16x32_bf16 v[64:67], a[180:183], v[184:187], v[64:67]\n"
"ds_read_b64_tr_b16 a[162:163], v1 offset:10240\n"
"buffer_store_dwordx4 a[232:235], v9, s[64:67], s97 offen offset:0 sc1\n"
"v_mfma_f32_16x16x32_bf16 v[68:71], a[176:179], v[180:183], v[68:71]\n"
"v_rcp_f32 v16, v16\n"
"v_mfma_f32_16x16x32_bf16 v[68:71], a[180:183], v[188:191], v[68:71]\n"
"ds_read_b64_tr_b16 a[164:165], v1 offset:3072\n"
"buffer_store_dwordx4 a[236:239], v9, s[64:67], s97 offen offset:1024 sc1\n"
"v_mfma_f32_16x16x32_bf16 v[72:75], a[176:179], a[48:51], v[72:75]\n"
"s_add_u32 s97, s96, s80\n"
"s_add_u32 s97, s97, 22528\n"
"ds_read_b64_tr_b16 a[166:167], v1 offset:11264\n"
"v_mfma_f32_16x16x32_bf16 v[72:75], a[180:183], a[56:59], v[72:75]\n"
"buffer_load_dwordx4 v0, s[60:63], s97 offen offset:0 lds\n"
"v_mfma_f32_16x16x32_bf16 v[76:79], a[176:179], a[52:55], v[76:79]\n"
"ds_read_b64_tr_b16 a[168:169], v2 offset:2048\n"
"v_exp_f32 v18, v38\n"
"v_mfma_f32_16x16x32_bf16 v[76:79], a[180:183], a[60:63], v[76:79]\n"
"ds_read_b64_tr_b16 a[170:171], v2 offset:10240\n"
"v_mul_f32 v36, v36, v16\n"
"v_mfma_f32_16x16x32_bf16 v[80:83], a[184:187], v[176:179], v[80:83]\n"
"v_rcp_f32 v17, v17\n"
"v_mfma_f32_16x16x32_bf16 v[80:83], a[188:191], v[184:187], v[80:83]\n"
"ds_read_b64_tr_b16 a[172:173], v2 offset:3072\n"
"v_add_f32 v18, 1.0, v18\n"
"v_mfma_f32_16x16x32_bf16 v[84:87], a[184:187], v[180:183], v[84:87]\n"
"s_add_u32 s97, s96, s81\n"
"s_add_u32 s97, s97, 21504\n"
"ds_read_b64_tr_b16 a[174:175], v2 offset:11264\n"
"v_mfma_f32_16x16x32_bf16 v[84:87], a[188:191], v[188:191], v[84:87]\n"
"buffer_load_dwordx4 v0, s[60:63], s97 offen offset:1024 lds\n"
"v_mfma_f32_16x16x32_bf16 v[88:91], a[184:187], a[48:51], v[88:91]\n"
"s_cmp_eq_u32 s74, 0\n"
"s_cselect_b32 s97, s80, s81\n"
"s_sub_u32 s97, s97, s78\n"
"s_add_u32 s97, s97, s79\n"
"v_mfma_f32_16x16x32_bf16 v[88:91], a[188:191], a[56:59], v[88:91]\n"
"s_add_u32 s97, s97, 22528\n"
"s_lshl_b32 s96, s74, 10\n"
"s_add_u32 m0, s96, 155648\n"
"v_mfma_f32_16x16x32_bf16 v[92:95], a[184:187], a[52:55], v[92:95]\n"
"buffer_load_dwordx4 v8, s[60:63], s97 offen lds\n"
"v_mfma_f32_16x16x32_bf16 v[92:95], a[188:191], a[60:63], v[92:95]\n"
"s_waitcnt lgkmcnt(0)\n"
"v_exp_f32 v19, v39\n"
"v_mfma_f32_16x16x32_bf16 v[64:67], a[160:163], v[192:195], v[64:67]\n"
"s_waitcnt vmcnt(19)\n"
"s_barrier\n"
"ds_read_b64_tr_b16 a[176:177], v1 offset:49152\n"
"v_mul_f32 v37, v37, v17\n"
"v_mfma_f32_16x16x32_bf16 v[64:67], a[164:167], v[200:203], v[64:67]\n"
"ds_read_b64_tr_b16 a[178:179], v1 offset:57344\n"
"v_rcp_f32 v18, v18\n"
"v_mfma_f32_16x16x32_bf16 v[68:71], a[160:163], v[196:199], v[68:71]\n"
"v_add_f32 v19, 1.0, v19\n"
"v_mul_f32 v38, v38, v18\n"
"v_mfma_f32_16x16x32_bf16 v[68:71], a[164:167], v[204:207], v[68:71]\n"
"s_add_u32 m0, s77, 0\n"
"ds_read_b64_tr_b16 a[180:181], v1 offset:50176\n"
"v_xor_b32 v96, s74, v96\n"
"v_mfma_f32_16x16x32_bf16 v[72:75], a[160:163], a[64:67], v[72:75]\n"
"s_add_u32 s82, s40, 4352\n"
"ds_read_b64_tr_b16 a[182:183], v1 offset:58368\n"
"v_lshrrev_b32 v107, 1, v9\n"
"v_mfma_f32_16x16x32_bf16 v[72:75], a[164:167], a[72:75], v[72:75]\n"
"buffer_load_dwordx4 v12, s[56:59], s82 offen offset:0 lds\n"
"v_mfma_f32_16x16x32_bf16 v[76:79], a[160:163], a[68:71], v[76:79]\n"
"ds_read_b64_tr_b16 a[184:185], v2 offset:49152\n"
"v_exp_f32 v16, v52\n"
"v_mfma_f32_16x16x32_bf16 v[76:79], a[164:167], a[76:79], v[76:79]\n"
"ds_read_b64_tr_b16 a[186:187], v2 offset:57344\n"
"v_add_f32 v16, 1.0, v16\n"
"v_mfma_f32_16x16x32_bf16 v[80:83], a[168:171], v[192:195], v[80:83]\n"
"v_rcp_f32 v19, v19\n"
"v_mfma_f32_16x16x32_bf16 v[80:83], a[172:175], v[200:203], v[80:83]\n"
"ds_read_b64_tr_b16 a[188:189], v2 offset:50176\n"
"v_mul_f32 v39, v39, v19\n"
"v_mfma_f32_16x16x32_bf16 v[84:87], a[168:171], v[196:199], v[84:87]\n"
"s_add_u32 s82, s40, 40192\n"
"ds_read_b64_tr_b16 a[190:191], v2 offset:58368\n"
"v_lshrrev_b32 v8, 1, v7\n"
"v_mfma_f32_16x16x32_bf16 v[84:87], a[172:175], v[204:207], v[84:87]\n"
"buffer_load_dwordx4 v12, s[56:59], s82 offen offset:1024 lds\n"
"v_mfma_f32_16x16x32_bf16 v[88:91], a[168:171], a[64:67], v[88:91]\n"
"v_exp_f32 v17, v53\n"
"v_mfma_f32_16x16x32_bf16 v[88:91], a[172:175], a[72:75], v[88:91]\n"
"s_add_u32 s82, s40, 2432\n"
"v_rcp_f32 v16, v16\n"
"v_mfma_f32_16x16x32_bf16 v[92:95], a[168:171], a[68:71], v[92:95]\n"
"buffer_load_dwordx4 v12, s[56:59], s82 offen offset:2048 lds\n"
"v_mfma_f32_16x16x32_bf16 v[92:95], a[172:175], a[76:79], v[92:95]\n"
"s_waitcnt lgkmcnt(0)\n"
"v_lshl_add_u32 v96, v96, 5, v14\n"
"v_add_f32 v17, 1.0, v17\n"
"v_mfma_f32_16x16x32_bf16 v[64:67], a[176:179], v[208:211], v[64:67]\n"
"ds_read_b64_tr_b16 a[160:161], v1 offset:51200\n"
"v_add_u32 v97, 0x10000, v96\n"
"v_mfma_f32_16x16x32_bf16 v[64:67], a[180:183], v[216:219], v[64:67]\n"
"ds_read_b64_tr_b16 a[162:163], v1 offset:59392\n"
"v_add_u32 v103, 147456, v96\n"
"v_mfma_f32_16x16x32_bf16 v[68:71], a[176:179], v[212:215], v[68:71]\n"
"v_exp_f32 v18, v54\n"
"v_mfma_f32_16x16x32_bf16 v[68:71], a[180:183], v[220:223], v[68:71]\n"
"ds_read_b64_tr_b16 a[164:165], v1 offset:52224\n"
"v_mul_f32 v52, v52, v16\n"
"v_mfma_f32_16x16x32_bf16 v[72:75], a[176:179], a[80:83], v[72:75]\n"
"ds_read_b64_tr_b16 a[166:167], v1 offset:60416\n"
"v_and_b32 v101, 0xfff, v1\n"
"v_mfma_f32_16x16x32_bf16 v[72:75], a[180:183], a[88:91], v[72:75]\n"
"v_rcp_f32 v17, v17\n"
"v_mfma_f32_16x16x32_bf16 v[76:79], a[176:179], a[84:87], v[76:79]\n"
"ds_read_b64_tr_b16 a[168:169], v2 offset:51200\n"
"v_add_u32 v101, 147456, v101\n"
"v_mfma_f32_16x16x32_bf16 v[76:79], a[180:183], a[92:95], v[76:79]\n"
"ds_read_b64_tr_b16 a[170:171], v2 offset:59392\n"
"v_and_b32 v102, 0xfff, v2\n"
"v_mfma_f32_16x16x32_bf16 v[80:83], a[184:187], v[208:211], v[80:83]\n"
"v_add_u32 v102, 147456, v102\n"
"v_add_f32 v18, 1.0, v18\n"
"v_mfma_f32_16x16x32_bf16 v[80:83], a[188:191], v[216:219], v[80:83]\n"
"ds_read_b64_tr_b16 a[172:173], v2 offset:52224\n"
"v_lshl_add_u32 v104, s73, 10, v9\n"
"v_mfma_f32_16x16x32_bf16 v[84:87], a[184:187], v[212:215], v[84:87]\n"
"ds_read_b64_tr_b16 a[174:175], v2 offset:60416\n"
"v_mul_f32 v53, v53, v17\n"
"v_mfma_f32_16x16x32_bf16 v[84:87], a[188:191], v[220:223], v[84:87]\n"
"s_add_u32 s82, s40, 38272\n"
"v_exp_f32 v19, v55\n"
"v_mfma_f32_16x16x32_bf16 v[88:91], a[184:187], a[80:83], v[88:91]\n"
"buffer_load_dwordx4 v12, s[56:59], s82 offen offset:3072 lds\n"
"v_mfma_f32_16x16x32_bf16 v[88:91], a[188:191], a[88:91], v[88:91]\n"
"ds_write_b128 v6, v[108:111] offset:1024\n"
"v_mfma_f32_16x16x32_bf16 v[92:95], a[184:187], a[84:87], v[92:95]\n"
"buffer_store_dwordx4 v[108:111], v6, s[64:67], s18 offen offset:1024 sc1\n"
"v_lshl_add_u32 v107, s73, 13, v107\n"
"v_mfma_f32_16x16x32_bf16 v[92:95], a[188:191], a[92:95], v[92:95]\n"
"s_waitcnt lgkmcnt(1)\n"
"s_waitcnt vmcnt(12)\n"
"v_cmp_ne_u64 vcc, v[26:27], v[22:23]\n"
"s_nop 1\n"
"s_cbranch_vccz L_ok2_%=\n"
"s_mov_b32 s100, 0x4000\n"
"L_rt2_%=:\n"
"s_sleep 2\n"
"buffer_load_dwordx2 v[26:27], v25, s[64:67], s16 offen sc1\n"
"s_waitcnt vmcnt(0)\n"
"v_cmp_ne_u64 vcc, v[26:27], v[22:23]\n"
"s_nop 4\n"
"s_cbranch_vccz L_ok2_%=\n"
"s_sub_u32 s100, s100, 1\n"
"s_cmp_lg_u32 s100, 0\n"
"s_cbranch_scc1 L_rt2_%=\n"
"L_ok2_%=:\n"
"v_mfma_f32_16x16x32_bf16 v[64:67], a[160:163], v[224:227], v[64:67]\n"
"s_barrier\n"
"ds_read_b64_tr_b16 a[176:177], v3 offset:0\n"
"v_add_u32 v105, 0x10000, v104\n"
"v_add_u32 v108, 0x10000, v107\n"
"v_mfma_f32_16x16x32_bf16 v[64:67], a[164:167], v[232:235], v[64:67]\n"
"ds_read_b64_tr_b16 a[178:179], v3 offset:8192\n"
"v_rcp_f32 v18, v18\n"
"v_mfma_f32_16x16x32_bf16 v[68:71], a[160:163], v[228:231], v[68:71]\n"
"s_add_u32 s18, s9, 49152\n"
"v_add_f32 v19, 1.0, v19\n"
"v_mul_f32 v54, v54, v18\n"
"v_mfma_f32_16x16x32_bf16 v[68:71], a[164:167], v[236:239], v[68:71]\n"
"s_add_u32 m0, s77, 32768\n"
"ds_read_b64_tr_b16 a[180:181], v3 offset:1024\n"
"v_exp_f32 v16, v40\n"
"v_mfma_f32_16x16x32_bf16 v[72:75], a[160:163], a[96:99], v[72:75]\n"
"s_add_u32 s82, s8, 3072\n"
"ds_read_b64_tr_b16 a[182:183], v3 offset:9216\n"
"v_add_f32 v16, 1.0, v16\n"
"v_mfma_f32_16x16x32_bf16 v[72:75], a[164:167], a[104:107], v[72:75]\n"
"buffer_load_dwordx4 v9, s[64:67], s82 offen offset:0 sc1 lds\n"
"v_mfma_f32_16x16x32_bf16 v[76:79], a[160:163], a[100:103], v[76:79]\n"
"ds_read_b128 a[192:195], v7 offset:49152\n"
"ds_read_b64_tr_b16 a[184:185], v4 offset:0\n"
"v_mfma_f32_16x16x32_bf16 v[76:79], a[164:167], a[108:111], v[76:79]\n"
"ds_read_b64_tr_b16 a[186:187], v4 offset:8192\n"
"v_rcp_f32 v19, v19\n"
"v_mfma_f32_16x16x32_bf16 v[80:83], a[168:171], v[224:227], v[80:83]\n"
"ds_read_b128 a[196:199], v7 offset:50176\n"
"ds_read_b128 a[200:203], v7 offset:51200\n"
"v_mfma_f32_16x16x32_bf16 v[80:83], a[172:175], v[232:235], v[80:83]\n"
"ds_read_b128 a[204:207], v7 offset:52224\n"
"ds_read_b64_tr_b16 a[188:189], v4 offset:1024\n"
"v_mfma_f32_16x16x32_bf16 v[84:87], a[168:171], v[228:231], v[84:87]\n"
"s_addk_i32 s82, 3072\n"
"ds_read_b64_tr_b16 a[190:191], v4 offset:9216\n"
"v_exp_f32 v17, v41\n"
"v_mfma_f32_16x16x32_bf16 v[84:87], a[172:175], v[236:239], v[84:87]\n"
"buffer_load_dwordx4 v9, s[64:67], s82 offen offset:1024 sc1 lds\n"
"v_mfma_f32_16x16x32_bf16 v[88:91], a[168:171], a[96:99], v[88:91]\n"
"ds_read_b128 a[208:211], v7 offset:57344\n"
"v_mul_f32 v55, v55, v19\n"
"v_mfma_f32_16x16x32_bf16 v[88:91], a[172:175], a[104:107], v[88:91]\n"
"s_addk_i32 s82, 3072\n"
"v_rcp_f32 v16, v16\n"
"v_mfma_f32_16x16x32_bf16 v[92:95], a[168:171], a[100:103], v[92:95]\n"
"buffer_load_dwordx4 v9, s[64:67], s82 offen offset:2048 sc1 lds\n"
"v_mfma_f32_16x16x32_bf16 v[92:95], a[172:175], a[108:111], v[92:95]\n"
"s_waitcnt lgkmcnt(1)\n"
"ds_read_b128 a[212:215], v7 offset:58368\n"
"ds_read_b128 a[216:219], v7 offset:59392\n"
"v_mfma_f32_16x16x32_bf16 v[64:67], a[176:179], v[240:243], v[64:67]\n"
"ds_read_b64_tr_b16 a[160:161], v3 offset:2048\n"
"v_add_f32 v17, 1.0, v17\n"
"v_mfma_f32_16x16x32_bf16 v[64:67], a[180:183], v[248:251], v[64:67]\n"
"ds_read_b128 a[220:223], v7 offset:60416\n"
"ds_read_b64_tr_b16 a[162:163], v3 offset:10240\n"
"v_mfma_f32_16x16x32_bf16 v[68:71], a[176:179], v[244:247], v[68:71]\n"
"v_exp_f32 v18, v42\n"
"v_mfma_f32_16x16x32_bf16 v[68:71], a[180:183], v[252:255], v[68:71]\n"
"ds_read_b128 a[224:227], v7 offset:32768\n"
"ds_read_b64_tr_b16 a[164:165], v3 offset:3072\n"
"v_mfma_f32_16x16x32_bf16 v[72:75], a[176:179], a[112:115], v[72:75]\n"
"s_addk_i32 s82, 3072\n"
"ds_read_b128 a[228:231], v7 offset:33792\n"
"ds_read_b64_tr_b16 a[166:167], v3 offset:11264\n"
"v_mfma_f32_16x16x32_bf16 v[72:75], a[180:183], a[120:123], v[72:75]\n"
"buffer_load_dwordx4 v9, s[64:67], s82 offen offset:3072 sc1 lds\n"
"v_mul_f32 v40, v40, v16\n"
"v_mfma_f32_16x16x32_bf16 v[76:79], a[176:179], a[116:119], v[76:79]\n"
"ds_read_b128 a[232:235], v7 offset:34816\n"
"ds_read_b64_tr_b16 a[168:169], v4 offset:2048\n"
"v_mfma_f32_16x16x32_bf16 v[76:79], a[180:183], a[124:127], v[76:79]\n"
"ds_read_b64_tr_b16 a[170:171], v4 offset:10240\n"
"v_rcp_f32 v17, v17\n"
"v_mfma_f32_16x16x32_bf16 v[80:83], a[184:187], v[240:243], v[80:83]\n"
"ds_read_b128 a[236:239], v7 offset:35840\n"
"ds_read_b128 a[240:243], v7 offset:40960\n"
"v_add_f32 v18, 1.0, v18\n"
"v_mfma_f32_16x16x32_bf16 v[80:83], a[188:191], v[248:251], v[80:83]\n"
"s_addk_i32 m0, 16384\n"
"ds_read_b128 a[244:247], v7 offset:41984\n"
"ds_read_b64_tr_b16 a[172:173], v4 offset:3072\n"
"v_mfma_f32_16x16x32_bf16 v[84:87], a[184:187], v[244:247], v[84:87]\n"
"s_add_u32 s82, s4, 4096\n"
"ds_read_b64_tr_b16 a[174:175], v4 offset:11264\n"
"v_exp_f32 v19, v43\n"
"v_mfma_f32_16x16x32_bf16 v[84:87], a[188:191], v[252:255], v[84:87]\n"
"ds_read_b128 a[248:251], v7 offset:43008\n"
"ds_read_b128 a[252:255], v7 offset:44032\n"
"buffer_load_dwordx4 v10, s[56:59], s82 offen offset:0 lds\n"
"v_mfma_f32_16x16x32_bf16 v[88:91], a[184:187], a[112:115], v[88:91]\n"
"v_mul_f32 v41, v41, v17\n"
"v_add_f32 v19, 1.0, v19\n"
"v_mfma_f32_16x16x32_bf16 v[88:91], a[188:191], a[120:123], v[88:91]\n"
"s_add_u32 s82, s8, 18432\n"
"v_rcp_f32 v18, v18\n"
"v_mfma_f32_16x16x32_bf16 v[92:95], a[184:187], a[116:119], v[92:95]\n"
"buffer_load_dwordx4 v9, s[64:67], s82 offen offset:1024 sc1 lds\n"
"v_mfma_f32_16x16x32_bf16 v[92:95], a[188:191], a[124:127], v[92:95]\n"
"s_waitcnt lgkmcnt(2)\n"
"v_exp_f32 v16, v56\n"
"v_mfma_f32_16x16x32_bf16 v[64:67], a[160:163], a[144:147], v[64:67]\n"
"s_waitcnt lgkmcnt(0)\n"
"s_barrier\n"
"ds_read_b64_tr_b16 a[176:177], v1 offset:16384\n"
"v_mfma_f32_16x16x32_bf16 v[64:67], a[164:167], a[152:155], v[64:67]\n"
"ds_read_b64_tr_b16 a[178:179], v1 offset:24576\n"
"v_mul_f32 v42, v42, v18\n"
"v_mfma_f32_16x16x32_bf16 v[68:71], a[160:163], a[148:151], v[68:71]\n"
"v_rcp_f32 v19, v19\n"
"v_mfma_f32_16x16x32_bf16 v[68:71], a[164:167], a[156:159], v[68:71]\n"
"ds_read_b64_tr_b16 a[180:181], v1 offset:17408\n"
"v_add_f32 v16, 1.0, v16\n"
"v_mfma_f32_16x16x32_bf16 v[72:75], a[160:163], a[128:131], v[72:75]\n"
"s_addk_i32 s82, 3072\n"
"ds_read_b64_tr_b16 a[182:183], v1 offset:25600\n"
"v_mul_f32 v43, v43, v19\n"
"v_mfma_f32_16x16x32_bf16 v[72:75], a[164:167], a[136:139], v[72:75]\n"
"buffer_load_dwordx4 v9, s[64:67], s82 offen offset:2048 sc1 lds\n"
"v_mfma_f32_16x16x32_bf16 v[76:79], a[160:163], a[132:135], v[76:79]\n"
"ds_read_b64_tr_b16 a[184:185], v2 offset:16384\n"
"v_mul_f32 v32, v32, v64\n"
"v_mfma_f32_16x16x32_bf16 v[76:79], a[164:167], a[140:143], v[76:79]\n"
"ds_read_b64_tr_b16 a[186:187], v2 offset:24576\n"
"v_mul_f32 v33, v33, v65\n"
"v_mfma_f32_16x16x32_bf16 v[80:83], a[168:171], a[144:147], v[80:83]\n"
"v_exp_f32 v17, v57\n"
"v_mfma_f32_16x16x32_bf16 v[80:83], a[172:175], a[152:155], v[80:83]\n"
"ds_read_b64_tr_b16 a[188:189], v2 offset:17408\n"
"v_add_f32 v17, 1.0, v17\n"
"v_mfma_f32_16x16x32_bf16 v[84:87], a[168:171], a[148:151], v[84:87]\n"
"s_add_u32 s82, s40, 74752\n"
"ds_read_b64_tr_b16 a[190:191], v2 offset:25600\n"
"v_mul_f32 v34, v34, v66\n"
"v_mfma_f32_16x16x32_bf16 v[84:87], a[172:175], a[156:159], v[84:87]\n"
"buffer_load_dwordx4 v12, s[56:59], s82 offen offset:3072 lds\n"
"v_mfma_f32_16x16x32_bf16 v[88:91], a[168:171], a[128:131], v[88:91]\n"
"s_addk_i32 m0, 16384\n"
"v_rcp_f32 v16, v16\n"
"v_mfma_f32_16x16x32_bf16 v[88:91], a[172:175], a[136:139], v[88:91]\n"
"s_add_u32 s82, s40, 114688\n"
"v_exp_f32 v18, v58\n"
"v_mfma_f32_16x16x32_bf16 v[92:95], a[168:171], a[132:135], v[92:95]\n"
"buffer_load_dwordx4 v12, s[56:59], s82 offen offset:0 lds\n"
"v_mfma_f32_16x16x32_bf16 v[92:95], a[172:175], a[140:143], v[92:95]\n"
"s_waitcnt lgkmcnt(0)\n"
"v_mul_f32 v56, v56, v16\n"
"v_add_f32 v18, 1.0, v18\n"
"v_mfma_f32_16x16x32_bf16 v[128:131], a[176:179], a[192:195], 0\n"
"ds_read_b64_tr_b16 a[160:161], v1 offset:18432\n"
"v_mul_f32 v35, v35, v67\n"
"v_mfma_f32_16x16x32_bf16 v[128:131], a[180:183], a[208:211], v[128:131]\n"
"s_add_u32 s82, s8, 26624\n"
"ds_read_b64_tr_b16 a[162:163], v1 offset:26624\n"
"v_mul_f32 v48, v48, v80\n"
"v_mfma_f32_16x16x32_bf16 v[132:135], a[176:179], a[196:199], 0\n"
"buffer_load_dwordx4 v9, s[64:67], s82 offen offset:1024 sc1 lds\n"
"v_mfma_f32_16x16x32_bf16 v[132:135], a[180:183], a[212:215], v[132:135]\n"
"ds_read_b64_tr_b16 a[164:165], v1 offset:19456\n"
"v_mul_f32 v49, v49, v81\n"
"v_mfma_f32_16x16x32_bf16 v[136:139], a[176:179], a[200:203], 0\n"
"ds_read_b64_tr_b16 a[166:167], v1 offset:27648\n"
"v_rcp_f32 v17, v17\n"
"v_mfma_f32_16x16x32_bf16 v[136:139], a[180:183], a[216:219], v[136:139]\n"
"v_exp_f32 v19, v59\n"
"v_mfma_f32_16x16x32_bf16 v[140:143], a[176:179], a[204:207], 0\n"
"ds_read_b64_tr_b16 a[168:169], v2 offset:18432\n"
"v_mul_f32 v57, v57, v17\n"
"v_mfma_f32_16x16x32_bf16 v[140:143], a[180:183], a[220:223], v[140:143]\n"
"s_addk_i32 s82, 3072\n"
"ds_read_b64_tr_b16 a[170:171], v2 offset:26624\n"
"v_add_f32 v19, 1.0, v19\n"
"v_mfma_f32_16x16x32_bf16 v[144:147], a[184:187], a[192:195], 0\n"
"buffer_load_dwordx4 v9, s[64:67], s82 offen offset:2048 sc1 lds\n"
"v_mfma_f32_16x16x32_bf16 v[144:147], a[188:191], a[208:211], v[144:147]\n"
"ds_read_b64_tr_b16 a[172:173], v2 offset:19456\n"
"v_mul_f32 v50, v50, v82\n"
"v_mfma_f32_16x16x32_bf16 v[148:151], a[184:187], a[196:199], 0\n"
"ds_read_b64_tr_b16 a[174:175], v2 offset:27648\n"
"v_mul_f32 v51, v51, v83\n"
"v_mfma_f32_16x16x32_bf16 v[148:151], a[188:191], a[212:215], v[148:151]\n"
"s_add_u32 s82, s40, 74880\n"
"v_rcp_f32 v18, v18\n"
"v_mfma_f32_16x16x32_bf16 v[152:155], a[184:187], a[200:203], 0\n"
"buffer_load_dwordx4 v12, s[56:59], s82 offen offset:3072 lds\n"
"v_mfma_f32_16x16x32_bf16 v[152:155], a[188:191], a[216:219], v[152:155]\n"
"s_add_u32 m0, s77, 114688\n"
"v_exp_f32 v16, v44\n"
"v_mfma_f32_16x16x32_bf16 v[156:159], a[184:187], a[204:207], 0\n"
"s_add_u32 s82, s40, 114816\n"
"v_mul_f32 v58, v58, v18\n"
"v_add_f32 v16, 1.0, v16\n"
"v_mfma_f32_16x16x32_bf16 v[156:159], a[188:191], a[220:223], v[156:159]\n"
"s_waitcnt lgkmcnt(0)\n"
"buffer_load_dwordx4 v12, s[56:59], s82 offen offset:0 lds\n"
"v_mfma_f32_16x16x32_bf16 v[160:163], a[160:163], a[192:195], 0\n"
"s_waitcnt vmcnt(9)\n"
"s_barrier\n"
"ds_read_b64_tr_b16 a[176:177], v1 offset:0\n"
"buffer_store_dwordx2 v[20:21], v24, s[64:67], s14 offen sc1\n"
"v_mfma_f32_16x16x32_bf16 v[160:163], a[164:167], a[208:211], v[160:163]\n"
"ds_read_b64_tr_b16 a[178:179], v1 offset:8192\n"
"v_cvt_pk_bf16_f32 v64, v32, v33\n"
"v_mfma_f32_16x16x32_bf16 v[164:167], a[160:163], a[196:199], 0\n"
"v_rcp_f32 v19, v19\n"
"v_mfma_f32_16x16x32_bf16 v[164:167], a[164:167], a[212:215], v[164:167]\n"
"ds_read_b64_tr_b16 a[180:181], v1 offset:1024\n"
"v_cvt_pk_bf16_f32 v65, v34, v35\n"
"v_mfma_f32_16x16x32_bf16 v[168:171], a[160:163], a[200:203], 0\n"
"s_add_u32 s82, s40, 77056\n"
"ds_read_b64_tr_b16 a[182:183], v1 offset:9216\n"
"v_mul_f32 v59, v59, v19\n"
"v_mfma_f32_16x16x32_bf16 v[168:171], a[164:167], a[216:219], v[168:171]\n"
"buffer_load_dwordx4 v12, s[56:59], s82 offen offset:1024 lds\n"
"v_mfma_f32_16x16x32_bf16 v[172:175], a[160:163], a[204:207], 0\n"
"ds_read_b64_tr_b16 a[184:185], v2 offset:0\n"
"v_exp_f32 v17, v45\n"
"v_mfma_f32_16x16x32_bf16 v[172:175], a[164:167], a[220:223], v[172:175]\n"
"ds_read_b64_tr_b16 a[186:187], v2 offset:8192\n"
"v_cvt_pk_bf16_f32 v66, v48, v49\n"
"v_mfma_f32_16x16x32_bf16 v[176:179], a[168:171], a[192:195], 0\n"
"v_add_f32 v17, 1.0, v17\n"
"v_cvt_pk_bf16_f32 v67, v50, v51\n"
"v_mfma_f32_16x16x32_bf16 v[176:179], a[172:175], a[208:211], v[176:179]\n"
"ds_read_b64_tr_b16 a[188:189], v2 offset:1024\n"
"v_mul_f32 v36, v36, v68\n"
"v_mfma_f32_16x16x32_bf16 v[180:183], a[168:171], a[196:199], 0\n"
"s_add_u32 s82, s40, 112896\n"
"ds_read_b64_tr_b16 a[190:191], v2 offset:9216\n"
"v_rcp_f32 v16, v16\n"
"v_mfma_f32_16x16x32_bf16 v[180:183], a[172:175], a[212:215], v[180:183]\n"
"buffer_load_dwordx4 v12, s[56:59], s82 offen offset:2048 lds\n"
"v_mfma_f32_16x16x32_bf16 v[184:187], a[168:171], a[200:203], 0\n"
"ds_write_b128 v5, v[64:67] offset:49152\n"
"v_mfma_f32_16x16x32_bf16 v[184:187], a[172:175], a[216:219], v[184:187]\n"
"s_add_u32 s82, s40, 75136\n"
"buffer_store_dwordx4 v[64:67], v5, s[64:67], s18 offen offset:0 sc1\n"
"v_mfma_f32_16x16x32_bf16 v[188:191], a[168:171], a[204:207], 0\n"
"buffer_load_dwordx4 v12, s[56:59], s82 offen offset:3072 lds\n"
"v_mfma_f32_16x16x32_bf16 v[188:191], a[172:175], a[220:223], v[188:191]\n"
"s_waitcnt lgkmcnt(1)\n"
"v_exp_f32 v18, v46\n"
"v_mfma_f32_16x16x32_bf16 v[192:195], a[176:179], a[192:195], 0\n"
"s_add_u32 m0, s77, 16384\n"
"ds_read_b64_tr_b16 a[160:161], v1 offset:2048\n"
"v_mul_f32 v44, v44, v16\n"
"v_mfma_f32_16x16x32_bf16 v[192:195], a[180:183], a[208:211], v[192:195]\n"
"s_add_u32 s82, s40, 115072\n"
"ds_read_b64_tr_b16 a[162:163], v1 offset:10240\n"
"v_mul_f32 v37, v37, v69\n"
"v_mfma_f32_16x16x32_bf16 v[196:199], a[176:179], a[196:199], 0\n"
"buffer_load_dwordx4 v12, s[56:59], s82 offen offset:0 lds\n"
"v_mul_f32 v38, v38, v70\n"
"v_mfma_f32_16x16x32_bf16 v[196:199], a[180:183], a[212:215], v[196:199]\n"
"ds_read_b64_tr_b16 a[164:165], v1 offset:3072\n"
"v_rcp_f32 v17, v17\n"
"v_mfma_f32_16x16x32_bf16 v[200:203], a[176:179], a[200:203], 0\n"
"ds_read_b64_tr_b16 a[166:167], v1 offset:11264\n"
"v_add_f32 v18, 1.0, v18\n"
"v_mul_f32 v39, v39, v71\n"
"v_mfma_f32_16x16x32_bf16 v[200:203], a[180:183], a[216:219], v[200:203]\n"
"v_exp_f32 v19, v47\n"
"v_mul_f32 v52, v52, v84\n"
"v_mfma_f32_16x16x32_bf16 v[204:207], a[176:179], a[204:207], 0\n"
"ds_read_b64_tr_b16 a[168:169], v2 offset:2048\n"
"v_mul_f32 v53, v53, v85\n"
"v_mfma_f32_16x16x32_bf16 v[204:207], a[180:183], a[220:223], v[204:207]\n"
"s_add_u32 s82, s8, 67584\n"
"ds_read_b64_tr_b16 a[170:171], v2 offset:10240\n"
"v_mul_f32 v54, v54, v86\n"
"v_mfma_f32_16x16x32_bf16 v[208:211], a[184:187], a[192:195], 0\n"
"buffer_load_dwordx4 v9, s[64:67], s82 offen offset:1024 sc1 lds\n"
"v_mul_f32 v45, v45, v17\n"
"v_mfma_f32_16x16x32_bf16 v[208:211], a[188:191], a[208:211], v[208:211]\n"
"ds_read_b128 a[0:3], v104 offset:32768\n"
"ds_read_b64_tr_b16 a[172:173], v2 offset:3072\n"
"v_mul_f32 v55, v55, v87\n"
"v_mfma_f32_16x16x32_bf16 v[212:215], a[184:187], a[196:199], 0\n"
"ds_read_b128 a[16:19], v104 offset:34816\n"
"ds_read_b64_tr_b16 a[174:175], v2 offset:11264\n"
"v_mfma_f32_16x16x32_bf16 v[212:215], a[188:191], a[212:215], v[212:215]\n"
"s_addk_i32 s82, 3072\n"
"ds_read_b128 a[4:7], v104 offset:36864\n"
"ds_read_b128 a[8:11], v104 offset:40960\n"
"ds_read_b128 a[12:15], v104 offset:45056\n"
"ds_read_b128 a[20:23], v104 offset:38912\n"
"ds_read_b128 a[24:27], v104 offset:43008\n"
"ds_read_b128 a[28:31], v104 offset:47104\n"
"v_rcp_f32 v18, v18\n"
"v_cvt_pk_bf16_f32 v68, v36, v37\n"
"v_mfma_f32_16x16x32_bf16 v[216:219], a[184:187], a[200:203], 0\n"
"buffer_load_dwordx4 v9, s[64:67], s82 offen offset:2048 sc1 lds\n"
"v_cvt_pk_bf16_f32 v69, v38, v39\n"
"v_mfma_f32_16x16x32_bf16 v[216:219], a[188:191], a[216:219], v[216:219]\n"
"v_add_f32 v19, 1.0, v19\n"
"v_cvt_pk_bf16_f32 v70, v52, v53\n"
"v_cvt_pk_bf16_f32 v71, v54, v55\n"
"v_mfma_f32_16x16x32_bf16 v[220:223], a[184:187], a[204:207], 0\n"
"s_add_u32 s82, s4, 74752\n"
"v_exp_f32 v16, v60\n"
"v_mfma_f32_16x16x32_bf16 v[220:223], a[188:191], a[220:223], v[220:223]\n"
"s_waitcnt lgkmcnt(6)\n"
"buffer_load_dwordx4 v10, s[56:59], s82 offen offset:3072 lds\n"
"v_mfma_f32_16x16x32_bf16 v[224:227], a[160:163], a[192:195], 0\n"
"s_waitcnt vmcnt(14) lgkmcnt(0)\n"
"s_barrier\n"
"ds_read_b64_tr_b16 a[176:177], v96 offset:49152\n"
"v_mul_f32 v46, v46, v18\n"
"v_mfma_f32_16x16x32_bf16 v[224:227], a[164:167], a[208:211], v[224:227]\n"
"ds_read_b64_tr_b16 a[178:179], v96 offset:53248\n"
"ds_write_b128 v5, v[68:71] offset:50176\n"
"v_mfma_f32_16x16x32_bf16 v[228:231], a[160:163], a[196:199], 0\n"
"v_rcp_f32 v19, v19\n"
"v_mfma_f32_16x16x32_bf16 v[228:231], a[164:167], a[212:215], v[228:231]\n"
"s_addk_i32 m0, -16384\n"
"ds_read_b64_tr_b16 a[180:181], v96 offset:57344\n"
"v_add_f32 v16, 1.0, v16\n"
"v_mul_f32 v40, v40, v72\n"
"v_mfma_f32_16x16x32_bf16 v[232:235], a[160:163], a[200:203], 0\n"
"s_add_u32 s82, s40, 151552\n"
"ds_read_b64_tr_b16 a[182:183], v96 offset:61440\n"
"v_exp_f32 v17, v61\n"
"v_mfma_f32_16x16x32_bf16 v[232:235], a[164:167], a[216:219], v[232:235]\n"
"buffer_load_dwordx4 v12, s[56:59], s82 offen offset:0 lds\n"
"v_mul_f32 v41, v41, v73\n"
"v_mfma_f32_16x16x32_bf16 v[236:239], a[160:163], a[204:207], 0\n"
"buffer_store_dwordx4 v[68:71], v5, s[64:67], s18 offen offset:1024 sc1\n"
"v_mul_f32 v42, v42, v74\n"
"v_mfma_f32_16x16x32_bf16 v[236:239], a[164:167], a[220:223], v[236:239]\n"
"v_mul_f32 v47, v47, v19\n"
"v_mul_f32 v43, v43, v75\n"
"v_mfma_f32_16x16x32_bf16 v[240:243], a[168:171], a[192:195], 0\n"
"v_rcp_f32 v16, v16\n"
"v_mul_f32 v56, v56, v88\n"
"v_mfma_f32_16x16x32_bf16 v[240:243], a[172:175], a[208:211], v[240:243]\n"
"v_add_f32 v17, 1.0, v17\n"
"v_mul_f32 v57, v57, v89\n"
"v_mfma_f32_16x16x32_bf16 v[244:247], a[168:171], a[196:199], 0\n"
"s_add_u32 s82, s40, 187392\n"
"v_mul_f32 v58, v58, v90\n"
"v_mul_f32 v59, v59, v91\n"
"v_mfma_f32_16x16x32_bf16 v[244:247], a[172:175], a[212:215], v[244:247]\n"
"buffer_load_dwordx4 v12, s[56:59], s82 offen offset:1024 lds\n"
"v_mfma_f32_16x16x32_bf16 v[248:251], a[168:171], a[200:203], 0\n"
"v_exp_f32 v18, v62\n"
"v_mfma_f32_16x16x32_bf16 v[248:251], a[172:175], a[216:219], v[248:251]\n"
"s_add_u32 s82, s40, 149632\n"
"v_mul_f32 v60, v60, v16\n"
"v_cvt_pk_bf16_f32 v72, v40, v41\n"
"v_mfma_f32_16x16x32_bf16 v[252:255], a[168:171], a[204:207], 0\n"
"buffer_load_dwordx4 v12, s[56:59], s82 offen offset:2048 lds\n"
"v_cvt_pk_bf16_f32 v73, v42, v43\n"
"v_mfma_f32_16x16x32_bf16 v[252:255], a[172:175], a[220:223], v[252:255]\n"
"s_waitcnt lgkmcnt(0)\n"
"v_rcp_f32 v17, v17\n"
"v_mfma_f32_16x16x32_bf16 v[112:115], a[176:179], a[192:195], 0\n"
"s_waitcnt vmcnt(14)\n"
"s_barrier\n"
"ds_read_b64_tr_b16 a[160:161], v1 offset:52224\n"
"ds_read_b64_tr_b16 a[162:163], v1 offset:60416\n"
"v_mfma_f32_16x16x32_bf16 v[112:115], a[180:183], a[208:211], v[112:115]\n"
"ds_read_b64_tr_b16 a[164:165], v3 offset:0\n"
"ds_read_b64_tr_b16 a[166:167], v3 offset:8192\n"
"v_mfma_f32_16x16x32_bf16 v[116:119], a[176:179], a[196:199], 0\n"
"ds_read_b64_tr_b16 a[168:169], v2 offset:52224\n"
"ds_read_b64_tr_b16 a[170:171], v2 offset:60416\n"
"buffer_load_dwordx2 v[26:27], v25, s[64:67], s19 offen sc1\n"
"v_mfma_f32_16x16x32_bf16 v[116:119], a[180:183], a[212:215], v[116:119]\n"
"ds_read_b128 a[32:35], v104 offset:50176\n"
"ds_read_b128 a[36:39], v104 offset:54272\n"
"ds_read_b128 a[40:43], v104 offset:58368\n"
"ds_read_b128 a[44:47], v104 offset:62464\n"
"ds_read_b64_tr_b16 a[172:173], v4 offset:0\n"
"ds_read_b64_tr_b16 a[174:175], v4 offset:8192\n"
"v_mfma_f32_16x16x32_bf16 v[120:123], a[176:179], a[200:203], 0\n"
"v_add_f32 v18, 1.0, v18\n"
"v_cvt_pk_bf16_f32 v74, v56, v57\n"
"v_cvt_pk_bf16_f32 v75, v58, v59\n"
"v_mfma_f32_16x16x32_bf16 v[120:123], a[180:183], a[216:219], v[120:123]\n"
"s_add_u32 s82, s40, 185472\n"
"ds_write_b128 v6, v[72:75] offset:49152\n"
"v_mfma_f32_16x16x32_bf16 v[124:127], a[176:179], a[204:207], 0\n"
"buffer_load_dwordx4 v12, s[56:59], s82 offen offset:3072 lds\n"
"v_mul_f32 v61, v61, v17\n"
"v_mfma_f32_16x16x32_bf16 v[124:127], a[180:183], a[220:223], v[124:127]\n"
"s_waitcnt lgkmcnt(1)\n"
"v_exp_f32 v19, v63\n"
"v_mul_f32 v44, v44, v76\n"
"v_mfma_f32_16x16x32_bf16 v[128:131], a[160:163], a[224:227], v[128:131]\n"
"s_waitcnt vmcnt(10)\n"
"s_barrier\n"
"ds_read_b64_tr_b16 a[176:177], v3 offset:3072\n"
"v_mul_f32 v45, v45, v77\n"
"v_mfma_f32_16x16x32_bf16 v[128:131], a[164:167], a[240:243], v[128:131]\n"
"ds_read_b64_tr_b16 a[178:179], v3 offset:11264\n"
"buffer_store_dwordx4 v[72:75], v6, s[64:67], s18 offen offset:0 sc1\n"
"v_mfma_f32_16x16x32_bf16 v[132:135], a[160:163], a[228:231], v[132:135]\n"
"v_rcp_f32 v18, v18\n"
"v_add_f32 v19, 1.0, v19\n"
"v_mfma_f32_16x16x32_bf16 v[132:135], a[164:167], a[244:247], v[132:135]\n"
"s_add_u32 m0, s77, 32768\n"
"ds_read_b64_tr_b16 a[180:181], v3 offset:49152\n"
"v_mul_f32 v46, v46, v78\n"
"v_mfma_f32_16x16x32_bf16 v[136:139], a[160:163], a[232:235], v[136:139]\n"
"s_add_u32 s82, s40, 151808\n"
"ds_read_b64_tr_b16 a[182:183], v3 offset:57344\n"
"v_mul_f32 v62, v62, v18\n"
"v_mfma_f32_16x16x32_bf16 v[136:139], a[164:167], a[248:251], v[136:139]\n"
"buffer_load_dwordx4 v12, s[56:59], s82 offen offset:0 lds\n"
"v_mul_f32 v47, v47, v79\n"
"v_mfma_f32_16x16x32_bf16 v[140:143], a[160:163], a[236:239], v[140:143]\n"
"ds_read_b64_tr_b16 a[184:185], v4 offset:3072\n"
"v_rcp_f32 v19, v19\n"
"v_mfma_f32_16x16x32_bf16 v[140:143], a[164:167], a[252:255], v[140:143]\n"
"ds_read_b64_tr_b16 a[186:187], v4 offset:11264\n"
"v_mul_f32 v60, v60, v92\n"
"v_mul_f32 v61, v61, v93\n"
"v_mfma_f32_16x16x32_bf16 v[144:147], a[168:171], a[224:227], v[144:147]\n"
"v_mul_f32 v63, v63, v19\n"
"v_mul_f32 v62, v62, v94\n"
"v_mul_f32 v63, v63, v95\n"
"v_mfma_f32_16x16x32_bf16 v[144:147], a[172:175], a[240:243], v[144:147]\n"
"ds_read_b64_tr_b16 a[188:189], v4 offset:49152\n"
"v_cvt_pk_bf16_f32 v76, v44, v45\n"
"v_mfma_f32_16x16x32_bf16 v[148:151], a[168:171], a[228:231], v[148:151]\n"
"s_add_u32 s82, s40, 187648\n"
"ds_read_b64_tr_b16 a[190:191], v4 offset:57344\n"
"v_cvt_pk_bf16_f32 v77, v46, v47\n"
"v_mfma_f32_16x16x32_bf16 v[148:151], a[172:175], a[244:247], v[148:151]\n"
"buffer_load_dwordx4 v12, s[56:59], s82 offen offset:1024 lds\n"
"v_mfma_f32_16x16x32_bf16 v[152:155], a[168:171], a[232:235], v[152:155]\n"
"v_cvt_pk_bf16_f32 v78, v60, v61\n"
"v_cvt_pk_bf16_f32 v79, v62, v63\n"
"v_mfma_f32_16x16x32_bf16 v[152:155], a[172:175], a[248:251], v[152:155]\n"
"s_add_u32 s82, s40, 149888\n"
"ds_write_b128 v6, v[76:79] offset:50176\n"
"v_mfma_f32_16x16x32_bf16 v[156:159], a[168:171], a[236:239], v[156:159]\n"
"buffer_load_dwordx4 v12, s[56:59], s82 offen offset:2048 lds\n"
"v_mfma_f32_16x16x32_bf16 v[156:159], a[172:175], a[252:255], v[156:159]\n"
"s_waitcnt lgkmcnt(1)\n"
"ds_read_b128 v[32:35], v7 offset:0\n"
"ds_read_b128 v[36:39], v7 offset:1024\n"
"v_mfma_f32_16x16x32_bf16 v[160:163], a[176:179], a[224:227], v[160:163]\n"
"ds_read_b64_tr_b16 a[160:161], v3 offset:50176\n"
"buffer_store_dwordx4 v[76:79], v6, s[64:67], s18 offen offset:1024 sc1\n"
"v_mfma_f32_16x16x32_bf16 v[160:163], a[180:183], a[240:243], v[160:163]\n"
"ds_read_b128 v[40:43], v7 offset:2048\n"
"ds_read_b64_tr_b16 a[162:163], v3 offset:58368\n"
"v_mfma_f32_16x16x32_bf16 v[164:167], a[176:179], a[228:231], v[164:167]\n"
"ds_read_b128 v[44:47], v7 offset:3072\n"
"ds_read_b128 v[48:51], v7 offset:8192\n"
"v_mfma_f32_16x16x32_bf16 v[164:167], a[180:183], a[244:247], v[164:167]\n"
"ds_read_b128 a[48:51], v105 offset:1024\n"
"ds_read_b64_tr_b16 a[164:165], v3 offset:51200\n"
"v_mfma_f32_16x16x32_bf16 v[168:171], a[176:179], a[232:235], v[168:171]\n"
"s_add_u32 s82, s40, 185728\n"
"ds_read_b64_tr_b16 a[166:167], v3 offset:59392\n"
"v_mfma_f32_16x16x32_bf16 v[168:171], a[180:183], a[248:251], v[168:171]\n"
"buffer_load_dwordx4 v12, s[56:59], s82 offen offset:3072 lds\n"
"v_mfma_f32_16x16x32_bf16 v[172:175], a[176:179], a[236:239], v[172:175]\n"
"ds_read_b128 v[52:55], v7 offset:9216\n"
"ds_read_b64_tr_b16 a[168:169], v4 offset:50176\n"
"v_mfma_f32_16x16x32_bf16 v[172:175], a[180:183], a[252:255], v[172:175]\n"
"ds_read_b128 a[52:55], v105 offset:5120\n"
"ds_read_b64_tr_b16 a[170:171], v4 offset:58368\n"
"v_mfma_f32_16x16x32_bf16 v[176:179], a[184:187], a[224:227], v[176:179]\n"
"ds_read_b128 v[56:59], v7 offset:10240\n"
"ds_read_b128 a[56:59], v105 offset:9216\n"
"v_mfma_f32_16x16x32_bf16 v[176:179], a[188:191], a[240:243], v[176:179]\n"
"s_addk_i32 m0, 16384\n"
"ds_read_b128 v[60:63], v7 offset:11264\n"
"ds_read_b64_tr_b16 a[172:173], v4 offset:51200\n"
"v_mfma_f32_16x16x32_bf16 v[180:183], a[184:187], a[228:231], v[180:183]\n"
"s_add_u32 s82, s4, 151552\n"
"ds_read_b64_tr_b16 a[174:175], v4 offset:59392\n"
"v_mfma_f32_16x16x32_bf16 v[180:183], a[188:191], a[244:247], v[180:183]\n"
"ds_read_b128 a[60:63], v105 offset:13312\n"
"buffer_load_dwordx4 v10, s[56:59], s82 offen offset:0 lds\n"
"v_mfma_f32_16x16x32_bf16 v[184:187], a[184:187], a[232:235], v[184:187]\n"
"ds_read_b128 v[64:67], v7 offset:24576\n"
"ds_read_b128 v[68:71], v7 offset:25600\n"
"v_mfma_f32_16x16x32_bf16 v[184:187], a[188:191], a[248:251], v[184:187]\n"
"s_add_u32 s82, s41, 3072\n"
"ds_read_b128 v[72:75], v7 offset:26624\n"
"v_mfma_f32_16x16x32_bf16 v[188:191], a[184:187], a[236:239], v[188:191]\n"
"buffer_load_dwordx4 v12, s[56:59], s82 offen offset:1024 lds\n"
"v_mfma_f32_16x16x32_bf16 v[188:191], a[188:191], a[252:255], v[188:191]\n"
"s_waitcnt lgkmcnt(4)\n"
"ds_read_b128 v[76:79], v7 offset:27648\n"
"ds_read_b128 v[80:83], v7 offset:16384\n"
"v_mfma_f32_16x16x32_bf16 v[192:195], a[160:163], a[224:227], v[192:195]\n"
"s_waitcnt vmcnt(14) lgkmcnt(5)\n"
"s_barrier\n"
"ds_read_b64_tr_b16 a[176:177], v3 offset:52224\n"
"v_mfma_f32_16x16x32_bf16 v[192:195], a[164:167], a[240:243], v[192:195]\n"
"ds_read_b128 v[84:87], v7 offset:17408\n"
"ds_read_b64_tr_b16 a[178:179], v3 offset:60416\n"
"v_mfma_f32_16x16x32_bf16 v[196:199], a[160:163], a[228:231], v[196:199]\n"
"ds_read_b128 v[88:91], v7 offset:18432\n"
"ds_read_b128 v[92:95], v7 offset:19456\n"
"v_mfma_f32_16x16x32_bf16 v[196:199], a[164:167], a[244:247], v[196:199]\n"
"ds_read_b64_tr_b16 a[180:181], v1 offset:16384\n"
"v_add_u32 v8, 118784, v8\n"
"v_mfma_f32_16x16x32_bf16 v[200:203], a[160:163], a[232:235], v[200:203]\n"
"s_add_u32 s82, s80, 2048\n"
"ds_read_b64_tr_b16 a[182:183], v1 offset:24576\n"
"v_mfma_f32_16x16x32_bf16 v[200:203], a[164:167], a[248:251], v[200:203]\n"
"buffer_load_dwordx4 v13, s[60:63], s82 offen offset:2048 lds\n"
"v_mfma_f32_16x16x32_bf16 v[204:207], a[160:163], a[236:239], v[204:207]\n"
"ds_read_b64 a[192:193], v8 offset:0\n"
"ds_read_b64_tr_b16 a[184:185], v4 offset:52224\n"
"v_mfma_f32_16x16x32_bf16 v[204:207], a[164:167], a[252:255], v[204:207]\n"
"ds_read_b64_tr_b16 a[186:187], v4 offset:60416\n"
"ds_read_b128 a[96:99], v104 offset:17408\n"
"v_mfma_f32_16x16x32_bf16 v[208:211], a[168:171], a[224:227], v[208:211]\n"
"ds_read_b64 a[196:197], v8 offset:512\n"
"ds_read_b128 a[100:103], v104 offset:21504\n"
"v_mfma_f32_16x16x32_bf16 v[208:211], a[172:175], a[240:243], v[208:211]\n"
"ds_read_b64 a[200:201], v8 offset:1024\n"
"ds_read_b64_tr_b16 a[188:189], v2 offset:16384\n"
"v_mfma_f32_16x16x32_bf16 v[212:215], a[168:171], a[228:231], v[212:215]\n"
"s_add_u32 s82, s41, 1152\n"
"ds_read_b64_tr_b16 a[190:191], v2 offset:24576\n"
"v_mfma_f32_16x16x32_bf16 v[212:215], a[172:175], a[244:247], v[212:215]\n"
"buffer_load_dwordx4 v12, s[56:59], s82 offen offset:3072 lds\n"
"v_mfma_f32_16x16x32_bf16 v[216:219], a[168:171], a[232:235], v[216:219]\n"
"s_addk_i32 m0, 16384\n"
"ds_read_b64 a[204:205], v8 offset:1536\n"
"ds_read_b128 a[104:107], v104 offset:25600\n"
"v_mfma_f32_16x16x32_bf16 v[216:219], a[172:175], a[248:251], v[216:219]\n"
"s_add_u32 s82, s80, 4224\n"
"ds_read_b128 a[108:111], v104 offset:29696\n"
"v_mfma_f32_16x16x32_bf16 v[220:223], a[168:171], a[236:239], v[220:223]\n"
"buffer_load_dwordx4 v13, s[60:63], s82 offen offset:0 lds\n"
"v_mfma_f32_16x16x32_bf16 v[220:223], a[172:175], a[252:255], v[220:223]\n"
"s_waitcnt lgkmcnt(3)\n"
"s_waitcnt vmcnt(12)\n"
"v_cmp_ne_u64 vcc, v[26:27], v[22:23]\n"
"s_nop 1\n"
"s_cbranch_vccz L_ok3_%=\n"
"s_mov_b32 s100, 0x4000\n"
"L_rt3_%=:\n"
"s_sleep 2\n"
"buffer_load_dwordx2 v[26:27], v25, s[64:67], s19 offen sc1\n"
"s_waitcnt vmcnt(0)\n"
"v_cmp_ne_u64 vcc, v[26:27], v[22:23]\n"
"s_nop 4\n"
"s_cbranch_vccz L_ok3_%=\n"
"s_sub_u32 s100, s100, 1\n"
"s_cmp_lg_u32 s100, 0\n"
"s_cbranch_scc1 L_rt3_%=\n"
"L_ok3_%=:\n"
"v_mfma_f32_16x16x32_bf16 v[224:227], a[176:179], a[224:227], v[224:227]\n"
"ds_read_b64_tr_b16 a[160:161], v96 offset:19456\n"
"v_mfma_f32_16x16x32_bf16 v[224:227], a[180:183], a[240:243], v[224:227]\n"
"ds_read_b64_tr_b16 a[162:163], v96 offset:23552\n"
"v_mfma_f32_16x16x32_bf16 v[228:231], a[176:179], a[228:231], v[228:231]\n"
"v_mfma_f32_16x16x32_bf16 v[228:231], a[180:183], a[244:247], v[228:231]\n"
"ds_read_b64_tr_b16 a[164:165], v96 offset:27648\n"
"v_mfma_f32_16x16x32_bf16 v[232:235], a[176:179], a[232:235], v[232:235]\n"
"s_add_u32 s82, s8, 34816\n"
"ds_read_b64_tr_b16 a[166:167], v96 offset:31744\n"
"v_mfma_f32_16x16x32_bf16 v[232:235], a[180:183], a[248:251], v[232:235]\n"
"buffer_load_dwordx4 v9, s[64:67], s82 offen offset:1024 sc1 lds\n"
"v_mfma_f32_16x16x32_bf16 v[236:239], a[176:179], a[236:239], v[236:239]\n"
"v_mfma_f32_16x16x32_bf16 v[236:239], a[180:183], a[252:255], v[236:239]\n"
"v_mfma_f32_16x16x32_bf16 v[240:243], a[184:187], a[224:227], v[240:243]\n"
"s_waitcnt vmcnt(7)\n"
"buffer_store_dwordx2 v[20:21], v24, s[64:67], s98 offen sc1\n"
"v_mfma_f32_16x16x32_bf16 v[240:243], a[188:191], a[240:243], v[240:243]\n"
"v_mfma_f32_16x16x32_bf16 v[244:247], a[184:187], a[228:231], v[244:247]\n"
"s_addk_i32 s82, 3072\n"
"v_mfma_f32_16x16x32_bf16 v[244:247], a[188:191], a[244:247], v[244:247]\n"
"buffer_load_dwordx4 v9, s[64:67], s82 offen offset:2048 sc1 lds\n"
"v_mfma_f32_16x16x32_bf16 v[248:251], a[184:187], a[232:235], v[248:251]\n"
"v_mfma_f32_16x16x32_bf16 v[248:251], a[188:191], a[248:251], v[248:251]\n"
"s_add_u32 s82, s41, 1280\n"
"v_mfma_f32_16x16x32_bf16 v[252:255], a[184:187], a[236:239], v[252:255]\n"
"buffer_load_dwordx4 v12, s[56:59], s82 offen offset:3072 lds\n"
"v_mfma_f32_16x16x32_bf16 v[252:255], a[188:191], a[252:255], v[252:255]\n"
"s_waitcnt lgkmcnt(0)\n"
"v_mfma_f32_16x16x32_bf16 v[112:115], a[160:163], a[224:227], v[112:115]\n"
"s_barrier\n"
"ds_read_b64_tr_b16 a[176:177], v1 offset:0\n"
"ds_read_b64_tr_b16 a[178:179], v1 offset:8192\n"
"v_mfma_f32_16x16x32_bf16 v[112:115], a[164:167], a[240:243], v[112:115]\n"
"ds_read_b64_tr_b16 a[180:181], v1 offset:1024\n"
"ds_read_b64_tr_b16 a[182:183], v1 offset:9216\n"
"v_mfma_f32_16x16x32_bf16 v[116:119], a[160:163], a[228:231], v[116:119]\n"
"ds_read_b64_tr_b16 a[184:185], v2 offset:0\n"
"ds_read_b64_tr_b16 a[186:187], v2 offset:8192\n"
"v_mfma_f32_16x16x32_bf16 v[116:119], a[164:167], a[244:247], v[116:119]\n"
"ds_read_b64_tr_b16 a[188:189], v2 offset:1024\n"
"ds_read_b64_tr_b16 a[190:191], v2 offset:9216\n"
"v_mfma_f32_16x16x32_bf16 v[120:123], a[160:163], a[232:235], v[120:123]\n"
"s_addk_i32 m0, 16384\n"
"ds_read_b128 a[224:227], v7 offset:49152\n"
"ds_read_b128 a[228:231], v7 offset:50176\n"
"v_mfma_f32_16x16x32_bf16 v[120:123], a[164:167], a[248:251], v[120:123]\n"
"s_add_u32 s82, s80, 4352\n"
"ds_read_b128 a[232:235], v7 offset:51200\n"
"v_mfma_f32_16x16x32_bf16 v[124:127], a[160:163], a[236:239], v[124:127]\n"
"buffer_load_dwordx4 v13, s[60:63], s82 offen offset:0 lds\n"
"v_mfma_f32_16x16x32_bf16 v[124:127], a[164:167], a[252:255], v[124:127]\n"
"s_waitcnt lgkmcnt(3)\n"
"ds_read_b128 a[236:239], v7 offset:52224\n"
"ds_read_b128 a[240:243], v7 offset:57344\n"
"v_mfma_f32_16x16x32_bf16 v[128:131], a[176:179], v[32:35], v[128:131]\n"
"ds_read_b128 a[244:247], v7 offset:58368\n"
"ds_read_b64_tr_b16 a[160:161], v1 offset:2048\n"
"v_mfma_f32_16x16x32_bf16 v[128:131], a[180:183], v[48:51], v[128:131]\n"
"ds_read_b128 a[248:251], v7 offset:59392\n"
"ds_read_b64_tr_b16 a[162:163], v1 offset:10240\n"
"v_mfma_f32_16x16x32_bf16 v[132:135], a[176:179], v[36:39], v[132:135]\n"
"ds_read_b128 a[252:255], v7 offset:60416\n"
"v_mfma_f32_16x16x32_bf16 v[132:135], a[180:183], v[52:55], v[132:135]\n"
"ds_read_b64_tr_b16 a[164:165], v1 offset:3072\n"
"v_mfma_f32_16x16x32_bf16 v[136:139], a[176:179], v[40:43], v[136:139]\n"
"s_add_u32 s82, s8, 43008\n"
"ds_read_b64_tr_b16 a[166:167], v1 offset:11264\n"
"v_mfma_f32_16x16x32_bf16 v[136:139], a[180:183], v[56:59], v[136:139]\n"
"buffer_load_dwordx4 v9, s[64:67], s82 offen offset:1024 sc1 lds\n"
"v_mfma_f32_16x16x32_bf16 v[140:143], a[176:179], v[44:47], v[140:143]\n"
"ds_read_b64_tr_b16 a[168:169], v2 offset:2048\n"
"v_mfma_f32_16x16x32_bf16 v[140:143], a[180:183], v[60:63], v[140:143]\n"
"ds_read_b64_tr_b16 a[170:171], v2 offset:10240\n"
"v_mfma_f32_16x16x32_bf16 v[144:147], a[184:187], v[32:35], v[144:147]\n"
"v_mfma_f32_16x16x32_bf16 v[144:147], a[188:191], v[48:51], v[144:147]\n"
"ds_read_b64_tr_b16 a[172:173], v2 offset:3072\n"
"v_mfma_f32_16x16x32_bf16 v[148:151], a[184:187], v[36:39], v[148:151]\n"
"s_addk_i32 s82, 3072\n"
"ds_read_b64_tr_b16 a[174:175], v2 offset:11264\n"
"v_mfma_f32_16x16x32_bf16 v[148:151], a[188:191], v[52:55], v[148:151]\n"
"buffer_load_dwordx4 v9, s[64:67], s82 offen offset:2048 sc1 lds\n"
"v_mfma_f32_16x16x32_bf16 v[152:155], a[184:187], v[40:43], v[152:155]\n"
"v_mfma_f32_16x16x32_bf16 v[152:155], a[188:191], v[56:59], v[152:155]\n"
"s_add_u32 s82, s41, 1408\n"
"v_mfma_f32_16x16x32_bf16 v[156:159], a[184:187], v[44:47], v[156:159]\n"
"buffer_load_dwordx4 v12, s[56:59], s82 offen offset:3072 lds\n"
"v_mfma_f32_16x16x32_bf16 v[156:159], a[188:191], v[60:63], v[156:159]\n"
"s_waitcnt lgkmcnt(0)\n"
"v_mfma_f32_16x16x32_bf16 v[160:163], a[160:163], v[32:35], v[160:163]\n"
"s_waitcnt vmcnt(13)\n"
"s_barrier\n"
"ds_read_b64_tr_b16 a[176:177], v1 offset:32768\n"
"v_mfma_f32_16x16x32_bf16 v[160:163], a[164:167], v[48:51], v[160:163]\n"
"ds_read_b64_tr_b16 a[178:179], v1 offset:40960\n"
"v_mfma_f32_16x16x32_bf16 v[164:167], a[160:163], v[36:39], v[164:167]\n"
"v_mfma_f32_16x16x32_bf16 v[164:167], a[164:167], v[52:55], v[164:167]\n"
"s_add_u32 m0, s77, 16384\n"
"ds_read_b64_tr_b16 a[180:181], v1 offset:33792\n"
"v_mfma_f32_16x16x32_bf16 v[168:171], a[160:163], v[40:43], v[168:171]\n"
"s_add_u32 s82, s80, 4480\n"
"ds_read_b64_tr_b16 a[182:183], v1 offset:41984\n"
"v_mfma_f32_16x16x32_bf16 v[168:171], a[164:167], v[56:59], v[168:171]\n"
"buffer_load_dwordx4 v13, s[60:63], s82 offen offset:0 lds\n"
"v_mfma_f32_16x16x32_bf16 v[172:175], a[160:163], v[44:47], v[172:175]\n"
"ds_read_b64_tr_b16 a[184:185], v2 offset:32768\n"
"v_mfma_f32_16x16x32_bf16 v[172:175], a[164:167], v[60:63], v[172:175]\n"
"ds_read_b64_tr_b16 a[186:187], v2 offset:40960\n"
"v_mfma_f32_16x16x32_bf16 v[176:179], a[168:171], v[32:35], v[176:179]\n"
"v_mfma_f32_16x16x32_bf16 v[176:179], a[172:175], v[48:51], v[176:179]\n"
"ds_read_b64_tr_b16 a[188:189], v2 offset:33792\n"
"v_mfma_f32_16x16x32_bf16 v[180:183], a[168:171], v[36:39], v[180:183]\n"
"s_add_u32 s82, s6, 3072\n"
"ds_read_b64_tr_b16 a[190:191], v2 offset:41984\n"
"v_mfma_f32_16x16x32_bf16 v[180:183], a[172:175], v[52:55], v[180:183]\n"
"buffer_load_dwordx4 v10, s[52:55], s82 offen offset:1024 lds\n"
"v_mfma_f32_16x16x32_bf16 v[184:187], a[168:171], v[40:43], v[184:187]\n"
"v_mfma_f32_16x16x32_bf16 v[184:187], a[172:175], v[56:59], v[184:187]\n"
"s_add_u32 s82, s8, 74752\n"
"v_mfma_f32_16x16x32_bf16 v[188:191], a[168:171], v[44:47], v[188:191]\n"
"buffer_load_dwordx4 v9, s[64:67], s82 offen offset:2048 sc1 lds\n"
"v_mfma_f32_16x16x32_bf16 v[188:191], a[172:175], v[60:63], v[188:191]\n"
"s_waitcnt lgkmcnt(0)\n"
"v_mfma_f32_16x16x32_bf16 v[192:195], a[176:179], v[32:35], v[192:195]\n"
"ds_read_b64_tr_b16 a[160:161], v1 offset:34816\n"
"v_mfma_f32_16x16x32_bf16 v[192:195], a[180:183], v[48:51], v[192:195]\n"
"ds_read_b64_tr_b16 a[162:163], v1 offset:43008\n"
"v_mfma_f32_16x16x32_bf16 v[196:199], a[176:179], v[36:39], v[196:199]\n"
"v_mfma_f32_16x16x32_bf16 v[196:199], a[180:183], v[52:55], v[196:199]\n"
"ds_read_b64_tr_b16 a[164:165], v1 offset:35840\n"
"v_mfma_f32_16x16x32_bf16 v[200:203], a[176:179], v[40:43], v[200:203]\n"
"s_addk_i32 s82, 3072\n"
"ds_read_b64_tr_b16 a[166:167], v1 offset:44032\n"
"v_mfma_f32_16x16x32_bf16 v[200:203], a[180:183], v[56:59], v[200:203]\n"
"buffer_load_dwordx4 v9, s[64:67], s82 offen offset:3072 sc1 lds\n"
"v_mfma_f32_16x16x32_bf16 v[204:207], a[176:179], v[44:47], v[204:207]\n"
"ds_read_b64_tr_b16 a[168:169], v2 offset:34816\n"
"v_mfma_f32_16x16x32_bf16 v[204:207], a[180:183], v[60:63], v[204:207]\n"
"ds_read_b64_tr_b16 a[170:171], v2 offset:43008\n"
"v_mfma_f32_16x16x32_bf16 v[208:211], a[184:187], v[32:35], v[208:211]\n"
"v_mfma_f32_16x16x32_bf16 v[208:211], a[188:191], v[48:51], v[208:211]\n"
"s_add_u32 m0, s77, 98304\n"
"ds_read_b64_tr_b16 a[172:173], v2 offset:35840\n"
"v_mfma_f32_16x16x32_bf16 v[212:215], a[184:187], v[36:39], v[212:215]\n"
"s_addk_i32 s82, 7168\n"
"ds_read_b64_tr_b16 a[174:175], v2 offset:44032\n"
"v_mfma_f32_16x16x32_bf16 v[212:215], a[188:191], v[52:55], v[212:215]\n"
"buffer_load_dwordx4 v9, s[64:67], s82 offen offset:0 sc1 lds\n"
"v_mfma_f32_16x16x32_bf16 v[216:219], a[184:187], v[40:43], v[216:219]\n"
"v_mfma_f32_16x16x32_bf16 v[216:219], a[188:191], v[56:59], v[216:219]\n"
"s_add_u32 s82, s40, 224256\n"
"v_mfma_f32_16x16x32_bf16 v[220:223], a[184:187], v[44:47], v[220:223]\n"
"buffer_load_dwordx4 v12, s[56:59], s82 offen offset:1024 lds\n"
"v_mfma_f32_16x16x32_bf16 v[220:223], a[188:191], v[60:63], v[220:223]\n"
"s_waitcnt lgkmcnt(0)\n"
"v_mfma_f32_16x16x32_bf16 v[224:227], a[160:163], v[32:35], v[224:227]\n"
"s_waitcnt vmcnt(15)\n"
"s_barrier\n"
"ds_read_b64_tr_b16 a[176:177], v96 offset:49152\n"
"v_mfma_f32_16x16x32_bf16 v[224:227], a[164:167], v[48:51], v[224:227]\n"
"ds_read_b64_tr_b16 a[178:179], v96 offset:53248\n"
"v_mfma_f32_16x16x32_bf16 v[228:231], a[160:163], v[36:39], v[228:231]\n"
"v_mfma_f32_16x16x32_bf16 v[228:231], a[164:167], v[52:55], v[228:231]\n"
"ds_read_b64_tr_b16 a[180:181], v96 offset:57344\n"
"v_mfma_f32_16x16x32_bf16 v[232:235], a[160:163], v[40:43], v[232:235]\n"
"s_add_u32 s82, s40, 260096\n"
"ds_read_b64_tr_b16 a[182:183], v96 offset:61440\n"
"v_mfma_f32_16x16x32_bf16 v[232:235], a[164:167], v[56:59], v[232:235]\n"
"buffer_load_dwordx4 v12, s[56:59], s82 offen offset:2048 lds\n"
"v_mfma_f32_16x16x32_bf16 v[236:239], a[160:163], v[44:47], v[236:239]\n"
"v_mfma_f32_16x16x32_bf16 v[236:239], a[164:167], v[60:63], v[236:239]\n"
"v_mfma_f32_16x16x32_bf16 v[240:243], a[168:171], v[32:35], v[240:243]\n"
"v_mfma_f32_16x16x32_bf16 v[240:243], a[172:175], v[48:51], v[240:243]\n"
"v_mfma_f32_16x16x32_bf16 v[244:247], a[168:171], v[36:39], v[244:247]\n"
"s_add_u32 s82, s40, 222336\n"
"v_mfma_f32_16x16x32_bf16 v[244:247], a[172:175], v[52:55], v[244:247]\n"
"buffer_load_dwordx4 v12, s[56:59], s82 offen offset:3072 lds\n"
"v_mfma_f32_16x16x32_bf16 v[248:251], a[168:171], v[40:43], v[248:251]\n"
"s_addk_i32 m0, 16384\n"
"v_mfma_f32_16x16x32_bf16 v[248:251], a[172:175], v[56:59], v[248:251]\n"
"s_add_u32 s82, s40, 262272\n"
"v_mfma_f32_16x16x32_bf16 v[252:255], a[168:171], v[44:47], v[252:255]\n"
"buffer_load_dwordx4 v12, s[56:59], s82 offen offset:0 lds\n"
"v_mfma_f32_16x16x32_bf16 v[252:255], a[172:175], v[60:63], v[252:255]\n"
"s_waitcnt lgkmcnt(0)\n"
"v_mfma_f32_16x16x32_bf16 v[112:115], a[176:179], v[32:35], v[112:115]\n"
"ds_read_b64_tr_b16 a[160:161], v1 offset:50176\n"
"ds_read_b64_tr_b16 a[162:163], v1 offset:58368\n"
"v_mfma_f32_16x16x32_bf16 v[112:115], a[180:183], v[48:51], v[112:115]\n"
"ds_read_b64_tr_b16 a[164:165], v1 offset:51200\n"
"ds_read_b64_tr_b16 a[166:167], v1 offset:59392\n"
"v_mfma_f32_16x16x32_bf16 v[116:119], a[176:179], v[36:39], v[116:119]\n"
"ds_read_b64_tr_b16 a[168:169], v2 offset:50176\n"
"ds_read_b64_tr_b16 a[170:171], v2 offset:58368\n"
"v_mfma_f32_16x16x32_bf16 v[116:119], a[180:183], v[52:55], v[116:119]\n"
"ds_read_b64_tr_b16 a[172:173], v2 offset:51200\n"
"ds_read_b64_tr_b16 a[174:175], v2 offset:59392\n"
"v_mfma_f32_16x16x32_bf16 v[120:123], a[176:179], v[40:43], v[120:123]\n"
"v_mfma_f32_16x16x32_bf16 v[120:123], a[180:183], v[56:59], v[120:123]\n"
"s_add_u32 s82, s40, 224512\n"
"v_mfma_f32_16x16x32_bf16 v[124:127], a[176:179], v[44:47], v[124:127]\n"
"buffer_load_dwordx4 v12, s[56:59], s82 offen offset:1024 lds\n"
"v_mfma_f32_16x16x32_bf16 v[124:127], a[180:183], v[60:63], v[124:127]\n"
"s_waitcnt lgkmcnt(0)\n"
"v_mfma_f32_16x16x32_bf16 v[128:131], a[160:163], v[64:67], v[128:131]\n"
"s_waitcnt vmcnt(14)\n"
"s_barrier\n"
"ds_read_b64_tr_b16 a[176:177], v1 offset:52224\n"
"v_mfma_f32_16x16x32_bf16 v[128:131], a[164:167], v[80:83], v[128:131]\n"
"ds_read_b64_tr_b16 a[178:179], v1 offset:60416\n"
"ds_read_b128 a[64:67], v105 offset:1024\n"
"v_mfma_f32_16x16x32_bf16 v[132:135], a[160:163], v[68:71], v[132:135]\n"
"s_lshl_b32 s96, s73, 3\n"
"ds_read_b128 a[68:71], v105 offset:5120\n"
"ds_read_b128 a[72:75], v105 offset:9216\n"
"v_mfma_f32_16x16x32_bf16 v[132:135], a[164:167], v[84:87], v[132:135]\n"
"s_add_u32 s96, s96, s74\n"
"ds_read_b64_tr_b16 a[180:181], v3 offset:0\n"
"ds_read_b128 a[76:79], v105 offset:13312\n"
"v_mfma_f32_16x16x32_bf16 v[136:139], a[160:163], v[72:75], v[136:139]\n"
"s_add_u32 s82, s40, 260352\n"
"ds_read_b64_tr_b16 a[182:183], v3 offset:8192\n"
"v_lshrrev_b32 v53, 8, v9\n"
"v_mfma_f32_16x16x32_bf16 v[136:139], a[164:167], v[88:91], v[136:139]\n"
"buffer_load_dwordx4 v12, s[56:59], s82 offen offset:2048 lds\n"
"v_mfma_f32_16x16x32_bf16 v[140:143], a[160:163], v[76:79], v[140:143]\n"
"s_lshl_b32 s97, s96, 10\n"
"ds_read_b64_tr_b16 a[184:185], v2 offset:52224\n"
"v_and_b32 v52, 0xf0, v9\n"
"v_mfma_f32_16x16x32_bf16 v[140:143], a[164:167], v[92:95], v[140:143]\n"
"s_and_b32 s98, s75, 1\n"
"ds_read_b64_tr_b16 a[186:187], v2 offset:60416\n"
"v_add_u32 v46, s97, v9\n"
"v_mfma_f32_16x16x32_bf16 v[144:147], a[168:171], v[64:67], v[144:147]\n"
"s_lshl_b32 s96, s98, 4\n"
"v_lshrrev_b32 v47, 1, v46\n"
"v_mul_u32_u24 v55, 2752512, v53\n"
"v_mfma_f32_16x16x32_bf16 v[144:147], a[172:175], v[80:83], v[144:147]\n"
"s_add_u32 s96, s96, s76\n"
"ds_read_b64_tr_b16 a[188:189], v4 offset:0\n"
"v_add_u32 v48, v55, v52\n"
"v_mfma_f32_16x16x32_bf16 v[148:151], a[168:171], v[68:71], v[148:151]\n"
"s_lshr_b32 s97, s75, 1\n"
"s_add_u32 s82, s40, 222592\n"
"ds_read_b64_tr_b16 a[190:191], v4 offset:8192\n"
"v_mfma_f32_16x16x32_bf16 v[148:151], a[172:175], v[84:87], v[148:151]\n"
"buffer_load_dwordx4 v12, s[56:59], s82 offen offset:3072 lds\n"
"v_mfma_f32_16x16x32_bf16 v[152:155], a[168:171], v[72:75], v[152:155]\n"
"s_mul_i32 s96, s96, 86016\n"
"s_lshl_b32 s97, s97, 2\n"
"s_add_u32 m0, s77, 0\n"
"v_mfma_f32_16x16x32_bf16 v[152:155], a[172:175], v[88:91], v[152:155]\n"
"s_add_u32 s97, s97, s72\n"
"s_lshl_b32 s13, s97, 9\n"
"s_add_u32 s82, s40, 262528\n"
"v_mfma_f32_16x16x32_bf16 v[156:159], a[168:171], v[76:79], v[156:159]\n"
"buffer_load_dwordx4 v12, s[56:59], s82 offen offset:0 lds\n"
"v_mfma_f32_16x16x32_bf16 v[156:159], a[172:175], v[92:95], v[156:159]\n"
"s_waitcnt lgkmcnt(0)\n"
"s_add_u32 s13, s13, s96\n"
"s_add_u32 s13, s13, 4194304\n"
"v_mfma_f32_16x16x32_bf16 v[160:163], a[176:179], v[64:67], v[160:163]\n"
"s_waitcnt vmcnt(13)\n"
"s_barrier\n"
"ds_read_b64_tr_b16 a[160:161], v3 offset:3072\n"
"v_mfma_f32_16x16x32_bf16 v[160:163], a[180:183], v[80:83], v[160:163]\n"
"s_lshl_b32 s18, s97, 8\n"
"ds_read_b64_tr_b16 a[162:163], v3 offset:11264\n"
"ds_read_b128 a[80:83], v105 offset:17408\n"
"v_mfma_f32_16x16x32_bf16 v[164:167], a[176:179], v[68:71], v[164:167]\n"
"s_add_u32 s18, s18, s96\n"
"ds_read_b128 a[84:87], v105 offset:21504\n"
"ds_read_b128 a[88:91], v105 offset:25600\n"
"v_mfma_f32_16x16x32_bf16 v[164:167], a[180:183], v[84:87], v[164:167]\n"
"s_lshl_b32 s98, s98, 3\n"
"ds_read_b64_tr_b16 a[164:165], v3 offset:16384\n"
"ds_read_b128 a[92:95], v105 offset:29696\n"
"v_mfma_f32_16x16x32_bf16 v[168:171], a[176:179], v[72:75], v[168:171]\n"
"s_lshl_b32 s96, s76, 7\n"
"s_add_u32 s82, s4, 224256\n"
"ds_read_b64_tr_b16 a[166:167], v3 offset:24576\n"
"v_mfma_f32_16x16x32_bf16 v[168:171], a[180:183], v[88:91], v[168:171]\n"
"buffer_load_dwordx4 v10, s[56:59], s82 offen offset:1024 lds\n"
"v_mfma_f32_16x16x32_bf16 v[172:175], a[176:179], v[76:79], v[172:175]\n"
"s_add_u32 s18, s18, 4259840\n"
"s_lshl_b32 s97, s72, 7\n"
"ds_read_b64_tr_b16 a[168:169], v4 offset:3072\n"
"v_mfma_f32_16x16x32_bf16 v[172:175], a[180:183], v[92:95], v[172:175]\n"
"ds_read_b64_tr_b16 a[170:171], v4 offset:11264\n"
"v_and_b32 v49, 0x70, v9\n"
"v_mfma_f32_16x16x32_bf16 v[176:179], a[184:187], v[64:67], v[176:179]\n"
"v_add_u32 v49, s98, v49\n"
"v_mfma_f32_16x16x32_bf16 v[176:179], a[188:191], v[80:83], v[176:179]\n"
"ds_read_b64_tr_b16 a[172:173], v4 offset:16384\n"
"v_mfma_f32_16x16x32_bf16 v[180:183], a[184:187], v[68:71], v[180:183]\n"
"s_add_u32 s82, s42, 2048\n"
"ds_read_b64_tr_b16 a[174:175], v4 offset:24576\n"
"v_mfma_f32_16x16x32_bf16 v[180:183], a[188:191], v[84:87], v[180:183]\n"
"buffer_load_dwordx4 v12, s[56:59], s82 offen offset:2048 lds\n"
"v_mfma_f32_16x16x32_bf16 v[184:187], a[184:187], v[72:75], v[184:187]\n"
"v_mfma_f32_16x16x32_bf16 v[184:187], a[188:191], v[88:91], v[184:187]\n"
"s_add_u32 s82, s42, 37888\n"
"v_mfma_f32_16x16x32_bf16 v[188:191], a[184:187], v[76:79], v[188:191]\n"
"buffer_load_dwordx4 v12, s[56:59], s82 offen offset:3072 lds\n"
"v_mfma_f32_16x16x32_bf16 v[188:191], a[188:191], v[92:95], v[188:191]\n"
"s_waitcnt lgkmcnt(0)\n"
"v_mfma_f32_16x16x32_bf16 v[192:195], a[160:163], v[64:67], v[192:195]\n"
"s_add_u32 s19, s19, 32\n"
"s_waitcnt vmcnt(12)\n"
"s_barrier\n"
"ds_read_b64_tr_b16 a[176:177], v3 offset:19456\n"
"v_mfma_f32_16x16x32_bf16 v[192:195], a[164:167], v[80:83], v[192:195]\n"
"ds_read_b64_tr_b16 a[178:179], v3 offset:27648\n"
"ds_read_b128 a[144:147], v104 offset:18432\n"
"v_mfma_f32_16x16x32_bf16 v[196:199], a[160:163], v[68:71], v[196:199]\n"
"buffer_load_dwordx2 v[26:27], v25, s[64:67], s19 offen sc1\n"
"v_mfma_f32_16x16x32_bf16 v[196:199], a[164:167], v[84:87], v[196:199]\n"
"s_add_u32 m0, s77, 32768\n"
"ds_read_b64_tr_b16 a[180:181], v1 offset:16384\n"
"v_mfma_f32_16x16x32_bf16 v[200:203], a[160:163], v[72:75], v[200:203]\n"
"s_add_u32 s82, s42, 77824\n"
"ds_read_b64_tr_b16 a[182:183], v1 offset:24576\n"
"v_mfma_f32_16x16x32_bf16 v[200:203], a[164:167], v[88:91], v[200:203]\n"
"buffer_load_dwordx4 v12, s[56:59], s82 offen offset:0 lds\n"
"v_mfma_f32_16x16x32_bf16 v[204:207], a[160:163], v[76:79], v[204:207]\n"
"ds_read_b64_tr_b16 a[184:185], v4 offset:19456\n"
"ds_read_b128 a[148:151], v104 offset:22528\n"
"v_mfma_f32_16x16x32_bf16 v[204:207], a[164:167], v[92:95], v[204:207]\n"
"ds_read_b64_tr_b16 a[186:187], v4 offset:27648\n"
"ds_read_b128 a[152:155], v104 offset:26624\n"
"v_mfma_f32_16x16x32_bf16 v[208:211], a[168:171], v[64:67], v[208:211]\n"
"ds_read_b128 a[156:159], v104 offset:30720\n"
"v_mfma_f32_16x16x32_bf16 v[208:211], a[172:175], v[80:83], v[208:211]\n"
"ds_read_b64_tr_b16 a[188:189], v2 offset:16384\n"
"v_mfma_f32_16x16x32_bf16 v[212:215], a[168:171], v[68:71], v[212:215]\n"
"s_add_u32 s82, s42, 113664\n"
"ds_read_b64_tr_b16 a[190:191], v2 offset:24576\n"
"v_mfma_f32_16x16x32_bf16 v[212:215], a[172:175], v[84:87], v[212:215]\n"
"buffer_load_dwordx4 v12, s[56:59], s82 offen offset:1024 lds\n"
"v_mfma_f32_16x16x32_bf16 v[216:219], a[168:171], v[72:75], v[216:219]\n"
"s_addk_i32 m0, 16384\n"
"v_mfma_f32_16x16x32_bf16 v[216:219], a[172:175], v[88:91], v[216:219]\n"
"s_add_u32 s82, s42, 149504\n"
"v_mfma_f32_16x16x32_bf16 v[220:223], a[168:171], v[76:79], v[220:223]\n"
"buffer_load_dwordx4 v12, s[56:59], s82 offen offset:2048 lds\n"
"v_mfma_f32_16x16x32_bf16 v[220:223], a[172:175], v[92:95], v[220:223]\n"
"s_waitcnt lgkmcnt(0)\n"
"v_mfma_f32_16x16x32_bf16 v[224:227], a[176:179], v[64:67], v[224:227]\n"
"ds_read_b64_tr_b16 a[160:161], v96 offset:17408\n"
"v_mfma_f32_16x16x32_bf16 v[224:227], a[180:183], v[80:83], v[224:227]\n"
"ds_read_b64_tr_b16 a[162:163], v96 offset:21504\n"
"v_mfma_f32_16x16x32_bf16 v[228:231], a[176:179], v[68:71], v[228:231]\n"
"buffer_load_dwordx2 v[40:41], v49, s[64:67], s96 offen sc1\n"
"v_mfma_f32_16x16x32_bf16 v[228:231], a[180:183], v[84:87], v[228:231]\n"
"s_mul_i32 s96, s76, 2560\n"
"ds_read_b64_tr_b16 a[164:165], v96 offset:25600\n"
"v_mfma_f32_16x16x32_bf16 v[232:235], a[176:179], v[72:75], v[232:235]\n"
"s_add_u32 s82, s42, 185344\n"
"ds_read_b64_tr_b16 a[166:167], v96 offset:29696\n"
"v_mfma_f32_16x16x32_bf16 v[232:235], a[180:183], v[88:91], v[232:235]\n"
"buffer_load_dwordx4 v12, s[56:59], s82 offen offset:3072 lds\n"
"v_mfma_f32_16x16x32_bf16 v[236:239], a[176:179], v[76:79], v[236:239]\n"
"s_add_u32 s96, s96, 24576\n"
"s_add_u32 s88, s96, s97\n"
"s_lshl_b32 s97, s75, 3\n"
"v_mfma_f32_16x16x32_bf16 v[236:239], a[180:183], v[92:95], v[236:239]\n"
"s_add_u32 s88, s88, s97\n"
"s_lshr_b32 s97, s75, 3\n"
"s_lshl_b32 s97, s97, 1\n"
"v_mfma_f32_16x16x32_bf16 v[240:243], a[184:187], v[64:67], v[240:243]\n"
"s_add_u32 s97, s97, s73\n"
"s_lshl_b32 s97, s97, 7\n"
"s_add_u32 s15, s96, s97\n"
"v_mfma_f32_16x16x32_bf16 v[240:243], a[188:191], v[80:83], v[240:243]\n"
"s_lshr_b32 s96, s75, 1\n"
"s_lshl_b32 s96, s96, 2\n"
"s_addk_i32 m0, 16384\n"
"v_mfma_f32_16x16x32_bf16 v[244:247], a[184:187], v[68:71], v[244:247]\n"
"s_add_u32 s96, s96, s72\n"
"s_mul_i32 s96, s96, 4608\n"
"s_add_u32 s82, s43, 4096\n"
"v_mfma_f32_16x16x32_bf16 v[244:247], a[188:191], v[84:87], v[244:247]\n"
"buffer_load_dwordx4 v12, s[56:59], s82 offen offset:0 lds\n"
"v_mfma_f32_16x16x32_bf16 v[248:251], a[184:187], v[72:75], v[248:251]\n"
"s_and_b32 s97, s75, 1\n"
"s_mul_i32 s97, s97, 576\n"
"s_add_u32 s96, s96, s97\n"
"v_mfma_f32_16x16x32_bf16 v[248:251], a[188:191], v[88:91], v[248:251]\n"
"s_add_u32 s82, s81, 3072\n"
"v_mul_u32_u24 v54, 1152, v53\n"
"v_lshrrev_b32 v55, 1, v52\n"
"v_mfma_f32_16x16x32_bf16 v[252:255], a[184:187], v[76:79], v[252:255]\n"
"buffer_load_dwordx4 v13, s[60:63], s82 offen offset:1024 lds\n"
"v_mfma_f32_16x16x32_bf16 v[252:255], a[188:191], v[92:95], v[252:255]\n"
"s_waitcnt lgkmcnt(0)\n"
"v_add3_u32 v54, v54, v55, s96\n"
"v_add_u32 v50, s78, v54\n"
"v_mfma_f32_16x16x32_bf16 v[112:115], a[160:163], v[64:67], v[112:115]\n"
"s_waitcnt vmcnt(16)\n"
"s_barrier\n"
"ds_read_b64_tr_b16 a[176:177], v3 offset:33792\n"
"ds_read_b64_tr_b16 a[178:179], v3 offset:41984\n"
"v_mfma_f32_16x16x32_bf16 v[112:115], a[164:167], v[80:83], v[112:115]\n"
"s_lshl_b32 s96, s73, 3\n"
"ds_read_b64_tr_b16 a[180:181], v3 offset:34816\n"
"ds_read_b64_tr_b16 a[182:183], v3 offset:43008\n"
"v_mfma_f32_16x16x32_bf16 v[116:119], a[160:163], v[68:71], v[116:119]\n"
"s_lshl_b32 s97, s74, 1\n"
"ds_read_b64_tr_b16 a[184:185], v4 offset:33792\n"
"ds_read_b64_tr_b16 a[186:187], v4 offset:41984\n"
"v_mfma_f32_16x16x32_bf16 v[116:119], a[164:167], v[84:87], v[116:119]\n"
"s_add_u32 s96, s96, s97\n"
"ds_read_b64_tr_b16 a[188:189], v4 offset:34816\n"
"ds_read_b64_tr_b16 a[190:191], v4 offset:43008\n"
"v_mfma_f32_16x16x32_bf16 v[120:123], a[160:163], v[72:75], v[120:123]\n"
"s_lshl_b32 s97, s73, 2\n"
"ds_read_b64 a[194:195], v108 offset:32768\n"
"ds_read_b64 a[198:199], v108 offset:33280\n"
"v_mfma_f32_16x16x32_bf16 v[120:123], a[164:167], v[88:91], v[120:123]\n"
"s_add_u32 s82, s42, 223232\n"
"ds_read_b64 a[202:203], v108 offset:36864\n"
"v_add_u32 v54, s79, v54\n"
"v_mfma_f32_16x16x32_bf16 v[124:127], a[160:163], v[76:79], v[124:127]\n"
"buffer_load_dwordx4 v12, s[56:59], s82 offen offset:2048 lds\n"
"v_mfma_f32_16x16x32_bf16 v[124:127], a[164:167], v[92:95], v[124:127]\n"
"s_waitcnt lgkmcnt(3)\n"
"ds_read_b64 a[206:207], v108 offset:37376\n"
"v_mov_b32 v55, 0x7ffffff0\n"
"v_mfma_f32_16x16x32_bf16 v[128:131], a[176:179], a[224:227], v[128:131]\n"
"s_waitcnt vmcnt(13)\n"
"s_barrier\n"
"ds_read_b64_tr_b16 a[160:161], v3 offset:35840\n"
"v_mfma_f32_16x16x32_bf16 v[128:131], a[180:183], a[240:243], v[128:131]\n"
"ds_read_b64_tr_b16 a[162:163], v3 offset:44032\n"
"v_lshrrev_b32 v56, 1, v53\n"
"v_mfma_f32_16x16x32_bf16 v[132:135], a[176:179], a[228:231], v[132:135]\n"
"buffer_load_dwordx2 v[14:15], v50, s[36:39], 0 offen offset:0\n"
"v_mfma_f32_16x16x32_bf16 v[132:135], a[180:183], a[244:247], v[132:135]\n"
"ds_read_b64_tr_b16 a[164:165], v3 offset:49152\n"
"v_bfe_u32 v57, v9, 4, 3\n"
"v_mfma_f32_16x16x32_bf16 v[136:139], a[176:179], a[232:235], v[136:139]\n"
"s_add_u32 s82, s42, 259072\n"
"ds_read_b64_tr_b16 a[166:167], v3 offset:57344\n"
"v_mfma_f32_16x16x32_bf16 v[136:139], a[180:183], a[248:251], v[136:139]\n"
"buffer_load_dwordx4 v12, s[56:59], s82 offen offset:3072 lds\n"
"v_mfma_f32_16x16x32_bf16 v[140:143], a[176:179], a[236:239], v[140:143]\n"
"ds_read_b64_tr_b16 a[168:169], v4 offset:35840\n"
"v_mfma_f32_16x16x32_bf16 v[140:143], a[180:183], a[252:255], v[140:143]\n"
"ds_read_b64_tr_b16 a[170:171], v4 offset:44032\n"
"v_mfma_f32_16x16x32_bf16 v[144:147], a[184:187], a[224:227], v[144:147]\n"
"buffer_load_dwordx2 v[28:29], v50, s[36:39], 0 offen offset:128\n"
"v_mfma_f32_16x16x32_bf16 v[144:147], a[188:191], a[240:243], v[144:147]\n"
"s_add_u32 m0, s77, 16384\n"
"ds_read_b64_tr_b16 a[172:173], v4 offset:49152\n"
"v_mfma_f32_16x16x32_bf16 v[148:151], a[184:187], a[228:231], v[148:151]\n"
"s_add_u32 s82, s42, 4224\n"
"ds_read_b64_tr_b16 a[174:175], v4 offset:57344\n"
"v_mfma_f32_16x16x32_bf16 v[148:151], a[188:191], a[244:247], v[148:151]\n"
"buffer_load_dwordx4 v12, s[56:59], s82 offen offset:0 lds\n"
"v_mfma_f32_16x16x32_bf16 v[152:155], a[184:187], a[232:235], v[152:155]\n"
"v_mfma_f32_16x16x32_bf16 v[152:155], a[188:191], a[248:251], v[152:155]\n"
"s_add_u32 s82, s42, 40064\n"
"v_mfma_f32_16x16x32_bf16 v[156:159], a[184:187], a[236:239], v[156:159]\n"
"buffer_load_dwordx4 v12, s[56:59], s82 offen offset:1024 lds\n"
"v_mfma_f32_16x16x32_bf16 v[156:159], a[188:191], a[252:255], v[156:159]\n"
"s_waitcnt lgkmcnt(0)\n"
"s_waitcnt vmcnt(13)\n"
"v_cmp_ne_u64 vcc, v[26:27], v[22:23]\n"
"s_nop 1\n"
"s_cbranch_vccz L_ok4_%=\n"
"s_mov_b32 s100, 0x4000\n"
"L_rt4_%=:\n"
"s_sleep 2\n"
"buffer_load_dwordx2 v[26:27], v25, s[64:67], s19 offen sc1\n"
"s_waitcnt vmcnt(0)\n"
"v_cmp_ne_u64 vcc, v[26:27], v[22:23]\n"
"s_nop 4\n"
"s_cbranch_vccz L_ok4_%=\n"
"s_sub_u32 s100, s100, 1\n"
"s_cmp_lg_u32 s100, 0\n"
"s_cbranch_scc1 L_rt4_%=\n"
"L_ok4_%=:\n"
"v_mfma_f32_16x16x32_bf16 v[160:163], a[160:163], a[224:227], v[160:163]\n"
"s_addk_i32 m0, 16384\n"
"ds_read_b64_tr_b16 a[176:177], v3 offset:50176\n"
"v_cmp_gt_u32 vcc, 0x80, v52\n"
"v_mfma_f32_16x16x32_bf16 v[160:163], a[164:167], a[240:243], v[160:163]\n"
"s_add_u32 s82, s8, 50176\n"
"ds_read_b64_tr_b16 a[178:179], v3 offset:58368\n"
"v_cndmask_b32 v51, v55, v54, vcc\n"
"v_mfma_f32_16x16x32_bf16 v[164:167], a[160:163], a[228:231], v[164:167]\n"
"buffer_load_dwordx4 v9, s[64:67], s82 offen offset:2048 sc1 lds\n"
"v_mfma_f32_16x16x32_bf16 v[164:167], a[164:167], a[244:247], v[164:167]\n"
"ds_read_b64_tr_b16 a[180:181], v3 offset:51200\n"
"v_lshl_add_u32 v54, s72, 2, v53\n"
"v_mfma_f32_16x16x32_bf16 v[168:171], a[160:163], a[232:235], v[168:171]\n"
"ds_read_b64_tr_b16 a[182:183], v3 offset:59392\n"
"v_and_b32 v55, 7, v54\n"
"v_mfma_f32_16x16x32_bf16 v[168:171], a[164:167], a[248:251], v[168:171]\n"
"v_lshlrev_b32 v55, 4, v55\n"
"v_xor_b32 v55, v55, v52\n"
"v_mfma_f32_16x16x32_bf16 v[172:175], a[160:163], a[236:239], v[172:175]\n"
"ds_read_b64_tr_b16 a[184:185], v4 offset:50176\n"
"v_lshl_add_u32 v5, v54, 8, v55\n"
"v_mfma_f32_16x16x32_bf16 v[172:175], a[164:167], a[252:255], v[172:175]\n"
"s_addk_i32 s82, 3072\n"
"ds_read_b64_tr_b16 a[186:187], v4 offset:58368\n"
"v_and_b32 v54, 1, v53\n"
"v_mfma_f32_16x16x32_bf16 v[176:179], a[168:171], a[224:227], v[176:179]\n"
"buffer_load_dwordx4 v9, s[64:67], s82 offen offset:3072 sc1 lds\n"
"v_mfma_f32_16x16x32_bf16 v[176:179], a[172:175], a[240:243], v[176:179]\n"
"ds_read_b64_tr_b16 a[188:189], v4 offset:51200\n"
"v_lshl_add_u32 v55, v54, 2, s96\n"
"v_mfma_f32_16x16x32_bf16 v[180:183], a[168:171], a[228:231], v[180:183]\n"
"s_addk_i32 m0, 16384\n"
"ds_read_b64_tr_b16 a[190:191], v4 offset:59392\n"
"v_add_u32 v55, v55, v56\n"
"v_mfma_f32_16x16x32_bf16 v[180:183], a[172:175], a[244:247], v[180:183]\n"
"s_addk_i32 s82, 7168\n"
"v_xor_b32 v55, v55, v57\n"
"v_lshlrev_b32 v55, 4, v55\n"
"v_mfma_f32_16x16x32_bf16 v[184:187], a[168:171], a[232:235], v[184:187]\n"
"buffer_load_dwordx4 v9, s[64:67], s82 offen offset:0 sc1 lds\n"
"v_mfma_f32_16x16x32_bf16 v[184:187], a[172:175], a[248:251], v[184:187]\n"
"s_lshl_b32 s96, s72, 1\n"
"v_lshl_add_u32 v6, v52, 4, v55\n"
"v_lshl_add_u32 v57, v57, 1, v54\n"
"v_mfma_f32_16x16x32_bf16 v[188:191], a[168:171], a[236:239], v[188:191]\n"
"s_addk_i32 s82, 3072\n"
"v_bfe_u32 v55, v9, 7, 1\n"
"v_add_u32 v55, s96, v55\n"
"v_mfma_f32_16x16x32_bf16 v[188:191], a[172:175], a[252:255], v[188:191]\n"
"s_waitcnt lgkmcnt(0)\n"
"buffer_load_dwordx4 v9, s[64:67], s82 offen offset:1024 sc1 lds\n"
"v_mfma_f32_16x16x32_bf16 v[192:195], a[176:179], a[224:227], v[192:195]\n"
"s_barrier\n"
"ds_read_b64_tr_b16 a[160:161], v3 offset:52224\n"
"v_mfma_f32_16x16x32_bf16 v[192:195], a[180:183], a[240:243], v[192:195]\n"
"s_lshr_b32 s96, s75, 1\n"
"ds_read_b64_tr_b16 a[162:163], v3 offset:60416\n"
"v_and_b32 v54, 7, v57\n"
"v_mfma_f32_16x16x32_bf16 v[196:199], a[176:179], a[228:231], v[196:199]\n"
"buffer_load_dwordx2 v[30:31], v50, s[36:39], 0 offen offset:256\n"
"v_mfma_f32_16x16x32_bf16 v[196:199], a[180:183], a[244:247], v[196:199]\n"
"s_add_u32 m0, s77, 16384\n"
"ds_read_b64_tr_b16 a[164:165], v1 offset:0\n"
"v_xor_b32 v55, v55, v54\n"
"v_mfma_f32_16x16x32_bf16 v[200:203], a[176:179], a[232:235], v[200:203]\n"
"s_add_u32 s82, s42, 75904\n"
"ds_read_b64_tr_b16 a[166:167], v1 offset:8192\n"
"v_lshlrev_b32 v55, 4, v55\n"
"v_mfma_f32_16x16x32_bf16 v[200:203], a[180:183], a[248:251], v[200:203]\n"
"buffer_load_dwordx4 v12, s[56:59], s82 offen offset:2048 lds\n"
"v_mfma_f32_16x16x32_bf16 v[204:207], a[176:179], a[236:239], v[204:207]\n"
"s_sub_u32 s9, s96, s97\n"
"ds_read_b64_tr_b16 a[168:169], v4 offset:52224\n"
"v_lshl_add_u32 v55, v57, 8, v55\n"
"v_mfma_f32_16x16x32_bf16 v[204:207], a[180:183], a[252:255], v[204:207]\n"
"s_sub_u32 s97, s96, 4\n"
"ds_read_b64_tr_b16 a[170:171], v4 offset:60416\n"
"v_lshl_add_u32 v7, v56, 13, v55\n"
"v_mfma_f32_16x16x32_bf16 v[208:211], a[184:187], a[224:227], v[208:211]\n"
"buffer_load_dwordx2 v[62:63], v50, s[36:39], 0 offen offset:384\n"
"v_mfma_f32_16x16x32_bf16 v[208:211], a[188:191], a[240:243], v[208:211]\n"
"ds_read_b64_tr_b16 a[172:173], v2 offset:0\n"
"v_lshrrev_b32 v54, 8, v9\n"
"v_mfma_f32_16x16x32_bf16 v[212:215], a[184:187], a[228:231], v[212:215]\n"
"s_add_u32 s82, s42, 111744\n"
"ds_read_b64_tr_b16 a[174:175], v2 offset:8192\n"
"v_cmp_eq_u32 vcc, s97, v54\n"
"v_mfma_f32_16x16x32_bf16 v[212:215], a[188:191], a[244:247], v[212:215]\n"
"buffer_load_dwordx4 v12, s[56:59], s82 offen offset:3072 lds\n"
"v_mfma_f32_16x16x32_bf16 v[216:219], a[184:187], a[232:235], v[216:219]\n"
"s_add_u32 m0, s77, 81920\n"
"v_mov_b32 v55, 0x7ffffff0\n"
"v_cndmask_b32 v98, v48, v55, vcc\n"
"v_mfma_f32_16x16x32_bf16 v[216:219], a[188:191], a[248:251], v[216:219]\n"
"s_add_u32 s82, s42, 151680\n"
"v_cmp_eq_u32 vcc, s96, v54\n"
"s_nop 1\n"
"v_cndmask_b32 v48, v48, v55, vcc\n"
"v_mfma_f32_16x16x32_bf16 v[220:223], a[184:187], a[236:239], v[220:223]\n"
"buffer_load_dwordx4 v12, s[56:59], s82 offen offset:0 lds\n"
"v_mfma_f32_16x16x32_bf16 v[220:223], a[188:191], a[252:255], v[220:223]\n"
"s_waitcnt lgkmcnt(0)\n"
"s_lshl_b32 s96, s74, 10\n"
"s_add_u32 s96, s96, 131072\n"
"v_mfma_f32_16x16x32_bf16 v[224:227], a[160:163], a[224:227], v[224:227]\n"
"ds_read_b64_tr_b16 a[176:177], v96 offset:1024\n"
"v_add_u32 v99, s96, v9\n"
"v_mfma_f32_16x16x32_bf16 v[224:227], a[164:167], a[240:243], v[224:227]\n"
"s_lshl_b32 s96, s72, 9\n"
"ds_read_b64_tr_b16 a[178:179], v96 offset:5120\n"
"v_and_b32 v54, 0xf0, v9\n"
"v_mfma_f32_16x16x32_bf16 v[228:231], a[160:163], a[228:231], v[228:231]\n"
"buffer_load_dwordx2 v[110:111], v51, s[36:39], 0 offen offset:0\n"
"v_mfma_f32_16x16x32_bf16 v[228:231], a[164:167], a[244:247], v[228:231]\n"
"s_add_u32 s96, s96, 131072\n"
"ds_read_b64_tr_b16 a[180:181], v96 offset:9216\n"
"v_add_u32 v100, s96, v54\n"
"v_mfma_f32_16x16x32_bf16 v[232:235], a[160:163], a[232:235], v[232:235]\n"
"s_lshl_b32 s96, s72, 8\n"
"s_add_u32 s82, s42, 187520\n"
"ds_read_b64_tr_b16 a[182:183], v96 offset:13312\n"
"v_mfma_f32_16x16x32_bf16 v[232:235], a[164:167], a[248:251], v[232:235]\n"
"buffer_load_dwordx4 v12, s[56:59], s82 offen offset:1024 lds\n"
"v_mfma_f32_16x16x32_bf16 v[236:239], a[160:163], a[236:239], v[236:239]\n"
"s_add_u32 s96, s96, 139264\n"
"v_add_u32 v106, s96, v54\n"
"v_lshrrev_b32 v109, 1, v99\n"
"v_mfma_f32_16x16x32_bf16 v[236:239], a[164:167], a[252:255], v[236:239]\n"
"v_add_u32 v109, 73728, v109\n"
"v_mfma_f32_16x16x32_bf16 v[240:243], a[168:171], a[224:227], v[240:243]\n"
"v_mfma_f32_16x16x32_bf16 v[240:243], a[172:175], a[240:243], v[240:243]\n"
"v_mfma_f32_16x16x32_bf16 v[244:247], a[168:171], a[228:231], v[244:247]\n"
"s_add_u32 s82, s43, 2176\n"
"v_mfma_f32_16x16x32_bf16 v[244:247], a[172:175], a[244:247], v[244:247]\n"
"buffer_load_dwordx4 v12, s[56:59], s82 offen offset:2048 lds\n"
"v_mfma_f32_16x16x32_bf16 v[248:251], a[168:171], a[232:235], v[248:251]\n"
"v_mfma_f32_16x16x32_bf16 v[248:251], a[172:175], a[248:251], v[248:251]\n"
"s_add_u32 s82, s81, 1152\n"
"v_mfma_f32_16x16x32_bf16 v[252:255], a[168:171], a[236:239], v[252:255]\n"
"buffer_load_dwordx4 v13, s[60:63], s82 offen offset:3072 lds\n"
"v_mfma_f32_16x16x32_bf16 v[252:255], a[172:175], a[252:255], v[252:255]\n"
"s_waitcnt lgkmcnt(0)\n"
"v_mfma_f32_16x16x32_bf16 v[112:115], a[176:179], a[224:227], v[112:115]\n"
"ds_read_b64_tr_b16 a[160:161], v1 offset:2048\n"
"ds_read_b64_tr_b16 a[162:163], v1 offset:10240\n"
"v_mfma_f32_16x16x32_bf16 v[112:115], a[180:183], a[240:243], v[112:115]\n"
"ds_read_b64_tr_b16 a[164:165], v1 offset:3072\n"
"ds_read_b64_tr_b16 a[166:167], v1 offset:11264\n"
"v_mfma_f32_16x16x32_bf16 v[116:119], a[176:179], a[228:231], v[116:119]\n"
"ds_read_b64_tr_b16 a[168:169], v2 offset:2048\n"
"ds_read_b64_tr_b16 a[170:171], v2 offset:10240\n"
"v_mfma_f32_16x16x32_bf16 v[116:119], a[180:183], a[244:247], v[116:119]\n"
"ds_read_b64_tr_b16 a[172:173], v2 offset:3072\n"
"ds_read_b64_tr_b16 a[174:175], v2 offset:11264\n"
"v_mfma_f32_16x16x32_bf16 v[120:123], a[176:179], a[232:235], v[120:123]\n"
"s_addk_i32 m0, 16384\n"
"v_mfma_f32_16x16x32_bf16 v[120:123], a[180:183], a[248:251], v[120:123]\n"
"s_add_u32 s82, s42, 225408\n"
"v_mfma_f32_16x16x32_bf16 v[124:127], a[176:179], a[236:239], v[124:127]\n"
"buffer_load_dwordx4 v12, s[56:59], s82 offen offset:0 lds\n"
"v_mfma_f32_16x16x32_bf16 v[124:127], a[180:183], a[252:255], v[124:127]\n"
"s_waitcnt lgkmcnt(0)\n"
"v_mfma_f32_16x16x32_bf16 v[128:131], a[160:163], a[0:3], v[128:131]\n"
"s_waitcnt vmcnt(12)\n"
"s_barrier\n"
"ds_read_b64_tr_b16 a[176:177], v1 offset:32768\n"
"v_mfma_f32_16x16x32_bf16 v[128:131], a[164:167], a[16:19], v[128:131]\n"
"ds_read_b64_tr_b16 a[178:179], v1 offset:40960\n"
"v_bfe_u32 v54, v9, 5, 3\n"
"v_mfma_f32_16x16x32_bf16 v[132:135], a[160:163], a[4:7], v[132:135]\n"
"v_bfe_u32 v55, v9, 4, 1\n"
"v_lshlrev_b32 v55, 4, v55\n"
"v_mfma_f32_16x16x32_bf16 v[132:135], a[164:167], a[20:23], v[132:135]\n"
"ds_read_b64_tr_b16 a[180:181], v1 offset:33792\n"
"v_mov_b32 v56, 0x3f80\n"
"v_mfma_f32_16x16x32_bf16 v[136:139], a[160:163], a[8:11], v[136:139]\n"
"s_add_u32 s82, s42, 261248\n"
"ds_read_b64_tr_b16 a[182:183], v1 offset:41984\n"
"v_lshlrev_b32 v55, v55, v56\n"
"v_mfma_f32_16x16x32_bf16 v[136:139], a[164:167], a[24:27], v[136:139]\n"
"buffer_load_dwordx4 v12, s[56:59], s82 offen offset:1024 lds\n"
"v_mfma_f32_16x16x32_bf16 v[140:143], a[160:163], a[12:15], v[140:143]\n"
"ds_read_b64_tr_b16 a[184:185], v2 offset:32768\n"
"v_cmp_eq_u32 vcc, 0, v54\n"
"v_mfma_f32_16x16x32_bf16 v[140:143], a[164:167], a[28:31], v[140:143]\n"
"ds_read_b64_tr_b16 a[186:187], v2 offset:40960\n"
"v_cndmask_b32 v32, 0, v55, vcc\n"
"v_mfma_f32_16x16x32_bf16 v[144:147], a[168:171], a[0:3], v[144:147]\n"
"ds_read_b128 a[112:115], v104 offset:34816\n"
"v_cmp_eq_u32 vcc, 1, v54\n"
"v_mfma_f32_16x16x32_bf16 v[144:147], a[172:175], a[16:19], v[144:147]\n"
"ds_read_b64_tr_b16 a[188:189], v2 offset:33792\n"
"ds_read_b128 a[116:119], v104 offset:38912\n"
"v_mfma_f32_16x16x32_bf16 v[148:151], a[168:171], a[4:7], v[148:151]\n"
"s_add_u32 s82, s42, 2304\n"
"ds_read_b64_tr_b16 a[190:191], v2 offset:41984\n"
"v_cndmask_b32 v33, 0, v55, vcc\n"
"v_mfma_f32_16x16x32_bf16 v[148:151], a[172:175], a[20:23], v[148:151]\n"
"buffer_load_dwordx4 v12, s[56:59], s82 offen offset:2048 lds\n"
"v_mfma_f32_16x16x32_bf16 v[152:155], a[168:171], a[8:11], v[152:155]\n"
"ds_read_b128 a[120:123], v104 offset:43008\n"
"v_cmp_eq_u32 vcc, 2, v54\n"
"v_mfma_f32_16x16x32_bf16 v[152:155], a[172:175], a[24:27], v[152:155]\n"
"s_add_u32 s82, s42, 38144\n"
"ds_read_b128 a[124:127], v104 offset:47104\n"
"v_cndmask_b32 v34, 0, v55, vcc\n"
"v_mfma_f32_16x16x32_bf16 v[156:159], a[168:171], a[12:15], v[156:159]\n"
"buffer_load_dwordx4 v12, s[56:59], s82 offen offset:3072 lds\n"
"v_mfma_f32_16x16x32_bf16 v[156:159], a[172:175], a[28:31], v[156:159]\n"
"s_waitcnt lgkmcnt(2)\n"
"v_cmp_eq_u32 vcc, 3, v54\n"
"s_nop 1\n"
"v_cndmask_b32 v35, 0, v55, vcc\n"
"v_mfma_f32_16x16x32_bf16 v[128:131], a[176:179], a[32:35], v[128:131]\n"
"s_waitcnt vmcnt(13)\n"
"s_barrier\n"
"ds_read_b64_tr_b16 a[160:161], v1 offset:51200\n"
"v_mfma_f32_16x16x32_bf16 v[128:131], a[180:183], a[48:51], v[128:131]\n"
"ds_read_b64_tr_b16 a[162:163], v1 offset:59392\n"
"v_cmp_eq_u32 vcc, 4, v54\n"
"v_mfma_f32_16x16x32_bf16 v[132:135], a[176:179], a[36:39], v[132:135]\n"
"s_nop 0\n"
"v_cndmask_b32 v36, 0, v55, vcc\n"
"v_cmp_eq_u32 vcc, 5, v54\n"
"v_mfma_f32_16x16x32_bf16 v[132:135], a[180:183], a[52:55], v[132:135]\n"
"s_add_u32 m0, s77, 0\n"
"ds_read_b64_tr_b16 a[164:165], v1 offset:52224\n"
"v_cndmask_b32 v37, 0, v55, vcc\n"
"v_mfma_f32_16x16x32_bf16 v[136:139], a[176:179], a[40:43], v[136:139]\n"
"s_add_u32 s82, s42, 78080\n"
"ds_read_b64_tr_b16 a[166:167], v1 offset:60416\n"
"v_cmp_eq_u32 vcc, 6, v54\n"
"v_mfma_f32_16x16x32_bf16 v[136:139], a[180:183], a[56:59], v[136:139]\n"
"buffer_load_dwordx4 v12, s[56:59], s82 offen offset:0 lds\n"
"v_mfma_f32_16x16x32_bf16 v[140:143], a[176:179], a[44:47], v[140:143]\n"
"ds_read_b64_tr_b16 a[168:169], v2 offset:51200\n"
"v_cndmask_b32 v38, 0, v55, vcc\n"
"v_mfma_f32_16x16x32_bf16 v[140:143], a[180:183], a[60:63], v[140:143]\n"
"ds_read_b64_tr_b16 a[170:171], v2 offset:59392\n"
"v_cmp_eq_u32 vcc, 7, v54\n"
"v_mfma_f32_16x16x32_bf16 v[144:147], a[184:187], a[32:35], v[144:147]\n"
"s_nop 0\n"
"v_cndmask_b32 v39, 0, v55, vcc\n"
"v_cndmask_b32 v54, 0, v32, s[90:91]\n"
"v_mfma_f32_16x16x32_bf16 v[144:147], a[188:191], a[48:51], v[144:147]\n"
"ds_read_b64_tr_b16 a[172:173], v2 offset:52224\n"
"v_accvgpr_write_b32 a216, v54\n"
"v_mfma_f32_16x16x32_bf16 v[148:151], a[184:187], a[36:39], v[148:151]\n"
"s_add_u32 s82, s42, 113920\n"
"ds_read_b64_tr_b16 a[174:175], v2 offset:60416\n"
"v_mfma_f32_16x16x32_bf16 v[148:151], a[188:191], a[52:55], v[148:151]\n"
"buffer_load_dwordx4 v12, s[56:59], s82 offen offset:1024 lds\n"
"v_mfma_f32_16x16x32_bf16 v[152:155], a[184:187], a[40:43], v[152:155]\n"
"v_cndmask_b32 v54, 0, v33, s[90:91]\n"
"v_accvgpr_write_b32 a217, v54\n"
"v_mfma_f32_16x16x32_bf16 v[152:155], a[188:191], a[56:59], v[152:155]\n"
"s_add_u32 s82, s42, 149760\n"
"v_cndmask_b32 v54, 0, v34, s[90:91]\n"
"v_mfma_f32_16x16x32_bf16 v[156:159], a[184:187], a[44:47], v[156:159]\n"
"buffer_load_dwordx4 v12, s[56:59], s82 offen offset:2048 lds\n"
"v_mfma_f32_16x16x32_bf16 v[156:159], a[188:191], a[60:63], v[156:159]\n"
"s_waitcnt lgkmcnt(0)\n"
"v_accvgpr_write_b32 a218, v54\n"
"v_cndmask_b32 v54, 0, v35, s[90:91]\n"
"v_mfma_f32_16x16x32_bf16 v[128:131], a[160:163], a[64:67], v[128:131]\n"
"s_barrier\n"
"ds_read_b64_tr_b16 a[176:177], v3 offset:0\n"
"v_mfma_f32_16x16x32_bf16 v[128:131], a[164:167], a[80:83], v[128:131]\n"
"ds_read_b64_tr_b16 a[178:179], v3 offset:8192\n"
"v_accvgpr_write_b32 a219, v54\n"
"v_mfma_f32_16x16x32_bf16 v[132:135], a[160:163], a[68:71], v[132:135]\n"
"v_cndmask_b32 v54, 0, v36, s[90:91]\n"
"v_accvgpr_write_b32 a220, v54\n"
"v_mfma_f32_16x16x32_bf16 v[132:135], a[164:167], a[84:87], v[132:135]\n"
"ds_read_b64_tr_b16 a[180:181], v3 offset:1024\n"
"v_cndmask_b32 v54, 0, v37, s[90:91]\n"
"v_mfma_f32_16x16x32_bf16 v[136:139], a[160:163], a[72:75], v[136:139]\n"
"s_add_u32 s82, s42, 185600\n"
"ds_read_b64_tr_b16 a[182:183], v3 offset:9216\n"
"v_mfma_f32_16x16x32_bf16 v[136:139], a[164:167], a[88:91], v[136:139]\n"
"buffer_load_dwordx4 v12, s[56:59], s82 offen offset:3072 lds\n"
"v_mfma_f32_16x16x32_bf16 v[140:143], a[160:163], a[76:79], v[140:143]\n"
"ds_read_b64_tr_b16 a[184:185], v4 offset:0\n"
"v_accvgpr_write_b32 a221, v54\n"
"v_mfma_f32_16x16x32_bf16 v[140:143], a[164:167], a[92:95], v[140:143]\n"
"ds_read_b128 a[128:131], v104 offset:49152\n"
"ds_read_b64_tr_b16 a[186:187], v4 offset:8192\n"
"v_mfma_f32_16x16x32_bf16 v[144:147], a[168:171], a[64:67], v[144:147]\n"
"ds_read_b128 a[132:135], v104 offset:53248\n"
"v_cndmask_b32 v54, 0, v38, s[90:91]\n"
"v_mfma_f32_16x16x32_bf16 v[144:147], a[172:175], a[80:83], v[144:147]\n"
"s_add_u32 m0, s77, 114688\n"
"ds_read_b64_tr_b16 a[188:189], v4 offset:1024\n"
"v_mfma_f32_16x16x32_bf16 v[148:151], a[168:171], a[68:71], v[148:151]\n"
"s_add_u32 s82, s43, 4352\n"
"ds_read_b64_tr_b16 a[190:191], v4 offset:9216\n"
"v_mfma_f32_16x16x32_bf16 v[148:151], a[172:175], a[84:87], v[148:151]\n"
"buffer_load_dwordx4 v12, s[56:59], s82 offen offset:0 lds\n"
"v_mfma_f32_16x16x32_bf16 v[152:155], a[168:171], a[72:75], v[152:155]\n"
"ds_read_b128 a[136:139], v104 offset:57344\n"
"v_accvgpr_write_b32 a222, v54\n"
"v_mfma_f32_16x16x32_bf16 v[152:155], a[172:175], a[88:91], v[152:155]\n"
"s_add_u32 s82, s81, 3328\n"
"v_cndmask_b32 v54, 0, v39, s[90:91]\n"
"v_mfma_f32_16x16x32_bf16 v[156:159], a[168:171], a[76:79], v[156:159]\n"
"buffer_load_dwordx4 v13, s[60:63], s82 offen offset:1024 lds\n"
"v_mfma_f32_16x16x32_bf16 v[156:159], a[172:175], a[92:95], v[156:159]\n"
"s_waitcnt lgkmcnt(1)\n"
"ds_read_b128 a[140:143], v104 offset:61440\n"
"v_accvgpr_write_b32 a223, v54\n"
"v_mfma_f32_16x16x32_bf16 v[128:131], a[176:179], a[96:99], v[128:131]\n"
"ds_read_b64_tr_b16 a[160:161], v3 offset:2048\n"
"v_add_co_u32 v40, vcc, 0x7f4a7c15, v40\n"
"v_mfma_f32_16x16x32_bf16 v[128:131], a[180:183], a[144:147], v[128:131]\n"
"ds_read_b64_tr_b16 a[162:163], v3 offset:10240\n"
"v_mov_b32 v54, 0x9e3779b9\n"
"v_mfma_f32_16x16x32_bf16 v[132:135], a[176:179], a[100:103], v[132:135]\n"
"v_addc_co_u32 v41, vcc, v54, v41, vcc\n"
"v_mfma_f32_16x16x32_bf16 v[132:135], a[180:183], a[148:151], v[132:135]\n"
"ds_read_b64_tr_b16 a[164:165], v3 offset:3072\n"
"v_mfma_f32_16x16x32_bf16 v[136:139], a[176:179], a[104:107], v[136:139]\n"
"ds_read_b64_tr_b16 a[166:167], v3 offset:11264\n"
"v_mfma_f32_16x16x32_bf16 v[136:139], a[180:183], a[152:155], v[136:139]\n"
"v_mfma_f32_16x16x32_bf16 v[140:143], a[176:179], a[108:111], v[140:143]\n"
"ds_read_b64_tr_b16 a[168:169], v4 offset:2048\n"
"v_mfma_f32_16x16x32_bf16 v[140:143], a[180:183], a[156:159], v[140:143]\n"
"s_add_u32 s82, s42, 223488\n"
"ds_read_b64_tr_b16 a[170:171], v4 offset:10240\n"
"v_mfma_f32_16x16x32_bf16 v[144:147], a[184:187], a[96:99], v[144:147]\n"
"buffer_load_dwordx4 v12, s[56:59], s82 offen offset:2048 lds\n"
"v_mfma_f32_16x16x32_bf16 v[144:147], a[188:191], a[144:147], v[144:147]\n"
"ds_read_b64_tr_b16 a[172:173], v4 offset:3072\n"
"v_mfma_f32_16x16x32_bf16 v[148:151], a[184:187], a[100:103], v[148:151]\n"
"ds_read_b64_tr_b16 a[174:175], v4 offset:11264\n"
"v_mfma_f32_16x16x32_bf16 v[148:151], a[188:191], a[148:151], v[148:151]\n"
"ds_read_b64_tr_b16 a[208:209], v101 offset:0\n"
"v_mfma_f32_16x16x32_bf16 v[152:155], a[184:187], a[104:107], v[152:155]\n"
"s_add_u32 s82, s42, 259328\n"
"ds_read_b64_tr_b16 a[210:211], v101 offset:1024\n"
"v_mfma_f32_16x16x32_bf16 v[152:155], a[188:191], a[152:155], v[152:155]\n"
"ds_read_b64_tr_b16 a[212:213], v102 offset:0\n"
"buffer_load_dwordx4 v12, s[56:59], s82 offen offset:3072 lds\n"
"v_mfma_f32_16x16x32_bf16 v[156:159], a[184:187], a[108:111], v[156:159]\n"
"ds_read_b64_tr_b16 a[214:215], v102 offset:1024\n"
"v_mfma_f32_16x16x32_bf16 v[156:159], a[188:191], a[156:159], v[156:159]\n"
"s_waitcnt lgkmcnt(0)\n"
"v_mfma_f32_16x16x32_bf16 v[128:131], a[160:163], a[112:115], v[128:131]\n"
"s_waitcnt vmcnt(17)\n"
"s_barrier\n"
"ds_read_b64_tr_b16 a[176:177], v1 offset:16384\n"
"v_mfma_f32_16x16x32_bf16 v[128:131], a[164:167], a[128:131], v[128:131]\n"
"ds_read_b64_tr_b16 a[178:179], v1 offset:24576\n"
"v_mfma_f32_16x16x32_bf16 v[128:131], a[208:211], a[192:195], v[128:131]\n"
"v_mfma_f32_16x16x32_bf16 v[132:135], a[160:163], a[116:119], v[132:135]\n"
"s_add_u32 m0, s77, 32768\n"
"ds_read_b64_tr_b16 a[180:181], v1 offset:17408\n"
"v_mfma_f32_16x16x32_bf16 v[132:135], a[164:167], a[132:135], v[132:135]\n"
"s_add_u32 s82, s42, 4480\n"
"ds_read_b64_tr_b16 a[182:183], v1 offset:25600\n"
"v_mfma_f32_16x16x32_bf16 v[132:135], a[208:211], a[196:199], v[132:135]\n"
"buffer_load_dwordx4 v12, s[56:59], s82 offen offset:0 lds\n"
"v_mfma_f32_16x16x32_bf16 v[136:139], a[160:163], a[120:123], v[136:139]\n"
"ds_read_b64_tr_b16 a[184:185], v2 offset:16384\n"
"v_mfma_f32_16x16x32_bf16 v[136:139], a[164:167], a[136:139], v[136:139]\n"
"ds_read_b64_tr_b16 a[186:187], v2 offset:24576\n"
"v_mfma_f32_16x16x32_bf16 v[136:139], a[208:211], a[200:203], v[136:139]\n"
"v_mfma_f32_16x16x32_bf16 v[140:143], a[160:163], a[124:127], v[140:143]\n"
"ds_read_b64_tr_b16 a[188:189], v2 offset:17408\n"
"v_mfma_f32_16x16x32_bf16 v[140:143], a[164:167], a[140:143], v[140:143]\n"
"s_add_u32 s82, s42, 40320\n"
"ds_read_b64_tr_b16 a[190:191], v2 offset:25600\n"
"v_mfma_f32_16x16x32_bf16 v[140:143], a[208:211], a[204:207], v[140:143]\n"
"buffer_load_dwordx4 v12, s[56:59], s82 offen offset:1024 lds\n"
"v_mfma_f32_16x16x32_bf16 v[144:147], a[168:171], a[112:115], v[144:147]\n"
"v_mfma_f32_16x16x32_bf16 v[144:147], a[172:175], a[128:131], v[144:147]\n"
"s_add_u32 s82, s42, 76160\n"
"v_mfma_f32_16x16x32_bf16 v[144:147], a[212:215], a[192:195], v[144:147]\n"
"buffer_load_dwordx4 v12, s[56:59], s82 offen offset:2048 lds\n"
"v_mfma_f32_16x16x32_bf16 v[148:151], a[168:171], a[116:119], v[148:151]\n"
"v_mfma_f32_16x16x32_bf16 v[148:151], a[172:175], a[132:135], v[148:151]\n"
"v_mfma_f32_16x16x32_bf16 v[148:151], a[212:215], a[196:199], v[148:151]\n"
"v_mfma_f32_16x16x32_bf16 v[152:155], a[168:171], a[120:123], v[152:155]\n"
"v_mfma_f32_16x16x32_bf16 v[152:155], a[172:175], a[136:139], v[152:155]\n"
"v_mfma_f32_16x16x32_bf16 v[152:155], a[212:215], a[200:203], v[152:155]\n"
"v_mfma_f32_16x16x32_bf16 v[156:159], a[168:171], a[124:127], v[156:159]\n"
"v_mfma_f32_16x16x32_bf16 v[156:159], a[172:175], a[140:143], v[156:159]\n"
"v_mfma_f32_16x16x32_bf16 v[156:159], a[212:215], a[204:207], v[156:159]\n"
"s_waitcnt lgkmcnt(0)\n"
"v_mfma_f32_16x16x32_bf16 v[160:163], a[176:179], a[0:3], v[160:163]\n"
"ds_read_b64_tr_b16 a[160:161], v1 offset:18432\n"
"v_mfma_f32_16x16x32_bf16 v[160:163], a[180:183], a[16:19], v[160:163]\n"
"ds_read_b64_tr_b16 a[162:163], v1 offset:26624\n"
"v_mfma_f32_16x16x32_bf16 v[164:167], a[176:179], a[4:7], v[164:167]\n"
"v_mfma_f32_16x16x32_bf16 v[164:167], a[180:183], a[20:23], v[164:167]\n"
"s_add_u32 s96, s12, 0\n"
"ds_read_b64_tr_b16 a[164:165], v1 offset:19456\n"
"v_cvt_pk_bf16_f32 v128, v128, v129\n"
"v_mfma_f32_16x16x32_bf16 v[168:171], a[176:179], a[8:11], v[168:171]\n"
"s_add_u32 s82, s42, 112000\n"
"ds_read_b64_tr_b16 a[166:167], v1 offset:27648\n"
"v_mfma_f32_16x16x32_bf16 v[168:171], a[180:183], a[24:27], v[168:171]\n"
"buffer_load_dwordx4 v12, s[56:59], s82 offen offset:3072 lds\n"
"v_mfma_f32_16x16x32_bf16 v[172:175], a[176:179], a[12:15], v[172:175]\n"
"ds_read_b64_tr_b16 a[168:169], v2 offset:18432\n"
"v_cvt_pk_bf16_f32 v129, v130, v131\n"
"v_mfma_f32_16x16x32_bf16 v[172:175], a[180:183], a[28:31], v[172:175]\n"
"ds_read_b64_tr_b16 a[170:171], v2 offset:26624\n"
"v_cvt_pk_bf16_f32 v130, v144, v145\n"
"v_mfma_f32_16x16x32_bf16 v[176:179], a[184:187], a[0:3], v[176:179]\n"
"v_cvt_pk_bf16_f32 v131, v146, v147\n"
"v_cvt_pk_bf16_f32 v132, v132, v133\n"
"v_mfma_f32_16x16x32_bf16 v[176:179], a[188:191], a[16:19], v[176:179]\n"
"s_addk_i32 m0, 16384\n"
"ds_read_b64_tr_b16 a[172:173], v2 offset:19456\n"
"v_cvt_pk_bf16_f32 v133, v134, v135\n"
"v_mfma_f32_16x16x32_bf16 v[180:183], a[184:187], a[4:7], v[180:183]\n"
"s_add_u32 s82, s42, 151936\n"
"ds_read_b64_tr_b16 a[174:175], v2 offset:27648\n"
"v_mfma_f32_16x16x32_bf16 v[180:183], a[188:191], a[20:23], v[180:183]\n"
"buffer_load_dwordx4 v12, s[56:59], s82 offen offset:0 lds\n"
"v_mfma_f32_16x16x32_bf16 v[184:187], a[184:187], a[8:11], v[184:187]\n"
"v_cvt_pk_bf16_f32 v134, v148, v149\n"
"v_cvt_pk_bf16_f32 v135, v150, v151\n"
"v_mfma_f32_16x16x32_bf16 v[184:187], a[188:191], a[24:27], v[184:187]\n"
"s_add_u32 s82, s42, 187776\n"
"v_cvt_pk_bf16_f32 v136, v136, v137\n"
"v_mfma_f32_16x16x32_bf16 v[188:191], a[184:187], a[12:15], v[188:191]\n"
"buffer_load_dwordx4 v12, s[56:59], s82 offen offset:1024 lds\n"
"v_mfma_f32_16x16x32_bf16 v[188:191], a[188:191], a[28:31], v[188:191]\n"
"s_waitcnt lgkmcnt(0)\n"
"s_cmp_eq_u32 s9, 0\n"
"s_cselect_b32 s8, 0x7ff00000, s96\n"
"s_cbranch_scc0 L_sf0_%=\n"
"ds_write_b128 v99, v[128:131] offset:0\n"
"L_sf0_%=:\n"
"buffer_store_dwordx4 v[128:131], v46, s[64:67], s8 offen offset:0 sc1\n"
"v_mfma_f32_16x16x32_bf16 v[160:163], a[160:163], a[32:35], v[160:163]\n"
"s_waitcnt vmcnt(15)\n"
"s_barrier\n"
"ds_read_b64_tr_b16 a[176:177], v3 offset:16384\n"
"v_mfma_f32_16x16x32_bf16 v[160:163], a[164:167], a[48:51], v[160:163]\n"
"ds_read_b64_tr_b16 a[178:179], v3 offset:24576\n"
"v_cvt_pk_bf16_f32 v137, v138, v139\n"
"v_mfma_f32_16x16x32_bf16 v[164:167], a[160:163], a[36:39], v[164:167]\n"
"v_cvt_pk_bf16_f32 v138, v152, v153\n"
"v_cvt_pk_bf16_f32 v139, v154, v155\n"
"v_mfma_f32_16x16x32_bf16 v[164:167], a[164:167], a[52:55], v[164:167]\n"
"s_add_u32 s97, s12, 4096\n"
"ds_read_b64_tr_b16 a[180:181], v3 offset:17408\n"
"v_mfma_f32_16x16x32_bf16 v[168:171], a[160:163], a[40:43], v[168:171]\n"
"ds_read_b64_tr_b16 a[182:183], v3 offset:25600\n"
"v_cvt_pk_bf16_f32 v140, v140, v141\n"
"v_mfma_f32_16x16x32_bf16 v[168:171], a[164:167], a[56:59], v[168:171]\n"
"v_cvt_pk_bf16_f32 v141, v142, v143\n"
"v_cvt_pk_bf16_f32 v142, v156, v157\n"
"v_mfma_f32_16x16x32_bf16 v[172:175], a[160:163], a[44:47], v[172:175]\n"
"ds_read_b64_tr_b16 a[184:185], v4 offset:16384\n"
"v_cvt_pk_bf16_f32 v143, v158, v159\n"
"v_mfma_f32_16x16x32_bf16 v[172:175], a[164:167], a[60:63], v[172:175]\n"
"s_add_u32 s82, s43, 2432\n"
"ds_read_b64_tr_b16 a[186:187], v4 offset:24576\n"
"v_mfma_f32_16x16x32_bf16 v[176:179], a[168:171], a[32:35], v[176:179]\n"
"buffer_load_dwordx4 v12, s[56:59], s82 offen offset:2048 lds\n"
"v_mfma_f32_16x16x32_bf16 v[176:179], a[172:175], a[48:51], v[176:179]\n"
"ds_read_b64_tr_b16 a[188:189], v4 offset:17408\n"
"v_mfma_f32_16x16x32_bf16 v[180:183], a[168:171], a[36:39], v[180:183]\n"
"ds_read_b64_tr_b16 a[190:191], v4 offset:25600\n"
"v_mfma_f32_16x16x32_bf16 v[180:183], a[172:175], a[52:55], v[180:183]\n"
"v_mfma_f32_16x16x32_bf16 v[184:187], a[168:171], a[40:43], v[184:187]\n"
"s_add_u32 s82, s81, 1408\n"
"v_mfma_f32_16x16x32_bf16 v[184:187], a[172:175], a[56:59], v[184:187]\n"
"buffer_load_dwordx4 v13, s[60:63], s82 offen offset:3072 lds\n"
"v_mfma_f32_16x16x32_bf16 v[188:191], a[168:171], a[44:47], v[188:191]\n"
"v_mfma_f32_16x16x32_bf16 v[188:191], a[172:175], a[60:63], v[188:191]\n"
"s_waitcnt lgkmcnt(0)\n"
"s_cmp_eq_u32 s9, 1\n"
"s_cselect_b32 s19, 0x7ff00000, s96\n"
"s_cbranch_scc0 L_sf1_%=\n"
"ds_write_b128 v99, v[132:135] offset:0\n"
"L_sf1_%=:\n"
"buffer_store_dwordx4 v[132:135], v46, s[64:67], s19 offen offset:2048 sc1\n"
"v_mfma_f32_16x16x32_bf16 v[160:163], a[176:179], a[64:67], v[160:163]\n"
"ds_read_b64_tr_b16 a[160:161], v3 offset:18432\n"
"v_mfma_f32_16x16x32_bf16 v[160:163], a[180:183], a[80:83], v[160:163]\n"
"ds_read_b64_tr_b16 a[162:163], v3 offset:26624\n"
"v_mfma_f32_16x16x32_bf16 v[164:167], a[176:179], a[68:71], v[164:167]\n"
"v_mfma_f32_16x16x32_bf16 v[164:167], a[180:183], a[84:87], v[164:167]\n"
"ds_read_b64_tr_b16 a[164:165], v3 offset:19456\n"
"v_mfma_f32_16x16x32_bf16 v[168:171], a[176:179], a[72:75], v[168:171]\n"
"ds_read_b64_tr_b16 a[166:167], v3 offset:27648\n"
"v_mfma_f32_16x16x32_bf16 v[168:171], a[180:183], a[88:91], v[168:171]\n"
"v_mfma_f32_16x16x32_bf16 v[172:175], a[176:179], a[76:79], v[172:175]\n"
"s_addk_i32 m0, 16384\n"
"ds_read_b64_tr_b16 a[168:169], v4 offset:18432\n"
"v_mfma_f32_16x16x32_bf16 v[172:175], a[180:183], a[92:95], v[172:175]\n"
"s_add_u32 s82, s42, 225664\n"
"ds_read_b64_tr_b16 a[170:171], v4 offset:26624\n"
"v_mfma_f32_16x16x32_bf16 v[176:179], a[184:187], a[64:67], v[176:179]\n"
"buffer_load_dwordx4 v12, s[56:59], s82 offen offset:0 lds\n"
"v_mfma_f32_16x16x32_bf16 v[176:179], a[188:191], a[80:83], v[176:179]\n"
"ds_read_b64_tr_b16 a[172:173], v4 offset:19456\n"
"v_mfma_f32_16x16x32_bf16 v[180:183], a[184:187], a[68:71], v[180:183]\n"
"ds_read_b64_tr_b16 a[174:175], v4 offset:27648\n"
"v_mfma_f32_16x16x32_bf16 v[180:183], a[188:191], a[84:87], v[180:183]\n"
"v_mfma_f32_16x16x32_bf16 v[184:187], a[184:187], a[72:75], v[184:187]\n"
"s_add_u32 s82, s42, 261504\n"
"v_mfma_f32_16x16x32_bf16 v[184:187], a[188:191], a[88:91], v[184:187]\n"
"buffer_load_dwordx4 v12, s[56:59], s82 offen offset:1024 lds\n"
"v_mfma_f32_16x16x32_bf16 v[188:191], a[184:187], a[76:79], v[188:191]\n"
"v_mfma_f32_16x16x32_bf16 v[188:191], a[188:191], a[92:95], v[188:191]\n"
"s_waitcnt lgkmcnt(0)\n"
"s_cmp_eq_u32 s9, 2\n"
"s_cselect_b32 s8, 0x7ff00000, s97\n"
"s_cbranch_scc0 L_sf2_%=\n"
"ds_write_b128 v99, v[136:139] offset:0\n"
"L_sf2_%=:\n"
"buffer_store_dwordx4 v[136:139], v46, s[64:67], s8 offen offset:0 sc1\n"
"v_mfma_f32_16x16x32_bf16 v[160:163], a[160:163], a[96:99], v[160:163]\n"
"ds_read_b64_tr_b16 a[176:177], v3 offset:32768\n"
"v_mfma_f32_16x16x32_bf16 v[160:163], a[164:167], a[144:147], v[160:163]\n"
"ds_read_b64_tr_b16 a[178:179], v3 offset:40960\n"
"v_mfma_f32_16x16x32_bf16 v[164:167], a[160:163], a[100:103], v[164:167]\n"
"v_mfma_f32_16x16x32_bf16 v[164:167], a[164:167], a[148:151], v[164:167]\n"
"ds_read_b64_tr_b16 a[180:181], v3 offset:33792\n"
"v_mfma_f32_16x16x32_bf16 v[168:171], a[160:163], a[104:107], v[168:171]\n"
"s_add_u32 s82, s5, 2048\n"
"ds_read_b64_tr_b16 a[182:183], v3 offset:41984\n"
"v_mfma_f32_16x16x32_bf16 v[168:171], a[164:167], a[152:155], v[168:171]\n"
"buffer_load_dwordx4 v10, s[56:59], s82 offen offset:2048 lds\n"
"v_mfma_f32_16x16x32_bf16 v[172:175], a[160:163], a[108:111], v[172:175]\n"
"ds_read_b64_tr_b16 a[184:185], v4 offset:32768\n"
"v_mfma_f32_16x16x32_bf16 v[172:175], a[164:167], a[156:159], v[172:175]\n"
"ds_read_b64_tr_b16 a[186:187], v4 offset:40960\n"
"v_mfma_f32_16x16x32_bf16 v[176:179], a[168:171], a[96:99], v[176:179]\n"
"v_mfma_f32_16x16x32_bf16 v[176:179], a[172:175], a[144:147], v[176:179]\n"
"ds_read_b64_tr_b16 a[188:189], v4 offset:33792\n"
"v_mfma_f32_16x16x32_bf16 v[180:183], a[168:171], a[100:103], v[180:183]\n"
"s_add_u32 s82, s5, 74752\n"
"ds_read_b64_tr_b16 a[190:191], v4 offset:41984\n"
"v_mfma_f32_16x16x32_bf16 v[180:183], a[172:175], a[148:151], v[180:183]\n"
"ds_read_b64_tr_b16 a[208:209], v101 offset:2048\n"
"buffer_load_dwordx4 v10, s[56:59], s82 offen offset:3072 lds\n"
"v_mfma_f32_16x16x32_bf16 v[184:187], a[168:171], a[104:107], v[184:187]\n"
"s_add_u32 m0, s77, 16384\n"
"ds_read_b64_tr_b16 a[210:211], v101 offset:3072\n"
"v_mfma_f32_16x16x32_bf16 v[184:187], a[172:175], a[152:155], v[184:187]\n"
"s_add_u32 s82, s5, 151552\n"
"ds_read_b64_tr_b16 a[212:213], v102 offset:2048\n"
"v_mfma_f32_16x16x32_bf16 v[188:191], a[168:171], a[108:111], v[188:191]\n"
"ds_read_b64_tr_b16 a[214:215], v102 offset:3072\n"
"buffer_load_dwordx4 v10, s[56:59], s82 offen offset:0 lds\n"
"v_mfma_f32_16x16x32_bf16 v[188:191], a[172:175], a[156:159], v[188:191]\n"
"s_waitcnt lgkmcnt(0)\n"
"s_cmp_eq_u32 s9, 3\n"
"s_cselect_b32 s19, 0x7ff00000, s97\n"
"s_cbranch_scc0 L_sf3_%=\n"
"ds_write_b128 v99, v[140:143] offset:0\n"
"L_sf3_%=:\n"
"buffer_store_dwordx4 v[140:143], v46, s[64:67], s19 offen offset:2048 sc1\n"
"v_mfma_f32_16x16x32_bf16 v[160:163], a[176:179], a[112:115], v[160:163]\n"
"ds_read_b64_tr_b16 a[160:161], v3 offset:34816\n"
"v_mfma_f32_16x16x32_bf16 v[160:163], a[180:183], a[128:131], v[160:163]\n"
"ds_read_b64_tr_b16 a[162:163], v3 offset:43008\n"
"v_mfma_f32_16x16x32_bf16 v[160:163], a[208:211], a[192:195], v[160:163]\n"
"v_mfma_f32_16x16x32_bf16 v[164:167], a[176:179], a[116:119], v[164:167]\n"
"ds_read_b64_tr_b16 a[164:165], v3 offset:35840\n"
"v_mfma_f32_16x16x32_bf16 v[164:167], a[180:183], a[132:135], v[164:167]\n"
"ds_read_b64_tr_b16 a[166:167], v3 offset:44032\n"
"v_mfma_f32_16x16x32_bf16 v[164:167], a[208:211], a[196:199], v[164:167]\n"
"v_mfma_f32_16x16x32_bf16 v[168:171], a[176:179], a[120:123], v[168:171]\n"
"ds_read_b64_tr_b16 a[168:169], v4 offset:34816\n"
"v_mfma_f32_16x16x32_bf16 v[168:171], a[180:183], a[136:139], v[168:171]\n"
"s_add_u32 s82, s7, 3072\n"
"ds_read_b64_tr_b16 a[170:171], v4 offset:43008\n"
"v_mfma_f32_16x16x32_bf16 v[168:171], a[208:211], a[200:203], v[168:171]\n"
"buffer_load_dwordx4 v10, s[52:55], s82 offen offset:1024 lds\n"
"v_mfma_f32_16x16x32_bf16 v[172:175], a[176:179], a[124:127], v[172:175]\n"
"ds_read_b64_tr_b16 a[172:173], v4 offset:35840\n"
"v_mfma_f32_16x16x32_bf16 v[172:175], a[180:183], a[140:143], v[172:175]\n"
"ds_read_b64_tr_b16 a[174:175], v4 offset:44032\n"
"v_mfma_f32_16x16x32_bf16 v[172:175], a[208:211], a[204:207], v[172:175]\n"
"v_mfma_f32_16x16x32_bf16 v[176:179], a[184:187], a[112:115], v[176:179]\n"
"s_add_u32 s82, s5, 223232\n"
"v_mfma_f32_16x16x32_bf16 v[176:179], a[188:191], a[128:131], v[176:179]\n"
"buffer_load_dwordx4 v10, s[56:59], s82 offen offset:2048 lds\n"
"v_mfma_f32_16x16x32_bf16 v[176:179], a[212:215], a[192:195], v[176:179]\n"
"v_mfma_f32_16x16x32_bf16 v[180:183], a[184:187], a[116:119], v[180:183]\n"
"v_mfma_f32_16x16x32_bf16 v[180:183], a[188:191], a[132:135], v[180:183]\n"
"v_mfma_f32_16x16x32_bf16 v[180:183], a[212:215], a[196:199], v[180:183]\n"
"v_mfma_f32_16x16x32_bf16 v[184:187], a[184:187], a[120:123], v[184:187]\n"
"v_mfma_f32_16x16x32_bf16 v[184:187], a[188:191], a[136:139], v[184:187]\n"
"v_mfma_f32_16x16x32_bf16 v[184:187], a[212:215], a[200:203], v[184:187]\n"
"v_mfma_f32_16x16x32_bf16 v[188:191], a[184:187], a[124:127], v[188:191]\n"
"v_mfma_f32_16x16x32_bf16 v[188:191], a[188:191], a[140:143], v[188:191]\n"
"v_mfma_f32_16x16x32_bf16 v[188:191], a[212:215], a[204:207], v[188:191]\n"
"s_waitcnt lgkmcnt(0)\n"
"v_mfma_f32_16x16x32_bf16 v[192:195], a[160:163], a[0:3], v[192:195]\n"
"s_waitcnt vmcnt(19)\n"
"s_barrier\n"
"ds_read_b64_tr_b16 a[176:177], v1 offset:0\n"
"v_mfma_f32_16x16x32_bf16 v[192:195], a[164:167], a[16:19], v[192:195]\n"
"ds_read_b64_tr_b16 a[178:179], v1 offset:8192\n"
"v_mfma_f32_16x16x32_bf16 v[196:199], a[160:163], a[4:7], v[196:199]\n"
"v_mfma_f32_16x16x32_bf16 v[196:199], a[164:167], a[20:23], v[196:199]\n"
"ds_read_b64_tr_b16 a[180:181], v1 offset:1024\n"
"v_cvt_pk_bf16_f32 v160, v160, v161\n"
"v_mfma_f32_16x16x32_bf16 v[200:203], a[160:163], a[8:11], v[200:203]\n"
"ds_read_b64_tr_b16 a[182:183], v1 offset:9216\n"
"v_cvt_pk_bf16_f32 v161, v162, v163\n"
"v_mfma_f32_16x16x32_bf16 v[200:203], a[164:167], a[24:27], v[200:203]\n"
"v_cvt_pk_bf16_f32 v162, v176, v177\n"
"v_cvt_pk_bf16_f32 v163, v178, v179\n"
"v_mfma_f32_16x16x32_bf16 v[204:207], a[160:163], a[12:15], v[204:207]\n"
"s_add_u32 s96, s12, 16384\n"
"ds_read_b64_tr_b16 a[184:185], v2 offset:0\n"
"v_mfma_f32_16x16x32_bf16 v[204:207], a[164:167], a[28:31], v[204:207]\n"
"ds_read_b64_tr_b16 a[186:187], v2 offset:8192\n"
"v_cvt_pk_bf16_f32 v164, v164, v165\n"
"v_mfma_f32_16x16x32_bf16 v[208:211], a[168:171], a[0:3], v[208:211]\n"
"v_cvt_pk_bf16_f32 v165, v166, v167\n"
"v_cvt_pk_bf16_f32 v166, v180, v181\n"
"v_mfma_f32_16x16x32_bf16 v[208:211], a[172:175], a[16:19], v[208:211]\n"
"ds_read_b64_tr_b16 a[188:189], v2 offset:1024\n"
"v_cvt_pk_bf16_f32 v167, v182, v183\n"
"v_mfma_f32_16x16x32_bf16 v[212:215], a[168:171], a[4:7], v[212:215]\n"
"ds_read_b64_tr_b16 a[190:191], v2 offset:9216\n"
"v_cvt_pk_bf16_f32 v168, v168, v169\n"
"v_mfma_f32_16x16x32_bf16 v[212:215], a[172:175], a[20:23], v[212:215]\n"
"v_cvt_pk_bf16_f32 v169, v170, v171\n"
"v_cvt_pk_bf16_f32 v170, v184, v185\n"
"v_mfma_f32_16x16x32_bf16 v[216:219], a[168:171], a[8:11], v[216:219]\n"
"s_add_u32 s97, s12, 20480\n"
"v_cvt_pk_bf16_f32 v171, v186, v187\n"
"v_mfma_f32_16x16x32_bf16 v[216:219], a[172:175], a[24:27], v[216:219]\n"
"v_cvt_pk_bf16_f32 v172, v172, v173\n"
"v_cvt_pk_bf16_f32 v173, v174, v175\n"
"v_mfma_f32_16x16x32_bf16 v[220:223], a[168:171], a[12:15], v[220:223]\n"
"v_cvt_pk_bf16_f32 v174, v188, v189\n"
"v_cvt_pk_bf16_f32 v175, v190, v191\n"
"v_mfma_f32_16x16x32_bf16 v[220:223], a[172:175], a[28:31], v[220:223]\n"
"s_waitcnt lgkmcnt(0)\n"
"s_cmp_eq_u32 s9, 0\n"
"s_cselect_b32 s8, 0x7ff00000, s96\n"
"s_cbranch_scc0 L_sf4_%=\n"
"ds_write_b128 v99, v[160:163] offset:2048\n"
"L_sf4_%=:\n"
"buffer_store_dwordx4 v[160:163], v46, s[64:67], s8 offen offset:0 sc1\n"
"v_mfma_f32_16x16x32_bf16 v[192:195], a[176:179], a[32:35], v[192:195]\n"
"ds_read_b64_tr_b16 a[160:161], v1 offset:2048\n"
"v_mfma_f32_16x16x32_bf16 v[192:195], a[180:183], a[48:51], v[192:195]\n"
"ds_read_b64_tr_b16 a[162:163], v1 offset:10240\n"
"v_mfma_f32_16x16x32_bf16 v[196:199], a[176:179], a[36:39], v[196:199]\n"
"v_mfma_f32_16x16x32_bf16 v[196:199], a[180:183], a[52:55], v[196:199]\n"
"ds_read_b64_tr_b16 a[164:165], v1 offset:3072\n"
"v_mfma_f32_16x16x32_bf16 v[200:203], a[176:179], a[40:43], v[200:203]\n"
"ds_read_b64_tr_b16 a[166:167], v1 offset:11264\n"
"v_mfma_f32_16x16x32_bf16 v[200:203], a[180:183], a[56:59], v[200:203]\n"
"s_add_u32 s98, s88, 0\n"
"v_mfma_f32_16x16x32_bf16 v[204:207], a[176:179], a[44:47], v[204:207]\n"
"ds_read_b64_tr_b16 a[168:169], v2 offset:2048\n"
"v_mfma_f32_16x16x32_bf16 v[204:207], a[180:183], a[60:63], v[204:207]\n"
"ds_read_b64_tr_b16 a[170:171], v2 offset:10240\n"
"v_mfma_f32_16x16x32_bf16 v[208:211], a[184:187], a[32:35], v[208:211]\n"
"s_waitcnt vmcnt(3)\n"
"buffer_store_dwordx2 v[20:21], v24, s[64:67], s98 offen sc1\n"
"v_mfma_f32_16x16x32_bf16 v[208:211], a[188:191], a[48:51], v[208:211]\n"
"ds_read_b64_tr_b16 a[172:173], v2 offset:3072\n"
"v_mfma_f32_16x16x32_bf16 v[212:215], a[184:187], a[36:39], v[212:215]\n"
"ds_read_b64_tr_b16 a[174:175], v2 offset:11264\n"
"v_mfma_f32_16x16x32_bf16 v[212:215], a[188:191], a[52:55], v[212:215]\n"
"v_mfma_f32_16x16x32_bf16 v[216:219], a[184:187], a[40:43], v[216:219]\n"
"v_mfma_f32_16x16x32_bf16 v[216:219], a[188:191], a[56:59], v[216:219]\n"
"v_mfma_f32_16x16x32_bf16 v[220:223], a[184:187], a[44:47], v[220:223]\n"
"v_mfma_f32_16x16x32_bf16 v[220:223], a[188:191], a[60:63], v[220:223]\n"
"s_waitcnt lgkmcnt(0)\n"
"s_cmp_eq_u32 s9, 1\n"
"s_cselect_b32 s19, 0x7ff00000, s96\n"
"s_cbranch_scc0 L_sf5_%=\n"
"ds_write_b128 v99, v[164:167] offset:2048\n"
"L_sf5_%=:\n"
"buffer_store_dwordx4 v[164:167], v46, s[64:67], s19 offen offset:2048 sc1\n"
"v_mfma_f32_16x16x32_bf16 v[192:195], a[160:163], a[64:67], v[192:195]\n"
"ds_read_b64_tr_b16 a[176:177], v3 offset:49152\n"
"v_mfma_f32_16x16x32_bf16 v[192:195], a[164:167], a[80:83], v[192:195]\n"
"ds_read_b64_tr_b16 a[178:179], v3 offset:57344\n"
"v_mfma_f32_16x16x32_bf16 v[196:199], a[160:163], a[68:71], v[196:199]\n"
"v_mfma_f32_16x16x32_bf16 v[196:199], a[164:167], a[84:87], v[196:199]\n"
"ds_read_b64_tr_b16 a[180:181], v3 offset:50176\n"
"v_mfma_f32_16x16x32_bf16 v[200:203], a[160:163], a[72:75], v[200:203]\n"
"ds_read_b64_tr_b16 a[182:183], v3 offset:58368\n"
"v_mfma_f32_16x16x32_bf16 v[200:203], a[164:167], a[88:91], v[200:203]\n"
"v_mfma_f32_16x16x32_bf16 v[204:207], a[160:163], a[76:79], v[204:207]\n"
"ds_read_b64_tr_b16 a[184:185], v4 offset:49152\n"
"v_mfma_f32_16x16x32_bf16 v[204:207], a[164:167], a[92:95], v[204:207]\n"
"ds_read_b64_tr_b16 a[186:187], v4 offset:57344\n"
"v_mfma_f32_16x16x32_bf16 v[208:211], a[168:171], a[64:67], v[208:211]\n"
"v_mfma_f32_16x16x32_bf16 v[208:211], a[172:175], a[80:83], v[208:211]\n"
"ds_read_b64_tr_b16 a[188:189], v4 offset:50176\n"
"v_mfma_f32_16x16x32_bf16 v[212:215], a[168:171], a[68:71], v[212:215]\n"
"ds_read_b64_tr_b16 a[190:191], v4 offset:58368\n"
"v_mfma_f32_16x16x32_bf16 v[212:215], a[172:175], a[84:87], v[212:215]\n"
"v_mfma_f32_16x16x32_bf16 v[216:219], a[168:171], a[72:75], v[216:219]\n"
"v_mfma_f32_16x16x32_bf16 v[216:219], a[172:175], a[88:91], v[216:219]\n"
"v_mfma_f32_16x16x32_bf16 v[220:223], a[168:171], a[76:79], v[220:223]\n"
"v_mfma_f32_16x16x32_bf16 v[220:223], a[172:175], a[92:95], v[220:223]\n"
"s_waitcnt lgkmcnt(0)\n"
"s_cmp_eq_u32 s9, 2\n"
"s_cselect_b32 s8, 0x7ff00000, s97\n"
"s_cbranch_scc0 L_sf6_%=\n"
"ds_write_b128 v99, v[168:171] offset:2048\n"
"L_sf6_%=:\n"
"buffer_store_dwordx4 v[168:171], v46, s[64:67], s8 offen offset:0 sc1\n"
"v_mfma_f32_16x16x32_bf16 v[192:195], a[176:179], a[96:99], v[192:195]\n"
"ds_read_b64_tr_b16 a[160:161], v3 offset:51200\n"
"v_mfma_f32_16x16x32_bf16 v[192:195], a[180:183], a[144:147], v[192:195]\n"
"ds_read_b64_tr_b16 a[162:163], v3 offset:59392\n"
"v_mfma_f32_16x16x32_bf16 v[196:199], a[176:179], a[100:103], v[196:199]\n"
"v_mfma_f32_16x16x32_bf16 v[196:199], a[180:183], a[148:151], v[196:199]\n"
"ds_read_b64_tr_b16 a[164:165], v3 offset:52224\n"
"v_mfma_f32_16x16x32_bf16 v[200:203], a[176:179], a[104:107], v[200:203]\n"
"ds_read_b64_tr_b16 a[166:167], v3 offset:60416\n"
"v_mfma_f32_16x16x32_bf16 v[200:203], a[180:183], a[152:155], v[200:203]\n"
"v_mfma_f32_16x16x32_bf16 v[204:207], a[176:179], a[108:111], v[204:207]\n"
"ds_read_b64_tr_b16 a[168:169], v4 offset:51200\n"
"v_mfma_f32_16x16x32_bf16 v[204:207], a[180:183], a[156:159], v[204:207]\n"
"ds_read_b64_tr_b16 a[170:171], v4 offset:59392\n"
"v_mfma_f32_16x16x32_bf16 v[208:211], a[184:187], a[96:99], v[208:211]\n"
"v_mfma_f32_16x16x32_bf16 v[208:211], a[188:191], a[144:147], v[208:211]\n"
"ds_read_b64_tr_b16 a[172:173], v4 offset:52224\n"
"v_mfma_f32_16x16x32_bf16 v[212:215], a[184:187], a[100:103], v[212:215]\n"
"ds_read_b64_tr_b16 a[174:175], v4 offset:60416\n"
"v_mfma_f32_16x16x32_bf16 v[212:215], a[188:191], a[148:151], v[212:215]\n"
"ds_read_b64_tr_b16 a[208:209], v101 offset:4096\n"
"v_mfma_f32_16x16x32_bf16 v[216:219], a[184:187], a[104:107], v[216:219]\n"
"ds_read_b64_tr_b16 a[210:211], v101 offset:5120\n"
"v_mfma_f32_16x16x32_bf16 v[216:219], a[188:191], a[152:155], v[216:219]\n"
"ds_read_b64_tr_b16 a[212:213], v102 offset:4096\n"
"v_mfma_f32_16x16x32_bf16 v[220:223], a[184:187], a[108:111], v[220:223]\n"
"ds_read_b64_tr_b16 a[214:215], v102 offset:5120\n"
"v_mfma_f32_16x16x32_bf16 v[220:223], a[188:191], a[156:159], v[220:223]\n"
"s_waitcnt lgkmcnt(0)\n"
"s_cmp_eq_u32 s9, 3\n"
"s_cselect_b32 s19, 0x7ff00000, s97\n"
"s_cbranch_scc0 L_sf7_%=\n"
"ds_write_b128 v99, v[172:175] offset:2048\n"
"L_sf7_%=:\n"
"buffer_store_dwordx4 v[172:175], v46, s[64:67], s19 offen offset:2048 sc1\n"
"v_mfma_f32_16x16x32_bf16 v[192:195], a[160:163], a[112:115], v[192:195]\n"
"s_barrier\n"
"ds_read_b64_tr_b16 a[176:177], v1 offset:32768\n"
"v_mfma_f32_16x16x32_bf16 v[192:195], a[164:167], a[128:131], v[192:195]\n"
"ds_read_b64_tr_b16 a[178:179], v1 offset:40960\n"
"v_mfma_f32_16x16x32_bf16 v[192:195], a[208:211], a[192:195], v[192:195]\n"
"v_mfma_f32_16x16x32_bf16 v[196:199], a[160:163], a[116:119], v[196:199]\n"
"ds_read_b64_tr_b16 a[180:181], v1 offset:33792\n"
"v_mfma_f32_16x16x32_bf16 v[196:199], a[164:167], a[132:135], v[196:199]\n"
"ds_read_b64_tr_b16 a[182:183], v1 offset:41984\n"
"v_mfma_f32_16x16x32_bf16 v[196:199], a[208:211], a[196:199], v[196:199]\n"
"v_mfma_f32_16x16x32_bf16 v[200:203], a[160:163], a[120:123], v[200:203]\n"
"ds_read_b64_tr_b16 a[184:185], v2 offset:32768\n"
"v_mfma_f32_16x16x32_bf16 v[200:203], a[164:167], a[136:139], v[200:203]\n"
"ds_read_b64_tr_b16 a[186:187], v2 offset:40960\n"
"v_mfma_f32_16x16x32_bf16 v[200:203], a[208:211], a[200:203], v[200:203]\n"
"v_mfma_f32_16x16x32_bf16 v[204:207], a[160:163], a[124:127], v[204:207]\n"
"ds_read_b64_tr_b16 a[188:189], v2 offset:33792\n"
"v_mfma_f32_16x16x32_bf16 v[204:207], a[164:167], a[140:143], v[204:207]\n"
"ds_read_b64_tr_b16 a[190:191], v2 offset:41984\n"
"v_mfma_f32_16x16x32_bf16 v[204:207], a[208:211], a[204:207], v[204:207]\n"
"v_mfma_f32_16x16x32_bf16 v[208:211], a[168:171], a[112:115], v[208:211]\n"
"v_mfma_f32_16x16x32_bf16 v[208:211], a[172:175], a[128:131], v[208:211]\n"
"v_mfma_f32_16x16x32_bf16 v[208:211], a[212:215], a[192:195], v[208:211]\n"
"v_mfma_f32_16x16x32_bf16 v[212:215], a[168:171], a[116:119], v[212:215]\n"
"v_mfma_f32_16x16x32_bf16 v[212:215], a[172:175], a[132:135], v[212:215]\n"
"v_mfma_f32_16x16x32_bf16 v[212:215], a[212:215], a[196:199], v[212:215]\n"
"v_mfma_f32_16x16x32_bf16 v[216:219], a[168:171], a[120:123], v[216:219]\n"
"v_mfma_f32_16x16x32_bf16 v[216:219], a[172:175], a[136:139], v[216:219]\n"
"v_mfma_f32_16x16x32_bf16 v[216:219], a[212:215], a[200:203], v[216:219]\n"
"v_mfma_f32_16x16x32_bf16 v[220:223], a[168:171], a[124:127], v[220:223]\n"
"v_mfma_f32_16x16x32_bf16 v[220:223], a[172:175], a[140:143], v[220:223]\n"
"v_mfma_f32_16x16x32_bf16 v[220:223], a[212:215], a[204:207], v[220:223]\n"
"s_waitcnt lgkmcnt(0)\n"
"v_mfma_f32_16x16x32_bf16 v[224:227], a[176:179], a[0:3], v[224:227]\n"
"ds_read_b64_tr_b16 a[160:161], v1 offset:34816\n"
"v_mfma_f32_16x16x32_bf16 v[224:227], a[180:183], a[16:19], v[224:227]\n"
"ds_read_b64_tr_b16 a[162:163], v1 offset:43008\n"
"v_mfma_f32_16x16x32_bf16 v[228:231], a[176:179], a[4:7], v[228:231]\n"
"v_mfma_f32_16x16x32_bf16 v[228:231], a[180:183], a[20:23], v[228:231]\n"
"ds_read_b64_tr_b16 a[164:165], v1 offset:35840\n"
"v_cvt_pk_bf16_f32 v192, v192, v193\n"
"v_mfma_f32_16x16x32_bf16 v[232:235], a[176:179], a[8:11], v[232:235]\n"
"ds_read_b64_tr_b16 a[166:167], v1 offset:44032\n"
"v_cvt_pk_bf16_f32 v193, v194, v195\n"
"v_mfma_f32_16x16x32_bf16 v[232:235], a[180:183], a[24:27], v[232:235]\n"
"v_cvt_pk_bf16_f32 v194, v208, v209\n"
"v_cvt_pk_bf16_f32 v195, v210, v211\n"
"v_mfma_f32_16x16x32_bf16 v[236:239], a[176:179], a[12:15], v[236:239]\n"
"s_add_u32 s96, s12, 32768\n"
"ds_read_b64_tr_b16 a[168:169], v2 offset:34816\n"
"v_mfma_f32_16x16x32_bf16 v[236:239], a[180:183], a[28:31], v[236:239]\n"
"ds_read_b64_tr_b16 a[170:171], v2 offset:43008\n"
"v_cvt_pk_bf16_f32 v196, v196, v197\n"
"v_mfma_f32_16x16x32_bf16 v[240:243], a[184:187], a[0:3], v[240:243]\n"
"v_cvt_pk_bf16_f32 v197, v198, v199\n"
"v_cvt_pk_bf16_f32 v198, v212, v213\n"
"v_mfma_f32_16x16x32_bf16 v[240:243], a[188:191], a[16:19], v[240:243]\n"
"ds_read_b64_tr_b16 a[172:173], v2 offset:35840\n"
"v_cvt_pk_bf16_f32 v199, v214, v215\n"
"v_mfma_f32_16x16x32_bf16 v[244:247], a[184:187], a[4:7], v[244:247]\n"
"ds_read_b64_tr_b16 a[174:175], v2 offset:44032\n"
"v_cvt_pk_bf16_f32 v200, v200, v201\n"
"v_mfma_f32_16x16x32_bf16 v[244:247], a[188:191], a[20:23], v[244:247]\n"
"v_cvt_pk_bf16_f32 v201, v202, v203\n"
"v_cvt_pk_bf16_f32 v202, v216, v217\n"
"v_mfma_f32_16x16x32_bf16 v[248:251], a[184:187], a[8:11], v[248:251]\n"
"s_add_u32 s97, s12, 36864\n"
"v_cvt_pk_bf16_f32 v203, v218, v219\n"
"v_mfma_f32_16x16x32_bf16 v[248:251], a[188:191], a[24:27], v[248:251]\n"
"v_cvt_pk_bf16_f32 v204, v204, v205\n"
"v_cvt_pk_bf16_f32 v205, v206, v207\n"
"v_mfma_f32_16x16x32_bf16 v[252:255], a[184:187], a[12:15], v[252:255]\n"
"v_cvt_pk_bf16_f32 v206, v220, v221\n"
"v_cvt_pk_bf16_f32 v207, v222, v223\n"
"v_mfma_f32_16x16x32_bf16 v[252:255], a[188:191], a[28:31], v[252:255]\n"
"s_waitcnt lgkmcnt(0)\n"
"s_cmp_eq_u32 s9, 0\n"
"s_cselect_b32 s8, 0x7ff00000, s96\n"
"s_cbranch_scc0 L_sf8_%=\n"
"ds_write_b128 v99, v[192:195] offset:4096\n"
"L_sf8_%=:\n"
"buffer_store_dwordx4 v[192:195], v46, s[64:67], s8 offen offset:0 sc1\n"
"v_mfma_f32_16x16x32_bf16 v[224:227], a[160:163], a[32:35], v[224:227]\n"
"ds_read_b64_tr_b16 a[176:177], v1 offset:49152\n"
"v_mfma_f32_16x16x32_bf16 v[224:227], a[164:167], a[48:51], v[224:227]\n"
"ds_read_b64_tr_b16 a[178:179], v1 offset:57344\n"
"v_mfma_f32_16x16x32_bf16 v[228:231], a[160:163], a[36:39], v[228:231]\n"
"v_mfma_f32_16x16x32_bf16 v[228:231], a[164:167], a[52:55], v[228:231]\n"
"ds_read_b64_tr_b16 a[180:181], v1 offset:50176\n"
"v_mfma_f32_16x16x32_bf16 v[232:235], a[160:163], a[40:43], v[232:235]\n"
"ds_read_b64_tr_b16 a[182:183], v1 offset:58368\n"
"v_mfma_f32_16x16x32_bf16 v[232:235], a[164:167], a[56:59], v[232:235]\n"
"s_add_u32 s98, s88, 512\n"
"v_mfma_f32_16x16x32_bf16 v[236:239], a[160:163], a[44:47], v[236:239]\n"
"ds_read_b64_tr_b16 a[184:185], v2 offset:49152\n"
"v_mfma_f32_16x16x32_bf16 v[236:239], a[164:167], a[60:63], v[236:239]\n"
"ds_read_b64_tr_b16 a[186:187], v2 offset:57344\n"
"v_mfma_f32_16x16x32_bf16 v[240:243], a[168:171], a[32:35], v[240:243]\n"
"s_waitcnt vmcnt(1)\n"
"buffer_store_dwordx2 v[20:21], v24, s[64:67], s98 offen sc1\n"
"v_mfma_f32_16x16x32_bf16 v[240:243], a[172:175], a[48:51], v[240:243]\n"
"ds_read_b64_tr_b16 a[188:189], v2 offset:50176\n"
"v_mfma_f32_16x16x32_bf16 v[244:247], a[168:171], a[36:39], v[244:247]\n"
"ds_read_b64_tr_b16 a[190:191], v2 offset:58368\n"
"v_mfma_f32_16x16x32_bf16 v[244:247], a[172:175], a[52:55], v[244:247]\n"
"v_mfma_f32_16x16x32_bf16 v[248:251], a[168:171], a[40:43], v[248:251]\n"
"v_mfma_f32_16x16x32_bf16 v[248:251], a[172:175], a[56:59], v[248:251]\n"
"v_mfma_f32_16x16x32_bf16 v[252:255], a[168:171], a[44:47], v[252:255]\n"
"v_mfma_f32_16x16x32_bf16 v[252:255], a[172:175], a[60:63], v[252:255]\n"
"s_waitcnt lgkmcnt(0)\n"
"s_cmp_eq_u32 s9, 1\n"
"s_cselect_b32 s19, 0x7ff00000, s96\n"
"s_cbranch_scc0 L_sf9_%=\n"
"ds_write_b128 v99, v[196:199] offset:4096\n"
"L_sf9_%=:\n"
"buffer_store_dwordx4 v[196:199], v46, s[64:67], s19 offen offset:2048 sc1\n"
"v_mfma_f32_16x16x32_bf16 v[224:227], a[176:179], a[64:67], v[224:227]\n"
"ds_read_b64_tr_b16 a[160:161], v1 offset:51200\n"
"v_mfma_f32_16x16x32_bf16 v[224:227], a[180:183], a[80:83], v[224:227]\n"
"ds_read_b64_tr_b16 a[162:163], v1 offset:59392\n"
"v_mfma_f32_16x16x32_bf16 v[228:231], a[176:179], a[68:71], v[228:231]\n"
"v_mfma_f32_16x16x32_bf16 v[228:231], a[180:183], a[84:87], v[228:231]\n"
"ds_read_b64_tr_b16 a[164:165], v1 offset:52224\n"
"v_mfma_f32_16x16x32_bf16 v[232:235], a[176:179], a[72:75], v[232:235]\n"
"ds_read_b64_tr_b16 a[166:167], v1 offset:60416\n"
"v_mfma_f32_16x16x32_bf16 v[232:235], a[180:183], a[88:91], v[232:235]\n"
"s_add_u32 s16, s15, 0\n"
"v_mfma_f32_16x16x32_bf16 v[236:239], a[176:179], a[76:79], v[236:239]\n"
"ds_read_b64_tr_b16 a[168:169], v2 offset:51200\n"
"v_mfma_f32_16x16x32_bf16 v[236:239], a[180:183], a[92:95], v[236:239]\n"
"ds_read_b64_tr_b16 a[170:171], v2 offset:59392\n"
"v_mfma_f32_16x16x32_bf16 v[240:243], a[184:187], a[64:67], v[240:243]\n"
"buffer_load_dwordx2 v[42:43], v49, s[64:67], s16 offen sc1\n"
"v_mfma_f32_16x16x32_bf16 v[240:243], a[188:191], a[80:83], v[240:243]\n"
"ds_read_b64_tr_b16 a[172:173], v2 offset:52224\n"
"v_mfma_f32_16x16x32_bf16 v[244:247], a[184:187], a[68:71], v[244:247]\n"
"ds_read_b64_tr_b16 a[174:175], v2 offset:60416\n"
"v_mfma_f32_16x16x32_bf16 v[244:247], a[188:191], a[84:87], v[244:247]\n"
"v_mfma_f32_16x16x32_bf16 v[248:251], a[184:187], a[72:75], v[248:251]\n"
"v_mfma_f32_16x16x32_bf16 v[248:251], a[188:191], a[88:91], v[248:251]\n"
"v_mfma_f32_16x16x32_bf16 v[252:255], a[184:187], a[76:79], v[252:255]\n"
"v_mfma_f32_16x16x32_bf16 v[252:255], a[188:191], a[92:95], v[252:255]\n"
"s_waitcnt lgkmcnt(0)\n"
"s_cmp_eq_u32 s9, 2\n"
"s_cselect_b32 s8, 0x7ff00000, s97\n"
"s_cbranch_scc0 L_sf10_%=\n"
"ds_write_b128 v99, v[200:203] offset:4096\n"
"L_sf10_%=:\n"
"buffer_store_dwordx4 v[200:203], v46, s[64:67], s8 offen offset:0 sc1\n"
"v_mfma_f32_16x16x32_bf16 v[224:227], a[160:163], a[96:99], v[224:227]\n"
"s_barrier\n"
"ds_read_b64_tr_b16 a[176:177], v3 offset:0\n"
"v_mfma_f32_16x16x32_bf16 v[224:227], a[164:167], a[144:147], v[224:227]\n"
"ds_read_b64_tr_b16 a[178:179], v3 offset:8192\n"
"v_mfma_f32_16x16x32_bf16 v[228:231], a[160:163], a[100:103], v[228:231]\n"
"v_mfma_f32_16x16x32_bf16 v[228:231], a[164:167], a[148:151], v[228:231]\n"
"ds_read_b64_tr_b16 a[180:181], v3 offset:1024\n"
"v_mfma_f32_16x16x32_bf16 v[232:235], a[160:163], a[104:107], v[232:235]\n"
"ds_read_b64_tr_b16 a[182:183], v3 offset:9216\n"
"v_mfma_f32_16x16x32_bf16 v[232:235], a[164:167], a[152:155], v[232:235]\n"
"v_mfma_f32_16x16x32_bf16 v[236:239], a[160:163], a[108:111], v[236:239]\n"
"ds_read_b64_tr_b16 a[184:185], v4 offset:0\n"
"v_mfma_f32_16x16x32_bf16 v[236:239], a[164:167], a[156:159], v[236:239]\n"
"ds_read_b64_tr_b16 a[186:187], v4 offset:8192\n"
"v_mfma_f32_16x16x32_bf16 v[240:243], a[168:171], a[96:99], v[240:243]\n"
"v_mfma_f32_16x16x32_bf16 v[240:243], a[172:175], a[144:147], v[240:243]\n"
"ds_read_b64_tr_b16 a[188:189], v4 offset:1024\n"
"v_mfma_f32_16x16x32_bf16 v[244:247], a[168:171], a[100:103], v[244:247]\n"
"ds_read_b64_tr_b16 a[190:191], v4 offset:9216\n"
"v_mfma_f32_16x16x32_bf16 v[244:247], a[172:175], a[148:151], v[244:247]\n"
"ds_read_b64_tr_b16 a[208:209], v101 offset:6144\n"
"v_mfma_f32_16x16x32_bf16 v[248:251], a[168:171], a[104:107], v[248:251]\n"
"ds_read_b64_tr_b16 a[210:211], v101 offset:7168\n"
"v_mfma_f32_16x16x32_bf16 v[248:251], a[172:175], a[152:155], v[248:251]\n"
"ds_read_b64_tr_b16 a[212:213], v102 offset:6144\n"
"v_mfma_f32_16x16x32_bf16 v[252:255], a[168:171], a[108:111], v[252:255]\n"
"ds_read_b64_tr_b16 a[214:215], v102 offset:7168\n"
"v_mfma_f32_16x16x32_bf16 v[252:255], a[172:175], a[156:159], v[252:255]\n"
"s_waitcnt lgkmcnt(0)\n"
"s_cmp_eq_u32 s9, 3\n"
"s_cselect_b32 s19, 0x7ff00000, s97\n"
"s_cbranch_scc0 L_sf11_%=\n"
"ds_write_b128 v99, v[204:207] offset:4096\n"
"L_sf11_%=:\n"
"buffer_store_dwordx4 v[204:207], v46, s[64:67], s19 offen offset:2048 sc1\n"
"v_mfma_f32_16x16x32_bf16 v[224:227], a[176:179], a[112:115], v[224:227]\n"
"s_add_u32 s96, s13, 0\n"
"s_add_u32 s97, s13, 11010048\n"
"ds_read_b64_tr_b16 a[160:161], v97 offset:2048\n"
"v_mfma_f32_16x16x32_bf16 v[224:227], a[180:183], a[128:131], v[224:227]\n"
"ds_read_b64_tr_b16 a[162:163], v97 offset:6144\n"
"v_mfma_f32_16x16x32_bf16 v[224:227], a[208:211], a[192:195], v[224:227]\n"
"v_mfma_f32_16x16x32_bf16 v[228:231], a[176:179], a[116:119], v[228:231]\n"
"ds_read_b64_tr_b16 a[164:165], v97 offset:10240\n"
"v_mfma_f32_16x16x32_bf16 v[228:231], a[180:183], a[132:135], v[228:231]\n"
"ds_read_b64_tr_b16 a[166:167], v97 offset:14336\n"
"v_mfma_f32_16x16x32_bf16 v[228:231], a[208:211], a[196:199], v[228:231]\n"
"v_mfma_f32_16x16x32_bf16 v[232:235], a[176:179], a[120:123], v[232:235]\n"
"ds_read_b64_tr_b16 a[168:169], v97 offset:3072\n"
"v_mfma_f32_16x16x32_bf16 v[232:235], a[180:183], a[136:139], v[232:235]\n"
"ds_read_b64_tr_b16 a[170:171], v97 offset:7168\n"
"v_mfma_f32_16x16x32_bf16 v[232:235], a[208:211], a[200:203], v[232:235]\n"
"v_mfma_f32_16x16x32_bf16 v[236:239], a[176:179], a[124:127], v[236:239]\n"
"ds_read_b64_tr_b16 a[172:173], v97 offset:11264\n"
"v_mfma_f32_16x16x32_bf16 v[236:239], a[180:183], a[140:143], v[236:239]\n"
"ds_read_b64_tr_b16 a[174:175], v97 offset:15360\n"
"v_mfma_f32_16x16x32_bf16 v[236:239], a[208:211], a[204:207], v[236:239]\n"
"v_mfma_f32_16x16x32_bf16 v[240:243], a[184:187], a[112:115], v[240:243]\n"
"v_mfma_f32_16x16x32_bf16 v[240:243], a[188:191], a[128:131], v[240:243]\n"
"v_mfma_f32_16x16x32_bf16 v[240:243], a[212:215], a[192:195], v[240:243]\n"
"v_mfma_f32_16x16x32_bf16 v[244:247], a[184:187], a[116:119], v[244:247]\n"
"s_waitcnt vmcnt(2)\n"
"v_cmp_ne_u64 vcc, v[42:43], v[40:41]\n"
"s_nop 1\n"
"s_cbranch_vccz L_ok5_%=\n"
"s_mov_b32 s100, 0x4000\n"
"L_rt5_%=:\n"
"s_sleep 2\n"
"buffer_load_dwordx2 v[42:43], v49, s[64:67], s16 offen sc1\n"
"s_waitcnt vmcnt(0)\n"
"v_cmp_ne_u64 vcc, v[42:43], v[40:41]\n"
"s_nop 4\n"
"s_cbranch_vccz L_ok5_%=\n"
"s_sub_u32 s100, s100, 1\n"
"s_cmp_lg_u32 s100, 0\n"
"s_cbranch_scc1 L_rt5_%=\n"
"L_ok5_%=:\n"
"v_mfma_f32_16x16x32_bf16 v[244:247], a[188:191], a[132:135], v[244:247]\n"
"v_mfma_f32_16x16x32_bf16 v[244:247], a[212:215], a[196:199], v[244:247]\n"
"buffer_load_dwordx4 v[64:67], v48, s[64:67], s96 offen offset:0 sc1\n"
"v_mfma_f32_16x16x32_bf16 v[248:251], a[184:187], a[120:123], v[248:251]\n"
"v_mfma_f32_16x16x32_bf16 v[248:251], a[188:191], a[136:139], v[248:251]\n"
"buffer_load_dwordx4 v[68:71], v48, s[64:67], s96 offen offset:256 sc1\n"
"v_mfma_f32_16x16x32_bf16 v[248:251], a[212:215], a[200:203], v[248:251]\n"
"v_mfma_f32_16x16x32_bf16 v[252:255], a[184:187], a[124:127], v[252:255]\n"
"buffer_load_dwordx4 v[72:75], v98, s[64:67], s97 offen offset:0 sc1\n"
"v_mfma_f32_16x16x32_bf16 v[252:255], a[188:191], a[140:143], v[252:255]\n"
"v_mfma_f32_16x16x32_bf16 v[252:255], a[212:215], a[204:207], v[252:255]\n"
"s_waitcnt lgkmcnt(0)\n"
"buffer_load_dwordx4 v[76:79], v98, s[64:67], s97 offen offset:256 sc1\n"
"v_mfma_f32_16x16x32_bf16 v[112:115], a[160:163], a[0:3], v[112:115]\n"
"ds_read_b64_tr_b16 a[176:177], v96 offset:16384\n"
"v_mfma_f32_16x16x32_bf16 v[112:115], a[164:167], a[16:19], v[112:115]\n"
"ds_read_b64_tr_b16 a[178:179], v96 offset:20480\n"
"v_mfma_f32_16x16x32_bf16 v[112:115], a[168:171], a[32:35], v[112:115]\n"
"v_mfma_f32_16x16x32_bf16 v[112:115], a[172:175], a[48:51], v[112:115]\n"
"ds_read_b64_tr_b16 a[180:181], v96 offset:24576\n"
"v_cvt_pk_bf16_f32 v224, v224, v225\n"
"v_mfma_f32_16x16x32_bf16 v[116:119], a[160:163], a[4:7], v[116:119]\n"
"ds_read_b64_tr_b16 a[182:183], v96 offset:28672\n"
"v_cvt_pk_bf16_f32 v225, v226, v227\n"
"v_mfma_f32_16x16x32_bf16 v[116:119], a[164:167], a[20:23], v[116:119]\n"
"s_add_u32 s96, s12, 49152\n"
"v_cvt_pk_bf16_f32 v226, v240, v241\n"
"v_cvt_pk_bf16_f32 v227, v242, v243\n"
"v_mfma_f32_16x16x32_bf16 v[116:119], a[168:171], a[36:39], v[116:119]\n"
"ds_read_b64_tr_b16 a[184:185], v96 offset:17408\n"
"v_cvt_pk_bf16_f32 v228, v228, v229\n"
"v_mfma_f32_16x16x32_bf16 v[116:119], a[172:175], a[52:55], v[116:119]\n"
"ds_read_b64_tr_b16 a[186:187], v96 offset:21504\n"
"v_cvt_pk_bf16_f32 v229, v230, v231\n"
"v_mfma_f32_16x16x32_bf16 v[120:123], a[160:163], a[8:11], v[120:123]\n"
"s_cmp_eq_u32 s9, 0\n"
"s_cselect_b32 s8, 0x7ff00000, s96\n"
"s_cbranch_scc0 L_sf12_%=\n"
"ds_write_b128 v99, v[224:227] offset:6144\n"
"L_sf12_%=:\n"
"v_mfma_f32_16x16x32_bf16 v[120:123], a[164:167], a[24:27], v[120:123]\n"
"ds_read_b64_tr_b16 a[188:189], v96 offset:25600\n"
"v_cvt_pk_bf16_f32 v230, v244, v245\n"
"v_mfma_f32_16x16x32_bf16 v[120:123], a[168:171], a[40:43], v[120:123]\n"
"ds_read_b64_tr_b16 a[190:191], v96 offset:29696\n"
"v_cvt_pk_bf16_f32 v231, v246, v247\n"
"v_mfma_f32_16x16x32_bf16 v[120:123], a[172:175], a[56:59], v[120:123]\n"
"buffer_store_dwordx4 v[224:227], v46, s[64:67], s8 offen offset:0 sc1\n"
"v_mfma_f32_16x16x32_bf16 v[124:127], a[160:163], a[12:15], v[124:127]\n"
"v_cvt_pk_bf16_f32 v232, v232, v233\n"
"v_cvt_pk_bf16_f32 v233, v234, v235\n"
"v_mfma_f32_16x16x32_bf16 v[124:127], a[164:167], a[28:31], v[124:127]\n"
"s_cmp_eq_u32 s9, 1\n"
"s_cselect_b32 s19, 0x7ff00000, s96\n"
"s_cbranch_scc0 L_sf13_%=\n"
"ds_write_b128 v99, v[228:231] offset:6144\n"
"L_sf13_%=:\n"
"v_mfma_f32_16x16x32_bf16 v[124:127], a[168:171], a[44:47], v[124:127]\n"
"v_cvt_pk_bf16_f32 v234, v248, v249\n"
"v_cvt_pk_bf16_f32 v235, v250, v251\n"
"v_mfma_f32_16x16x32_bf16 v[124:127], a[172:175], a[60:63], v[124:127]\n"
"s_waitcnt lgkmcnt(0)\n"
"buffer_store_dwordx4 v[228:231], v46, s[64:67], s19 offen offset:2048 sc1\n"
"v_mfma_f32_16x16x32_bf16 v[112:115], a[176:179], a[64:67], v[112:115]\n"
"s_add_u32 s97, s12, 53248\n"
"ds_read_b64_tr_b16 a[160:161], v96 offset:18432\n"
"v_mfma_f32_16x16x32_bf16 v[112:115], a[180:183], a[80:83], v[112:115]\n"
"ds_read_b64_tr_b16 a[162:163], v96 offset:22528\n"
"v_cvt_pk_bf16_f32 v236, v236, v237\n"
"v_mfma_f32_16x16x32_bf16 v[112:115], a[184:187], a[96:99], v[112:115]\n"
"v_cvt_pk_bf16_f32 v237, v238, v239\n"
"v_cvt_pk_bf16_f32 v238, v252, v253\n"
"v_mfma_f32_16x16x32_bf16 v[112:115], a[188:191], a[144:147], v[112:115]\n"
"ds_read_b64_tr_b16 a[164:165], v96 offset:26624\n"
"v_cvt_pk_bf16_f32 v239, v254, v255\n"
"v_mfma_f32_16x16x32_bf16 v[116:119], a[176:179], a[68:71], v[116:119]\n"
"ds_read_b64_tr_b16 a[166:167], v96 offset:30720\n"
"v_mfma_f32_16x16x32_bf16 v[116:119], a[180:183], a[84:87], v[116:119]\n"
"s_cmp_eq_u32 s9, 2\n"
"s_cselect_b32 s8, 0x7ff00000, s97\n"
"s_cbranch_scc0 L_sf14_%=\n"
"ds_write_b128 v99, v[232:235] offset:6144\n"
"L_sf14_%=:\n"
"v_mfma_f32_16x16x32_bf16 v[116:119], a[184:187], a[100:103], v[116:119]\n"
"s_add_u32 s98, s88, 1024\n"
"ds_read_b64_tr_b16 a[208:209], v103 offset:8192\n"
"v_mfma_f32_16x16x32_bf16 v[116:119], a[188:191], a[148:151], v[116:119]\n"
"ds_read_b64_tr_b16 a[210:211], v103 offset:9216\n"
"v_mfma_f32_16x16x32_bf16 v[120:123], a[176:179], a[72:75], v[120:123]\n"
"s_waitcnt vmcnt(6)\n"
"buffer_store_dwordx2 v[20:21], v24, s[64:67], s98 offen sc1\n"
"v_mfma_f32_16x16x32_bf16 v[120:123], a[180:183], a[88:91], v[120:123]\n"
"v_mfma_f32_16x16x32_bf16 v[120:123], a[184:187], a[104:107], v[120:123]\n"
"v_mfma_f32_16x16x32_bf16 v[120:123], a[188:191], a[152:155], v[120:123]\n"
"s_cmp_eq_u32 s9, 3\n"
"s_cselect_b32 s19, 0x7ff00000, s97\n"
"s_cbranch_scc0 L_sf15_%=\n"
"ds_write_b128 v99, v[236:239] offset:6144\n"
"L_sf15_%=:\n"
"buffer_store_dwordx4 v[232:235], v46, s[64:67], s8 offen offset:0 sc1\n"
"v_mfma_f32_16x16x32_bf16 v[124:127], a[176:179], a[76:79], v[124:127]\n"
"v_mfma_f32_16x16x32_bf16 v[124:127], a[180:183], a[92:95], v[124:127]\n"
"v_mfma_f32_16x16x32_bf16 v[124:127], a[184:187], a[108:111], v[124:127]\n"
"v_mfma_f32_16x16x32_bf16 v[124:127], a[188:191], a[156:159], v[124:127]\n"
"s_waitcnt lgkmcnt(0)\n"
"buffer_store_dwordx4 v[236:239], v46, s[64:67], s19 offen offset:2048 sc1\n"
"v_mfma_f32_16x16x32_bf16 v[112:115], a[160:163], a[112:115], v[112:115]\n"
"v_mfma_f32_16x16x32_bf16 v[112:115], a[164:167], a[128:131], v[112:115]\n"
"v_mfma_f32_16x16x32_bf16 v[112:115], a[208:211], a[192:195], v[112:115]\n"
"v_mfma_f32_16x16x32_bf16 v[116:119], a[160:163], a[116:119], v[116:119]\n"
"v_mfma_f32_16x16x32_bf16 v[116:119], a[164:167], a[132:135], v[116:119]\n"
"v_mfma_f32_16x16x32_bf16 v[116:119], a[208:211], a[196:199], v[116:119]\n"
"v_mfma_f32_16x16x32_bf16 v[120:123], a[160:163], a[120:123], v[120:123]\n"
"v_mfma_f32_16x16x32_bf16 v[120:123], a[164:167], a[136:139], v[120:123]\n"
"v_mfma_f32_16x16x32_bf16 v[120:123], a[208:211], a[200:203], v[120:123]\n"
"v_mfma_f32_16x16x32_bf16 v[124:127], a[160:163], a[124:127], v[124:127]\n"
"v_mfma_f32_16x16x32_bf16 v[124:127], a[164:167], a[140:143], v[124:127]\n"
"v_mfma_f32_16x16x32_bf16 v[124:127], a[208:211], a[204:207], v[124:127]\n"
"v_cvt_pk_bf16_f32 v112, v112, v113\n"
"v_cvt_pk_bf16_f32 v113, v114, v115\n"
"s_add_u32 s96, s12, 65536\n"
"s_cmp_eq_u32 s9, 0\n"
"s_cselect_b32 s8, 0x7ff00000, s96\n"
"s_cbranch_scc0 L_sf16_%=\n"
"ds_write_b64 v109, v[112:113]\n"
"L_sf16_%=:\n"
"buffer_store_dwordx2 v[112:113], v47, s[64:67], s8 offen offset:0 sc1\n"
"v_cvt_pk_bf16_f32 v116, v116, v117\n"
"v_cvt_pk_bf16_f32 v117, v118, v119\n"
"s_cmp_eq_u32 s9, 1\n"
"s_cselect_b32 s19, 0x7ff00000, s96\n"
"s_cbranch_scc0 L_sf17_%=\n"
"ds_write_b64 v109, v[116:117]\n"
"L_sf17_%=:\n"
"buffer_store_dwordx2 v[116:117], v47, s[64:67], s19 offen offset:1024 sc1\n"
"v_cvt_pk_bf16_f32 v120, v120, v121\n"
"v_cvt_pk_bf16_f32 v121, v122, v123\n"
"s_cmp_eq_u32 s9, 2\n"
"s_cselect_b32 s8, 0x7ff00000, s96\n"
"s_cbranch_scc0 L_sf18_%=\n"
"ds_write_b64 v109, v[120:121]\n"
"L_sf18_%=:\n"
"buffer_store_dwordx2 v[120:121], v47, s[64:67], s8 offen offset:2048 sc1\n"
"v_cvt_pk_bf16_f32 v124, v124, v125\n"
"v_cvt_pk_bf16_f32 v125, v126, v127\n"
"s_cmp_eq_u32 s9, 3\n"
"s_cselect_b32 s19, 0x7ff00000, s96\n"
"s_cbranch_scc0 L_sf19_%=\n"
"ds_write_b64 v109, v[124:125]\n"
"L_sf19_%=:\n"
"buffer_store_dwordx2 v[124:125], v47, s[64:67], s19 offen offset:3072 sc1\n"
"s_add_u32 s17, s15, 512\n"
"buffer_load_dwordx2 v[44:45], v49, s[64:67], s17 offen sc1\n"
"s_add_u32 s16, s15, 1024\n"
"buffer_load_dwordx2 v[42:43], v49, s[64:67], s16 offen sc1\n"
"s_waitcnt lgkmcnt(0)\n"
"s_barrier\n"
"ds_read_b128 v[144:147], v100 offset:0\n"
"ds_read_b128 v[148:151], v100 offset:256\n"
"ds_read_b128 v[152:155], v100 offset:2048\n"
"ds_read_b128 v[156:159], v100 offset:2304\n"
"s_add_u32 s98, s88, 2048\n"
"s_waitcnt vmcnt(2)\n"
"buffer_store_dwordx2 v[20:21], v24, s[64:67], s98 offen sc1\n"
"s_waitcnt vmcnt(2)\n"
"v_cmp_ne_u64 vcc, v[44:45], v[40:41]\n"
"s_nop 1\n"
"s_cbranch_vccz L_ok6_%=\n"
"s_mov_b32 s100, 0x4000\n"
"L_rt6_%=:\n"
"s_sleep 2\n"
"buffer_load_dwordx2 v[44:45], v49, s[64:67], s17 offen sc1\n"
"s_waitcnt vmcnt(0)\n"
"v_cmp_ne_u64 vcc, v[44:45], v[40:41]\n"
"s_nop 4\n"
"s_cbranch_vccz L_ok6_%=\n"
"s_sub_u32 s100, s100, 1\n"
"s_cmp_lg_u32 s100, 0\n"
"s_cbranch_scc1 L_rt6_%=\n"
"L_ok6_%=:\n"
"s_add_u32 s96, s13, 16384\n"
"buffer_load_dwordx4 v[80:83], v48, s[64:67], s96 offen offset:0 sc1\n"
"buffer_load_dwordx4 v[84:87], v48, s[64:67], s96 offen offset:256 sc1\n"
"s_add_u32 s97, s13, 11026432\n"
"buffer_load_dwordx4 v[88:91], v98, s[64:67], s97 offen offset:0 sc1\n"
"buffer_load_dwordx4 v[92:95], v98, s[64:67], s97 offen offset:256 sc1\n"
"s_waitcnt vmcnt(5)\n"
"v_cmp_ne_u64 vcc, v[42:43], v[40:41]\n"
"s_nop 1\n"
"s_cbranch_vccz L_ok7_%=\n"
"s_mov_b32 s100, 0x4000\n"
"L_rt7_%=:\n"
"s_sleep 2\n"
"buffer_load_dwordx2 v[42:43], v49, s[64:67], s16 offen sc1\n"
"s_waitcnt vmcnt(0)\n"
"v_cmp_ne_u64 vcc, v[42:43], v[40:41]\n"
"s_nop 4\n"
"s_cbranch_vccz L_ok7_%=\n"
"s_sub_u32 s100, s100, 1\n"
"s_cmp_lg_u32 s100, 0\n"
"s_cbranch_scc1 L_rt7_%=\n"
"L_ok7_%=:\n"
"s_add_u32 s96, s13, 32768\n"
"buffer_load_dwordx4 v[128:131], v48, s[64:67], s96 offen offset:0 sc1\n"
"buffer_load_dwordx4 v[132:135], v48, s[64:67], s96 offen offset:256 sc1\n"
"s_add_u32 s97, s13, 11042816\n"
"buffer_load_dwordx4 v[136:139], v98, s[64:67], s97 offen offset:0 sc1\n"
"buffer_load_dwordx4 v[140:143], v98, s[64:67], s97 offen offset:256 sc1\n"
"s_waitcnt lgkmcnt(3)\n"
"v_mfma_f32_16x16x32_bf16 v[16:19], a[216:219], v[144:147], 0\n"
"s_waitcnt lgkmcnt(2)\n"
"v_mfma_f32_16x16x32_bf16 v[16:19], a[220:223], v[148:151], v[16:19]\n"
"v_mfma_f32_16x16x32_bf16 v[16:19], v[32:35], v[64:67], v[16:19]\n"
"v_mfma_f32_16x16x32_bf16 v[16:19], v[36:39], v[68:71], v[16:19]\n"
"v_mfma_f32_16x16x32_bf16 v[16:19], v[32:35], v[72:75], v[16:19]\n"
"v_mfma_f32_16x16x32_bf16 v[16:19], v[36:39], v[76:79], v[16:19]\n"
"s_nop 7\n"
"ds_write_b128 v6, v[16:19] offset:0\n"
"ds_read_b128 v[144:147], v100 offset:4096\n"
"ds_read_b128 v[148:151], v100 offset:4352\n"
"s_add_u32 s16, s15, 2048\n"
"buffer_load_dwordx2 v[42:43], v49, s[64:67], s16 offen sc1\n"
"s_waitcnt lgkmcnt(4)\n"
"v_mfma_f32_16x16x32_bf16 v[228:231], a[216:219], v[152:155], 0\n"
"s_waitcnt lgkmcnt(3)\n"
"v_mfma_f32_16x16x32_bf16 v[228:231], a[220:223], v[156:159], v[228:231]\n"
"s_waitcnt vmcnt(8)\n"
"v_mfma_f32_16x16x32_bf16 v[228:231], v[32:35], v[80:83], v[228:231]\n"
"s_waitcnt vmcnt(7)\n"
"v_mfma_f32_16x16x32_bf16 v[228:231], v[36:39], v[84:87], v[228:231]\n"
"s_waitcnt vmcnt(6)\n"
"v_mfma_f32_16x16x32_bf16 v[228:231], v[32:35], v[88:91], v[228:231]\n"
"s_waitcnt vmcnt(5)\n"
"v_mfma_f32_16x16x32_bf16 v[228:231], v[36:39], v[92:95], v[228:231]\n"
"s_nop 7\n"
"ds_write_b128 v6, v[228:231] offset:4096\n"
"ds_read_b128 v[152:155], v100 offset:6144\n"
"ds_read_b128 v[156:159], v100 offset:6400\n"
"s_waitcnt lgkmcnt(2)\n"
"s_barrier\n"
"ds_read_b128 v[208:211], v5 offset:0\n"
"ds_read_b128 v[212:215], v5 offset:4096\n"
"v_lshlrev_b32 v56, 16, v14\n"
"v_and_b32 v57, 0xffff0000, v14\n"
"v_lshlrev_b32 v58, 16, v15\n"
"v_and_b32 v59, 0xffff0000, v15\n"
"s_waitcnt lgkmcnt(1)\n"
"v_fmamk_f32 v56, v208, 0x3ef5fdf0, v56\n"
"v_fmamk_f32 v57, v209, 0x3ef5fdf0, v57\n"
"v_fmamk_f32 v58, v210, 0x3ef5fdf0, v58\n"
"v_fmamk_f32 v59, v211, 0x3ef5fdf0, v59\n"
"v_cvt_pk_bf16_f32 v60, v56, v57\n"
"v_cvt_pk_bf16_f32 v61, v58, v59\n"
"buffer_store_dwordx2 v[60:61], v50, s[68:71], 0 offen offset:0 sc1\n"
"v_lshlrev_b32 v56, 16, v28\n"
"v_and_b32 v57, 0xffff0000, v28\n"
"v_lshlrev_b32 v58, 16, v29\n"
"v_and_b32 v59, 0xffff0000, v29\n"
"s_waitcnt lgkmcnt(0)\n"
"v_fmamk_f32 v56, v212, 0x3ef5fdf0, v56\n"
"v_fmamk_f32 v57, v213, 0x3ef5fdf0, v57\n"
"v_fmamk_f32 v58, v214, 0x3ef5fdf0, v58\n"
"v_fmamk_f32 v59, v215, 0x3ef5fdf0, v59\n"
"v_cvt_pk_bf16_f32 v60, v56, v57\n"
"v_cvt_pk_bf16_f32 v61, v58, v59\n"
"buffer_store_dwordx2 v[60:61], v50, s[68:71], 0 offen offset:128 sc1\n"
"s_waitcnt vmcnt(2)\n"
"v_cmp_ne_u64 vcc, v[42:43], v[40:41]\n"
"s_nop 1\n"
"s_cbranch_vccz L_ok8_%=\n"
"s_mov_b32 s100, 0x4000\n"
"L_rt8_%=:\n"
"s_sleep 2\n"
"buffer_load_dwordx2 v[42:43], v49, s[64:67], s16 offen sc1\n"
"s_waitcnt vmcnt(0)\n"
"v_cmp_ne_u64 vcc, v[42:43], v[40:41]\n"
"s_nop 4\n"
"s_cbranch_vccz L_ok8_%=\n"
"s_sub_u32 s100, s100, 1\n"
"s_cmp_lg_u32 s100, 0\n"
"s_cbranch_scc1 L_rt8_%=\n"
"L_ok8_%=:\n"
"s_add_u32 s96, s13, 49152\n"
"buffer_load_dwordx4 v[160:163], v48, s[64:67], s96 offen offset:0 sc1\n"
"buffer_load_dwordx4 v[164:167], v48, s[64:67], s96 offen offset:256 sc1\n"
"s_add_u32 s97, s13, 11059200\n"
"buffer_load_dwordx4 v[168:171], v98, s[64:67], s97 offen offset:0 sc1\n"
"buffer_load_dwordx4 v[172:175], v98, s[64:67], s97 offen offset:256 sc1\n"
"s_add_u32 s96, s18, 0\n"
"buffer_load_dwordx4 v[192:195], v48, s[64:67], s96 offen offset:0 sc1\n"
"s_add_u32 s97, s18, 11010048\n"
"buffer_load_dwordx4 v[200:203], v98, s[64:67], s97 offen offset:0 sc1\n"
"v_mfma_f32_16x16x32_bf16 v[16:19], a[216:219], v[144:147], 0\n"
"v_mfma_f32_16x16x32_bf16 v[16:19], a[220:223], v[148:151], v[16:19]\n"
"v_mfma_f32_16x16x32_bf16 v[16:19], v[32:35], v[128:131], v[16:19]\n"
"v_mfma_f32_16x16x32_bf16 v[16:19], v[36:39], v[132:135], v[16:19]\n"
"v_mfma_f32_16x16x32_bf16 v[16:19], v[32:35], v[136:139], v[16:19]\n"
"v_mfma_f32_16x16x32_bf16 v[16:19], v[36:39], v[140:143], v[16:19]\n"
"s_nop 7\n"
"ds_write_b128 v6, v[16:19] offset:8192\n"
"ds_read_b128 v[144:147], v106 offset:0\n"
"s_waitcnt lgkmcnt(1)\n"
"s_barrier\n"
"ds_read_b128 v[216:219], v5 offset:8192\n"
"v_lshlrev_b32 v56, 16, v30\n"
"v_and_b32 v57, 0xffff0000, v30\n"
"v_lshlrev_b32 v58, 16, v31\n"
"v_and_b32 v59, 0xffff0000, v31\n"
"s_waitcnt lgkmcnt(0)\n"
"v_fmamk_f32 v56, v216, 0x3ef5fdf0, v56\n"
"v_fmamk_f32 v57, v217, 0x3ef5fdf0, v57\n"
"v_fmamk_f32 v58, v218, 0x3ef5fdf0, v58\n"
"v_fmamk_f32 v59, v219, 0x3ef5fdf0, v59\n"
"v_cvt_pk_bf16_f32 v60, v56, v57\n"
"v_cvt_pk_bf16_f32 v61, v58, v59\n"
"buffer_store_dwordx2 v[60:61], v50, s[68:71], 0 offen offset:256 sc1\n"
"v_mfma_f32_16x16x32_bf16 v[228:231], a[216:219], v[152:155], 0\n"
"v_mfma_f32_16x16x32_bf16 v[228:231], a[220:223], v[156:159], v[228:231]\n"
"s_waitcnt vmcnt(6)\n"
"v_mfma_f32_16x16x32_bf16 v[228:231], v[32:35], v[160:163], v[228:231]\n"
"s_waitcnt vmcnt(5)\n"
"v_mfma_f32_16x16x32_bf16 v[228:231], v[36:39], v[164:167], v[228:231]\n"
"s_waitcnt vmcnt(4)\n"
"v_mfma_f32_16x16x32_bf16 v[228:231], v[32:35], v[168:171], v[228:231]\n"
"s_waitcnt vmcnt(3)\n"
"v_mfma_f32_16x16x32_bf16 v[228:231], v[36:39], v[172:175], v[228:231]\n"
"s_nop 7\n"
"ds_write_b128 v6, v[228:231] offset:12288\n"
"v_mfma_f32_16x16x32_bf16 v[16:19], a[216:219], v[144:147], 0\n"
"s_waitcnt vmcnt(2)\n"
"v_mfma_f32_16x16x32_bf16 v[16:19], v[32:35], v[192:195], v[16:19]\n"
"s_waitcnt vmcnt(1)\n"
"v_mfma_f32_16x16x32_bf16 v[16:19], v[32:35], v[200:203], v[16:19]\n"
"s_nop 7\n"
"ds_write_b128 v7, v[16:19] offset:16384\n"
"s_waitcnt lgkmcnt(0)\n"
"s_barrier\n"
"ds_read_b128 v[220:223], v5 offset:12288\n"
"ds_read_b128 v[224:227], v5 offset:16384\n"
"v_lshlrev_b32 v56, 16, v62\n"
"v_and_b32 v57, 0xffff0000, v62\n"
"v_lshlrev_b32 v58, 16, v63\n"
"v_and_b32 v59, 0xffff0000, v63\n"
"s_waitcnt lgkmcnt(1)\n"
"v_fmamk_f32 v56, v220, 0x3ef5fdf0, v56\n"
"v_fmamk_f32 v57, v221, 0x3ef5fdf0, v57\n"
"v_fmamk_f32 v58, v222, 0x3ef5fdf0, v58\n"
"v_fmamk_f32 v59, v223, 0x3ef5fdf0, v59\n"
"v_cvt_pk_bf16_f32 v60, v56, v57\n"
"v_cvt_pk_bf16_f32 v61, v58, v59\n"
"buffer_store_dwordx2 v[60:61], v50, s[68:71], 0 offen offset:384 sc1\n"
"v_lshlrev_b32 v56, 16, v110\n"
"v_and_b32 v57, 0xffff0000, v110\n"
"v_lshlrev_b32 v58, 16, v111\n"
"v_and_b32 v59, 0xffff0000, v111\n"
"s_waitcnt lgkmcnt(0)\n"
"v_fmamk_f32 v56, v224, 0x3ef5fdf0, v56\n"
"v_fmamk_f32 v57, v225, 0x3ef5fdf0, v57\n"
"v_fmamk_f32 v58, v226, 0x3ef5fdf0, v58\n"
"v_fmamk_f32 v59, v227, 0x3ef5fdf0, v59\n"
"v_cvt_pk_bf16_f32 v60, v56, v57\n"
"v_cvt_pk_bf16_f32 v61, v58, v59\n"
"buffer_store_dwordx2 v[60:61], v51, s[68:71], 0 offen offset:0 sc1\n"
"s_lshl_b32 s96, s76, 7\n"
"s_lshl_b32 s97, s75, 3\n"
"s_add_u32 s96, s96, s97\n"
"s_cmp_eq_u32 s72, 0\n"
"s_cselect_b32 s96, s96, 0x7ff00000\n"
"buffer_store_dwordx2 v[20:21], v24, s[64:67], s96 offen sc1\n"
"s_waitcnt vmcnt(0) lgkmcnt(0)\n"
"L_end%=:\n"
  : "+v"(tid) : "s"(__builtin_amdgcn_kernarg_segment_ptr()), "s"(blockIdx.x), "s"((unsigned)(size_t)lds)
  : "v1", "v2", "v3", "v4", "v5", "v6", "v7", "v8", "v9", "v10", "v11", "v12", "v13", "v14", "v15", "v16", "v17", "v18", "v19", "v20", "v21", "v22", "v23", "v24", "v25", "v26", "v27", "v28", "v29", "v30", "v31", "v32", "v33", "v34", "v35", "v36", "v37", "v38", "v39", "v40", "v41", "v42", "v43", "v44", "v45", "v46", "v47", "v48", "v49", "v50", "v51", "v52", "v53", "v54", "v55", "v56", "v57", "v58", "v59", "v60", "v61", "v62", "v63", "v64", "v65", "v66", "v67", "v68", "v69", "v70", "v71", "v72", "v73", "v74", "v75", "v76", "v77", "v78", "v79", "v80", "v81", "v82", "v83", "v84", "v85", "v86", "v87", "v88", "v89", "v90", "v91", "v92", "v93", "v94", "v95", "v96", "v97", "v98", "v99", "v100", "v101", "v102", "v103", "v104", "v105", "v106", "v107", "v108", "v109", "v110", "v111", "v112", "v113", "v114", "v115", "v116", "v117", "v118", "v119", "v120", "v121", "v122", "v123", "v124", "v125", "v126", "v127", "v128", "v129", "v130", "v131", "v132", "v133", "v134", "v135", "v136", "v137", "v138", "v139", "v140", "v141", "v142", "v143", "v144", "v145", "v146", "v147", "v148", "v149", "v150", "v151", "v152", "v153", "v154", "v155", "v156", "v157", "v158", "v159", "v160", "v161", "v162", "v163", "v164", "v165", "v166", "v167", "v168", "v169", "v170", "v171", "v172", "v173", "v174", "v175", "v176", "v177", "v178", "v179", "v180", "v181", "v182", "v183", "v184", "v185", "v186", "v187", "v188", "v189", "v190", "v191", "v192", "v193", "v194", "v195", "v196", "v197", "v198", "v199", "v200", "v201", "v202", "v203", "v204", "v205", "v206", "v207", "v208", "v209", "v210", "v211", "v212", "v213", "v214", "v215", "v216", "v217", "v218", "v219", "v220", "v221", "v222", "v223", "v224", "v225", "v226", "v227", "v228", "v229", "v230", "v231", "v232", "v233", "v234", "v235", "v236", "v237", "v238", "v239", "v240", "v241", "v242", "v243", "v244", "v245", "v246", "v247", "v248", "v249", "v250", "v251", "v252", "v253", "v254", "v255", "a0", "a1", "a2", "a3", "a4", "a5", "a6", "a7", "a8", "a9", "a10", "a11", "a12", "a13", "a14", "a15", "a16", "a17", "a18", "a19", "a20", "a21", "a22", "a23", "a24", "a25", "a26", "a27", "a28", "a29", "a30", "a31", "a32", "a33", "a34", "a35", "a36", "a37", "a38", "a39", "a40", "a41", "a42", "a43", "a44", "a45", "a46", "a47", "a48", "a49", "a50", "a51", "a52", "a53", "a54", "a55", "a56", "a57", "a58", "a59", "a60", "a61", "a62", "a63", "a64", "a65", "a66", "a67", "a68", "a69", "a70", "a71", "a72", "a73", "a74", "a75", "a76", "a77", "a78", "a79", "a80", "a81", "a82", "a83", "a84", "a85", "a86", "a87", "a88", "a89", "a90", "a91", "a92", "a93", "a94", "a95", "a96", "a97", "a98", "a99", "a100", "a101", "a102", "a103", "a104", "a105", "a106", "a107", "a108", "a109", "a110", "a111", "a112", "a113", "a114", "a115", "a116", "a117", "a118", "a119", "a120", "a121", "a122", "a123", "a124", "a125", "a126", "a127", "a128", "a129", "a130", "a131", "a132", "a133", "a134", "a135", "a136", "a137", "a138", "a139", "a140", "a141", "a142", "a143", "a144", "a145", "a146", "a147", "a148", "a149", "a150", "a151", "a152", "a153", "a154", "a155", "a156", "a157", "a158", "a159", "a160", "a161", "a162", "a163", "a164", "a165", "a166", "a167", "a168", "a169", "a170", "a171", "a172", "a173", "a174", "a175", "a176", "a177", "a178", "a179", "a180", "a181", "a182", "a183", "a184", "a185", "a186", "a187", "a188", "a189", "a190", "a191", "a192", "a193", "a194", "a195", "a196", "a197", "a198", "a199", "a200", "a201", "a202", "a203", "a204", "a205", "a206", "a207", "a208", "a209", "a210", "a211", "a212", "a213", "a214", "a215", "a216", "a217", "a218", "a219", "a220", "a221", "a222", "a223", "a224", "a225", "a226", "a227", "a228", "a229", "a230", "a231", "a232", "a233", "a234", "a235", "a236", "a237", "a238", "a239", "a240", "a241", "a242", "a243", "a244", "a245", "a246", "a247", "a248", "a249", "a250", "a251", "a252", "a253", "a254", "a255", "s4", "s5", "s6", "s7", "s8", "s9", "s10", "s11", "s12", "s13", "s14", "s15", "s16", "s17", "s18", "s19", "s20", "s21", "s22", "s23", "s24", "s25", "s26", "s27", "s28", "s29", "s30", "s31", "s34", "s35", "s36", "s37", "s38", "s39", "s40", "s41", "s42", "s43", "s44", "s45", "s46", "s47", "s48", "s49", "s50", "s51", "s52", "s53", "s54", "s55", "s56", "s57", "s58", "s59", "s60", "s61", "s62", "s63", "s64", "s65", "s66", "s67", "s68", "s69", "s70", "s71", "s72", "s73", "s74", "s75", "s76", "s77", "s78", "s79", "s80", "s81", "s82", "s83", "s84", "s85", "s86", "s87", "s88", "s89", "s90", "s91", "s92", "s93", "s94", "s95", "s96", "s97", "s98", "s99", "s100", "s101", "vcc", "scc", "memory");
}
extern "C" void kernel_launch(void* const* d_in, const int* in_sizes, int n_in, void* d_out, int out_size, void* d_ws, size_t ws_size, hipStream_t stream) {
  fused_kernel<<<256, 256, 0, stream>>>(d_in[0], d_in[1], d_in[2], d_in[4], d_in[5], d_in[6], d_out, d_ws);
}
```
